```python
import torch

def _softmax(x, axis=-1):
    return torch.softmax(x, dim=axis)

def _gelu(x):
    return torch.nn.functional.gelu(x, approximate='tanh')

def _silu(x):
    return torch.nn.functional.silu(x)

def _sigmoid(x):
    return torch.sigmoid(x)

def _layernorm(x, axis=-1, eps=1e-05):
    m = x.mean(dim=axis, keepdim=True)
    v = x.var(dim=axis, keepdim=True, unbiased=False)
    return (x - m) / torch.sqrt(v + eps)

def _scatter_add(tgt, idx, src, axis=0):
    return tgt.clone().index_add_(axis, idx, src)

def _segment_sum(src, seg, n):
    out = torch.zeros((n,) + src.shape[1:], dtype=src.dtype)
    return out.index_add_(0, seg, src)

def _pad(x, pw):
    flat = tuple((v for pair in reversed(pw) for v in pair))
    return torch.nn.functional.pad(x, flat)
kernel_name = 'adaLN_cond__ssm_conv_s4d__mixer_gmlp__prompt'

def setup_inputs():

    _w = lambda a: (lambda t: t.to(torch.bfloat16) if t.is_floating_point() else t)(torch.as_tensor(a))

    def _A_setup():
        Bsz, L, D, N = (1, 2048, 512, 128)
        return {'u': _w((lambda *s: torch.randn(*s, dtype=torch.bfloat16) * 0.05)(Bsz, L, D)), 'log_A_real': _w((lambda *s: torch.randn(*s, dtype=torch.bfloat16) * 3.2)(D, N) * 0.5), 'A_imag': _w((lambda *s: torch.randn(*s, dtype=torch.bfloat16) * 0.2)(D, N) * 3.14), 'C_real': _w((lambda *s: torch.randn(*s, dtype=torch.bfloat16) * 0.8)(D, N) / N ** 0.5), 'C_imag': _w((lambda *s: torch.randn(*s, dtype=torch.bfloat16) * 0.8)(D, N) / N ** 0.5), 'log_dt': _w((lambda *s: torch.randn(*s, dtype=torch.bfloat16) * 0.2)(D) * 0.5 - 4.0), 'D_skip': _w((lambda *s: torch.randn(*s, dtype=torch.bfloat16) * 0.2)(D))}
    _A_in = _A_setup()

    def _B_setup():
        B, S, D, F = (1, 2048, 512, 1376)
        Fh = F // 2
        return {'x': _w((lambda *s: torch.randn(*s, dtype=torch.bfloat16) * 0.05)(B, S, D)), 'w_in': _w((lambda *s: torch.randn(*s, dtype=torch.bfloat16) * 0.4)(D, 2 * Fh)), 'w_sp': _w(0.01 * (lambda *s: torch.randn(*s, dtype=torch.bfloat16) * 0.4)(S, S)), 'b_sp': _w(1.0 + 0.01 * (lambda *s: torch.randn(*s, dtype=torch.bfloat16) * 0.05)(S)), 'w_out': _w((lambda *s: torch.randn(*s, dtype=torch.bfloat16) * 0.05)(Fh, D))}
    _B_in = _B_setup()
    return {'A_u': _A_in['u'], 'A_log_A_real': _A_in['log_A_real'], 'A_A_imag': _A_in['A_imag'], 'A_C_real': _A_in['C_real'], 'A_C_imag': _A_in['C_imag'], 'A_log_dt': _A_in['log_dt'], 'A_D_skip': _A_in['D_skip'], 'B_x': _B_in['x'], 'B_w_in': _B_in['w_in'], 'B_w_sp': _B_in['w_sp'], 'B_b_sp': _B_in['b_sp'], 'B_w_out': _B_in['w_out'], 'c': _w((lambda *s: torch.randn(*s, dtype=torch.bfloat16) * 0.2)(1, 512) * 8.0), 'w_mod': _w((lambda *s: torch.randn(*s, dtype=torch.bfloat16) * 0.2)(512, 6 * 512) * 512 ** (-0.5) * 8.0)}

def reference(A_u, A_log_A_real, A_A_imag, A_C_real, A_C_imag, A_log_dt, A_D_skip, B_x, B_w_in, B_w_sp, B_b_sp, B_w_out, c, w_mod):

    def _A_ref(u, log_A_real, A_imag, C_real, C_imag, log_dt, D_skip):
        L = u.shape[1]
        dt = torch.exp(log_dt)
        A = -torch.exp(log_A_real) + 1j * A_imag
        C = (C_real + 1j * C_imag) * dt[:, None]
        pos = (lambda x, dt: x.to(dt))(torch.arange(L), log_dt.dtype)
        P = torch.exp((A * dt[:, None])[:, :, None] * pos[None, None, :])
        K = 2.0 * torch.einsum('dn,dnl->dl', C, P).real
        L2 = 2 * L
        u_t = (lambda x, *d: x.permute(*d))(u, 0, 2, 1)
        Kf = (lambda x, *a, **k: torch.fft.rfft(x.float(), *a, **k))(K, L2)
        Uf = (lambda x, *a, **k: torch.fft.rfft(x.float(), *a, **k))(u_t, L2)
        y = (lambda x, *a, **k: torch.fft.irfft(x, *a, **k).to(torch.bfloat16))(Uf * Kf[None, :, :], L2)[:, :, :L]
        y = (lambda x, *d: x.permute(*d))(y, 0, 2, 1)
        return y + u * D_skip[None, None, :]

    def _B_ref(x, w_in, w_sp, b_sp, w_out):
        B, S, D = x.shape
        F = w_in.shape[-1]
        Fh = F // 2
        z = _gelu(torch.einsum('bsd,df->bsf', x, w_in))
        z1 = z[..., :Fh].clone()
        z2 = _layernorm(z[..., Fh:], axis=-1)
        g = torch.einsum('ts,bsf->btf', w_sp, z2) + b_sp[None, :, None]
        return torch.einsum('bsf,fd->bsd', z1 * g, w_out)
    x = A_u
    mod = (_silu(c) @ w_mod).reshape(c.shape[0], 6, 1, -1)
    sa, ca, ga, sb, cb, gb = (mod[:, 0].clone(), mod[:, 1].clone(), mod[:, 2].clone(), mod[:, 3].clone(), mod[:, 4].clone(), mod[:, 5].clone())
    h = _layernorm(x, axis=-1) * (1 + ca) + sa
    x = x + ga * _A_ref(h, A_log_A_real, A_A_imag, A_C_real, A_C_imag, A_log_dt, A_D_skip)
    h = _layernorm(x, axis=-1) * (1 + cb) + sb
    x = x + gb * _B_ref(h, B_w_in, B_w_sp, B_b_sp, B_w_out)
    return x
```

```cpp
#include <hip/hip_runtime.h>
#include <hip/hip_bf16.h>
#include <stdint.h>
typedef unsigned short u16;
__device__ __forceinline__ float bf2f(u16 b){ return __uint_as_float(((unsigned)b)<<16); }
__device__ __forceinline__ u16 f2bf(float f){ unsigned u=__float_as_uint(f); u += 0x7FFFu + ((u>>16)&1u); return (u16)(u>>16); }
__device__ __forceinline__ float rbf(float f){ return bf2f(f2bf(f)); }
constexpr int L=2048, D=512, N=128, F=1376, FH=688;

__global__ void k_mod(const u16* c, const u16* wmod, float* mod){
  int j = blockIdx.x*blockDim.x+threadIdx.x; if(j>=3072) return;
  float acc=0;
  for(int k=0;k<512;k++){ float cv=bf2f(c[k]); float s=rbf(cv/(1.f+expf(-cv))); acc += s*bf2f(wmod[k*3072+j]); }
  mod[j]=rbf(acc);
}
__global__ void k_K(const u16* lar,const u16* aim,const u16* cr,const u16* ci,const u16* ldt,float* K){
  int d=blockIdx.y; int l=blockIdx.x*blockDim.x+threadIdx.x;
  float dt=rbf(expf(bf2f(ldt[d])));
  float pos=rbf((float)l);
  float acc=0;
  for(int n=0;n<N;n++){
    float ar=-rbf(expf(bf2f(lar[d*N+n])))*dt, ai=bf2f(aim[d*N+n])*dt;
    float Cr=bf2f(cr[d*N+n])*dt, Ci=bf2f(ci[d*N+n])*dt;
    float e=expf(ar*pos); float s,co; sincosf(ai*pos,&s,&co);
    acc += e*(Cr*co-Ci*s);
  }
  K[d*L+l]=2.f*acc;
}
__global__ void k_ln(const u16* x,int stride,int W,const float* sh,const float* sc,u16* out,int ostride){
  int t=blockIdx.x; __shared__ float red[256];
  float s=0; for(int i=threadIdx.x;i<W;i+=256) s+=bf2f(x[t*stride+i]);
  red[threadIdx.x]=s; __syncthreads();
  for(int o=128;o;o>>=1){ if(threadIdx.x<o) red[threadIdx.x]+=red[threadIdx.x+o]; __syncthreads(); }
  float m=red[0]/W; __syncthreads();
  s=0; for(int i=threadIdx.x;i<W;i+=256){ float v=bf2f(x[t*stride+i])-m; s+=v*v; }
  red[threadIdx.x]=s; __syncthreads();
  for(int o=128;o;o>>=1){ if(threadIdx.x<o) red[threadIdx.x]+=red[threadIdx.x+o]; __syncthreads(); }
  float v=rbf(red[0]/W); m=rbf(m);
  float den=rbf(sqrtf(rbf(v+1e-5f)));
  for(int i=threadIdx.x;i<W;i+=256){
    float h=rbf(rbf(bf2f(x[t*stride+i])-m)/den);
    if(sc){ h=rbf(h*rbf(1.f+sc[i])); h=rbf(h+sh[i]); }
    out[t*ostride+i]=f2bf(h);
  }
}
__global__ void k_conv(const u16* x,const u16* h,const float* K,const u16* dsk,const float* ga,u16* x1){
  int d=blockIdx.y; int t=blockIdx.x*256+threadIdx.x;
  __shared__ float hs[L]; __shared__ float ks[L];
  for(int i=threadIdx.x;i<L;i+=256){ hs[i]=bf2f(h[i*D+d]); ks[i]=K[d*L+i]; }
  __syncthreads();
  float acc=0; for(int j=0;j<=t;j++) acc+=ks[t-j]*hs[j];
  float y=rbf(acc); float a=rbf(y+rbf(hs[t]*bf2f(dsk[d])));
  x1[t*D+d]=f2bf(bf2f(x[t*D+d])+rbf(ga[d]*a));
}
__device__ float gelu(float v){ float u=0.7978845608028654f*(v+0.044715f*v*v*v); return 0.5f*v*(1.f+tanhf(u)); }
template<int MODE>
__global__ void k_gemm(const u16* A,int lda,const u16* B,int ldb,int M,int Nn,int K,u16* C,int ldc,const u16* e0,int lde,const u16* e1,const float* e2){
  __shared__ float As[16][17], Bs[16][17];
  int tx=threadIdx.x, ty=threadIdx.y; int r=blockIdx.y*16+ty, c=blockIdx.x*16+tx;
  float acc=0;
  for(int k0=0;k0<K;k0+=16){
    As[ty][tx]=bf2f(A[r*lda+k0+tx]); Bs[ty][tx]=bf2f(B[(k0+ty)*ldb+c]);
    __syncthreads();
    for(int k=0;k<16;k++) acc+=As[ty][k]*Bs[k][tx];
    __syncthreads();
  }
  float v=rbf(acc);
  if(MODE==0) v=gelu(v);
  if(MODE==1){ v=rbf(v+bf2f(e1[r])); v=bf2f(e0[r*lde+c])*v; }
  if(MODE==2){ v=rbf(e2[c]*v); v=bf2f(e0[r*lde+c])+v; }
  C[r*ldc+c]=f2bf(v);
}
__global__ void k_dummy(float* p){ if(threadIdx.x==0 && blockIdx.x==0) p[0]=1.f; }
#define NDUMMY 0
extern "C" void kernel_launch(void* const* d_in, const int* in_sizes, int n_in, void* d_out, int out_size, void* d_ws, size_t ws_size, hipStream_t stream){
  const u16* u=(const u16*)d_in[0]; const u16* lar=(const u16*)d_in[1]; const u16* aim=(const u16*)d_in[2];
  const u16* cr=(const u16*)d_in[3]; const u16* ci=(const u16*)d_in[4]; const u16* ldt=(const u16*)d_in[5]; const u16* dsk=(const u16*)d_in[6];
  const u16* win=(const u16*)d_in[8]; const u16* wsp=(const u16*)d_in[9]; const u16* bsp=(const u16*)d_in[10]; const u16* wout=(const u16*)d_in[11];
  const u16* c=(const u16*)d_in[12]; const u16* wmod=(const u16*)d_in[13];
  char* ws=(char*)d_ws;
  float* mod=(float*)ws; float* K=(float*)(ws+(1<<20)); u16* h=(u16*)(ws+(8<<20)); u16* x1=(u16*)(ws+(12<<20)); u16* h2=(u16*)(ws+(16<<20));
  u16* z=(u16*)(ws+(20<<20)); u16* z2=(u16*)(ws+(28<<20)); u16* p=(u16*)(ws+(32<<20));
  k_mod<<<12,256,0,stream>>>(c,wmod,mod);
  k_K<<<dim3(8,512),256,0,stream>>>(lar,aim,cr,ci,ldt,K);
  k_ln<<<L,256,0,stream>>>(u,D,D,mod,mod+512,h,D);
  k_conv<<<dim3(8,512),256,0,stream>>>(u,h,K,dsk,mod+1024,x1);
  k_ln<<<L,256,0,stream>>>(x1,D,D,mod+1536,mod+2048,h2,D);
  k_gemm<0><<<dim3(F/16,L/16),dim3(16,16),0,stream>>>(h2,D,win,F,L,F,D,z,F,nullptr,0,nullptr,nullptr);
  k_ln<<<L,256,0,stream>>>(z+FH,F,FH,nullptr,nullptr,z2,FH);
  k_gemm<1><<<dim3(FH/16,L/16),dim3(16,16),0,stream>>>(wsp,L,z2,FH,L,FH,L,p,FH,z,F,bsp,nullptr);
  k_gemm<2><<<dim3(D/16,L/16),dim3(16,16),0,stream>>>(p,FH,wout,D,L,D,FH,(u16*)d_out,D,x1,D,nullptr,mod+2560);
  for(int i=0;i<NDUMMY;i++) k_dummy<<<256,256,0,stream>>>((float*)(ws+(40<<20)));
}
```

```cpp
#include <hip/hip_runtime.h>
#include <stdint.h>

typedef unsigned u32;
typedef unsigned short u16;
typedef unsigned long long u64;
typedef u32 v4u __attribute__((ext_vector_type(4)));
typedef u32 v2u __attribute__((ext_vector_type(2)));
typedef float f4 __attribute__((ext_vector_type(4)));
typedef float f2 __attribute__((ext_vector_type(2)));
typedef __bf16 bf8 __attribute__((ext_vector_type(8)));
typedef __bf16 bf2 __attribute__((ext_vector_type(2)));
typedef _Float16 h8 __attribute__((ext_vector_type(8)));
typedef _Float16 h2 __attribute__((ext_vector_type(2)));
typedef short s4 __attribute__((ext_vector_type(4)));
#define LAS __attribute__((address_space(3)))
#define GAS __attribute__((address_space(1)))
#define CAS __attribute__((address_space(4)))
#define DI __device__ __forceinline__
typedef __amdgpu_buffer_rsrc_t rsrc_t;
typedef LAS unsigned char* lptr;

#define XMAP 2

constexpr int WS_E = 0, WS_STAT1 = 4096, WS_CD = 32768, WS_ST2 = 65536, WS_BWP = 393216, WS_MOD = 524288;
constexpr int WS_XG = 1 << 20, WS_X1 = 5 << 20, WS_Z2N = 7 << 20, WS_PB = 10 << 20, WS_WINP = 13 << 20, WS_WSPP = 16 << 20;
constexpr int WS_DBG = 24 << 20, WS_END = 25 << 20;
constexpr int OOB = 0x40000000;
constexpr int TABP = 528, TAB_CH = 63 * TABP;
constexpr int L_TAB = 0, L_Y = 0, Y_CH = 21504;
constexpr int L_TOE = 66560, TOE_C = 5312, TOE_CH = 4 * TOE_C;
constexpr int L_U = 109056, U_CH = 6320;
constexpr int L_PKW = 121856;
constexpr int L_PKI = 154624;
constexpr int L_XIMG = 0, L_TRS = 65536, TRS_P = 64, TRS_W = 64 * TRS_P;
constexpr int L_RED = 0, L_WO = 81920, L_WO2 = 0;
constexpr int L_XST = 159744, L_ZST = L_XST + 1024;
constexpr int L_MODP = L_PKI + 8192;
constexpr int LDS_BYTES = L_MODP + 1024;
constexpr int CODE_BYTES = 47104;
constexpr int SC1 = 16;
constexpr int SPIN_CAP = 1 << 15;
static_assert(LDS_BYTES <= 163840, "LDS");

DI float bflo(u32 v) { return __uint_as_float(v << 16); }
DI float bfhi(u32 v) { return __uint_as_float(v & 0xffff0000u); }
DI f2 bf2f(u32 v) { return f2{bflo(v), bfhi(v)}; }
DI u32 pkbf(float a, float b) { f2 v = {a, b}; return __builtin_bit_cast(u32, __builtin_convertvector(v, bf2)); }
DI u32 pkbf(f2 v) { return __builtin_bit_cast(u32, __builtin_convertvector(v, bf2)); }
DI u32 pkh(f2 v) { return __builtin_bit_cast(u32, __builtin_convertvector(v, h2)); }
DI float rbf(float a) { return bflo(pkbf(a, a)); }
DI u32 san(u32 v) { return v < 0xFFFFFFFEu ? v : 0xFFFFFFFEu; }
DI v4u san4(v4u v) { return v4u{san(v.x), san(v.y), san(v.z), san(v.w)}; }
DI u32 umax3(u32 a, u32 b, u32 c) { return max(max(a, b), c); }
DI u32 vmax(u32 m, v4u v) { return umax3(umax3(m, v.x, v.y), v.z, v.w); }
DI v4u ld128(rsrc_t rs, int off) { return __builtin_amdgcn_raw_buffer_load_b128(rs, off, 0, SC1); }
DI v4u ld128s(rsrc_t rs, int off, int soff) { return __builtin_amdgcn_raw_buffer_load_b128(rs, off, soff, SC1); }
DI void st128(rsrc_t rs, int off, v4u v) { __builtin_amdgcn_raw_buffer_store_b128(v, rs, off, 0, SC1); }
DI void st128s(rsrc_t rs, int off, int soff, v4u v) { __builtin_amdgcn_raw_buffer_store_b128(v, rs, off, soff, SC1); }
DI void st64(rsrc_t rs, int off, v2u v) { __builtin_amdgcn_raw_buffer_store_b64(v, rs, off, 0, SC1); }
#define DRAIN() asm volatile("s_waitcnt vmcnt(0)" ::: "memory")
#define SB() __builtin_amdgcn_sched_barrier(0)
#define CFENCE() do { asm volatile("" ::: "memory"); SB(); } while (0)
template <int C> DI u32 dppu(u32 v) { return (u32)__builtin_amdgcn_update_dpp(0, (int)v, C, 0xF, 0xF, true); }
template <int C> DI float dppadd(float v) { return v + __uint_as_float(dppu<C>(__float_as_uint(v))); }
DI float sw16sum(float a, float b) { auto r = __builtin_amdgcn_permlane16_swap(__float_as_uint(a), __float_as_uint(b), false, false); return __uint_as_float(r[0]) + __uint_as_float(r[1]); }
DI float sw32sum(float a, float b) { auto r = __builtin_amdgcn_permlane32_swap(__float_as_uint(a), __float_as_uint(b), false, false); return __uint_as_float(r[0]) + __uint_as_float(r[1]); }
DI float quadsum(float v) { v = dppadd<0xB1>(v); return dppadd<0x4E>(v); }
DI float rowsum(float v) { v = quadsum(v); v = dppadd<0x141>(v); return dppadd<0x140>(v); }
DI f4 mfma_bf(v4u a, v4u b, f4 c) { return __builtin_amdgcn_mfma_f32_16x16x32_bf16(__builtin_bit_cast(bf8, a), __builtin_bit_cast(bf8, b), c, 0, 0, 0); }
DI f4 mfma_h(v4u a, v4u b, f4 c) { return __builtin_amdgcn_mfma_f32_16x16x32_f16(__builtin_bit_cast(h8, a), __builtin_bit_cast(h8, b), c, 0, 0, 0); }
DI v2u trread(lptr p) { return __builtin_bit_cast(v2u, __builtin_amdgcn_ds_read_tr16_b64_v4i16((LAS s4*)p)); }
struct cx { f2 v, s; };
DI f2 cmul(f2 a, const cx& b) { f2 t = f2{a.x, a.x} * b.v; return __builtin_elementwise_fma(f2{a.y, a.y}, b.s, t); }
DI cx mkcx(f2 v) { cx r; r.v = v; r.s = f2{-v.y, v.x}; return r; }
DI f2 cexp_p(float al2, float rev, float p) {
  float m = __builtin_amdgcn_exp2f(al2 * p), f = __builtin_amdgcn_fractf(rev * p);
  return f2{m * __builtin_amdgcn_cosf(f), m * __builtin_amdgcn_sinf(f)};
}
DI f2 gelu2(f2 v) {
  const float c0 = -2.0f * 0.7978845608028654f * 1.4426950408889634f, c1 = c0 * 0.044715f;
  const f2 a = v * __builtin_elementwise_fma(v * v, f2{c1, c1}, f2{c0, c0});
  const f2 d = f2{__builtin_amdgcn_exp2f(a.x), __builtin_amdgcn_exp2f(a.y)} + 1.0f;
  return v * f2{__builtin_amdgcn_rcpf(d.x), __builtin_amdgcn_rcpf(d.y)};
}
DI float ln_den(float var) { return rbf(__builtin_amdgcn_sqrtf(rbf(rbf(var) + 1e-5f))); }
template <class T> DI T gld(const void* base, u32 off) { return *(const GAS T*)((const GAS char*)base + off); }
DI void glds16(const void* gsrc, u32 lds_dst) {
  u32 keep;
  asm volatile("s_mov_b32 %0, m0\n\ts_mov_b32 m0, %2\n\ts_nop 0\n\tglobal_load_lds_dwordx4 %1, off\n\ts_mov_b32 m0, %0" : "=&s"(keep) : "v"(gsrc), "s"(lds_dst) : "memory");
}
#define MF "v_mfma_f32_16x16x32_bf16 "
DI void m_av(f4& c, v4u a, v4u b) { asm volatile(MF "%0, %1, %2, %0" : "+v"(c) : "a"(a), "v"(b)); }
DI void m_va(f4& c, v4u a, v4u b) { asm volatile(MF "%0, %1, %2, %0" : "+v"(c) : "v"(a), "a"(b)); }
DI void m_vv(f4& c, v4u a, v4u b) { asm volatile(MF "%0, %1, %2, %0" : "+v"(c) : "v"(a), "v"(b)); }
DI void m_av0(f4& c, v4u a, v4u b) { asm volatile(MF "%0, %1, %2, 0" : "=&v"(c) : "a"(a), "v"(b)); }
DI void m_va0(f4& c, v4u a, v4u b) { asm volatile(MF "%0, %1, %2, 0" : "=&v"(c) : "v"(a), "a"(b)); }
DI void m_vv0(f4& c, v4u a, v4u b) { asm volatile(MF "%0, %1, %2, 0" : "=&v"(c) : "v"(a), "v"(b)); }
template <class T> DI void pin(T& v) { asm volatile("" : "+v"(v)); }
#define MM_HEAD() do { SB(); asm volatile("s_nop 7"); SB(); } while (0)
#define MM_TAIL() do { SB(); asm volatile("s_nop 7"); SB(); } while (0)
DI int toe_addr(int pos) { return 2 * pos + 8 * (pos >> 4); }

#define STAMP(n)
#define STAMP_FLUSH()
#define STAMP_INIT()
#define CNT(n,v)

template <int BAND> struct Band {
  static constexpr int R = 128 - 32 * BAND, NT = R / 16, NS = 15 + NT / 2;
  static constexpr int YO = BAND == 0 ? 0 : BAND == 1 ? 8448 : BAND == 2 ? 14848 : 19200;
  lptr al, ah, bb, yp;
  f4 acc[NT];
  v2u lo[32], hi[32];
  v4u bf[NS];
  DI void init(lptr L, int ch, int lane) {
    const int r = lane & 15, q = lane >> 4, c = (3 - r) & 3;
    const int P = 2048 - 512 * BAND - 496 - 4 * (r >> 2) + 8 * q;
    al = L + L_TOE + ch * TOE_CH + c * TOE_C + toe_addr(P);
    ah = L + L_TOE + ch * TOE_CH + c * TOE_C + toe_addr(P + 4);
    bb = L + L_U + ch * U_CH + 80 * (R / 32) * r + 16 * q;
    yp = L + L_Y + ch * Y_CH + YO + r * (4 * R + 16) + 16 * q;
  }
  DI void load(int s, int i) {
    if (i < 4) {
      const int n = 2 * s + (i >> 1);
      if (n < 32) { if (i & 1) hi[n] = *(LAS v2u*)(ah + 40 * (31 - n)); else lo[n] = *(LAS v2u*)(al + 40 * (31 - n)); }
    } else if (s < NS) bf[s] = *(LAS v4u*)(bb + 80 * (NT / 2 - 1 - s + 15));
  }
  DI void mma(int s, int t) {
    const int n = t - NT + 2 + 2 * s;
    if (s < NS && n >= 0 && n < 32) acc[t] = mfma_bf(v4u{lo[n].x, lo[n].y, hi[n].x, hi[n].y}, bf[s], n < 2 ? f4{0.f, 0.f, 0.f, 0.f} : acc[t]);
  }
  DI void flush(int s, int t) {
    const int n = t - NT + 2 + 2 * (s - 1);
    if (n == 30 || n == 31) *(LAS f4*)(yp + 64 * t) = acc[t];
  }
};
template <int B1, int B2, class F> DI void conv2(lptr L, int ch, int lane, F side) {
  Band<B1> u; Band<B2> v;
  u.init(L, ch, lane); v.init(L, ch, lane);
  constexpr int N1 = Band<B1>::NT, NS = Band<B1>::NS;
#pragma unroll
  for (int s = 0; s < 2; ++s)
#pragma unroll
    for (int i = 0; i < 5; ++i) { u.load(s, i); v.load(s, i); SB(); }
#pragma unroll
  for (int s = 0; s <= NS; ++s)
#pragma unroll
    for (int i = 0; i < 10; ++i) {
      if (i < N1) { u.mma(s, i); u.flush(s, i); } else { v.mma(s, i - N1); v.flush(s, i - N1); }
      if (i < 5) u.load(s + 2, i); else v.load(s + 2, i - 5);
      side(s, i);
      SB();
    }
}

__global__ void __launch_bounds__(256, 1)
fused_kernel(const u16* __restrict__ x, const u16* __restrict__ lar, const u16* __restrict__ aim, const u16* __restrict__ cre,
             const u16* __restrict__ cim, const u16* __restrict__ ldt, const u16* __restrict__ dsk, const u16* __restrict__ win,
             const u16* __restrict__ wsp, const u16* __restrict__ bsp, const u16* __restrict__ wout, const u16* __restrict__ cvec,
             const u16* __restrict__ wmod, u16* __restrict__ out, unsigned char* ws) {
  extern __shared__ __attribute__((aligned(16))) unsigned char smem[];
  lptr L = (lptr)smem;
  const u32 b = blockIdx.x, tid = threadIdx.x, lane = tid & 63;
  STAMP_INIT();
  STAMP(0);
  u32 cpf = 0;
  {
    const u64 pc = __builtin_amdgcn_s_getpc() & ~(u64)255;
    if (tid < CODE_BYTES / 32 / 64) cpf = gld<u32>((const void*)pc, (b >> 3) * (CODE_BYTES / 32) + tid * 64);
  }
  CFENCE();
  const u32 w = __builtin_amdgcn_readfirstlane(tid >> 6);
  const u32 xc = b & 7, rr = b >> 3, cp = xc * 32 + rr;
  const u32 c16 = lane & 15, q4 = lane >> 4;
  const u32 tag = *(const CAS u32*)(ws + WS_E) + 1u;
  const u32 tb = 512 * (b >> 6) + 32 * (b & 15) + 2 * ((b >> 4) & 3);
  const v4u sr0 = gld<v4u>(x, (tb + 8 * w) * 1024 + lane * 16), sr1 = gld<v4u>(x, (tb + 8 * w) * 1024 + 1024 + lane * 16);
  const u32 mG = b % 48, mP = min(b / 48, 3u), mrow = mP * 128 + w * 32 + (lane >> 3);
  v4u wm[4];
  u32 cv[4];
#pragma unroll
  for (int n = 0; n < 4; ++n) wm[n] = gld<v4u>(wmod, (mrow + 8 * n) * 6144 + mG * 128 + (lane & 7) * 16);
#pragma unroll
  for (int n = 0; n < 4; ++n) cv[n] = gld<u16>(cvec, (mrow + 8 * n) * 2);
  const u32 xgo = (tb + 8 * (lane & 3)) * 1024 + (16 * w + (lane >> 2)) * 16;
  const v4u xg0 = gld<v4u>(x, xgo), xg1 = gld<v4u>(x, xgo + 1024);
  const u32 pidx = (cp * 256 + tid) * 2;
  const u32 p_lar = gld<u16>(lar, pidx), p_aim = gld<u16>(aim, pidx), p_cr = gld<u16>(cre, pidx), p_ci = gld<u16>(cim, pidx);
  const u32 p_cr2 = gld<u16>(cre, pidx ^ 128), p_ci2 = gld<u16>(cim, pidx ^ 128);
  const u32 p_ldt = *(const CAS u32*)(ldt + 2 * cp), p_dsk = *(const CAS u32*)(dsk + 2 * cp);
  CFENCE();
  const u32 pi = (xc >> 1) * 8 + (rr & 7), pj = (xc & 1) * 4 + (rr >> 3);
  const u32 nb = pj < 3 ? 6 : 5, blk0 = 5 * pj + min(pj, 3u);
  const bool two = w + 4 < nb;
  const u32 wio_src = ((32 * (pi >> 1) + (lane >> 1)) * 1376 + (pi & 1) * 688 + 16 * (blk0 + w)) * 2 + (lane & 1) * 16;
  const v4u wi0 = gld<v4u>(win, wio_src), wi1 = gld<v4u>(win, two ? wio_src + 128 : wio_src);
  u32 opf = 0;
  if (tid < 86) opf = gld<u32>(wout, ((pi & 7) * 86 + tid) * 1024 + pj * 128);
  CFENCE();
  const rsrc_t rs = __builtin_amdgcn_make_buffer_rsrc((void*)ws, 0, WS_END, 0x00020000);
  const v4u SENT = {~0u, ~0u, ~0u, ~0u};
  if (tid < 40) *(LAS v4u*)(L + L_TOE + (tid / 5) * TOE_C + 5120 + (tid % 5) * 16) = v4u{0, 0, 0, 0};
  if (tid < 150) *(LAS v4u*)(L + L_U + (tid / 75) * U_CH + (tid % 75) * 16) = v4u{0, 0, 0, 0};
  STAMP(23);
  {
    f2 a[8];
#pragma unroll
    for (int e = 0; e < 4; ++e) { a[2 * e] = f2{bflo(sr0[e]), bflo(sr1[e])}; a[2 * e + 1] = f2{bfhi(sr0[e]), bfhi(sr1[e])}; }
    f2 s = ((a[0] + a[1]) + (a[2] + a[3])) + ((a[4] + a[5]) + (a[6] + a[7]));
    float t = rowsum(sw32sum(s.x, s.y));
    t = sw16sum(t, t) * (1.f / 512);
    auto mm = __builtin_amdgcn_permlane32_swap(__float_as_uint(t), __float_as_uint(t), false, false);
    const f2 m = {__uint_as_float(mm[0]), __uint_as_float(mm[1])};
    s = f2{0.f, 0.f};
#pragma unroll
    for (int e = 0; e < 8; ++e) { const f2 u = a[e] - m; s = __builtin_elementwise_fma(u, u, s); }
    float v = rowsum(sw32sum(s.x, s.y));
    v = sw16sum(v, v) * (1.f / 512);
    if ((lane & 31) == 0) st64(rs, WS_STAT1 + b * 64 + w * 16 + (lane >> 5) * 8, v2u{pkbf(t, ln_den(v)), tag});
  }
  STAMP(22);
  {
    f2 a[4];
#pragma unroll
    for (int n = 0; n < 4; ++n) {
      const float c = bflo(cv[n]);
      const float s = rbf(c * __builtin_amdgcn_rcpf(1.f + __builtin_amdgcn_exp2f(-1.4426950408889634f * c)));
#pragma unroll
      for (int e = 0; e < 4; ++e) a[e] = n ? a[e] + bf2f(wm[n][e]) * s : bf2f(wm[n][e]) * s;
    }
    const float u0 = sw32sum(a[0].x, a[2].x), u1 = sw32sum(a[0].y, a[2].y), u2 = sw32sum(a[1].x, a[3].x), u3 = sw32sum(a[1].y, a[3].y);
    const f2 t = {dppadd<0x128>(sw16sum(u0, u2)), dppadd<0x128>(sw16sum(u1, u3))};
    if (!(lane & 8)) *(LAS f2*)(L + L_MODP + w * 256 + (lane & 7) * 32 + (lane >> 4) * 8) = t;
  }
  {
    const int o = WS_XG + (4 * (16 * w + (lane >> 2))) * 16384 + b * 64 + (lane & 3) * 16;
    st128(rs, o, v4u{xg0.x, tag, xg1.x, tag});
    st128(rs, o + 16384, v4u{xg0.y, tag, xg1.y, tag});
    st128(rs, o + 32768, v4u{xg0.z, tag, xg1.z, tag});
    st128(rs, o + 49152, v4u{xg0.w, tag, xg1.w, tag});
  }
  __syncthreads();
  if (tid < 32 && b < 192) {
    LAS f2* mp = (LAS f2*)(L + L_MODP) + tid;
    const f2 s = (mp[0] + mp[32]) + (mp[64] + mp[96]);
    st128(rs, WS_MOD + (mP * 1536 + mG * 32 + tid) * 16, v4u{__float_as_uint(s.x), tag, __float_as_uint(s.y), tag});
  }
  CFENCE();
  STAMP(1);
  const int x1w = WS_X1 + ((tid >> 3) * 512 + 2 * cp) * 128 + (tid & 7) * 16;
  const int wio = WS_WINP + (((pj * 2 + (pi & 1)) * 6 + w) * 16 + (pi >> 1)) * 1024 + lane * 16;
  st128(rs, x1w, SENT);
  st128(rs, x1w + 128, SENT);
  {
    const int zco = WS_Z2N + ((pj * 6 + (w >> 1)) * 64 + 2 * pi + (w & 1)) * 1024 + lane * 16;
    st128(rs, zco, SENT);
    st128s(rs, zco, 131072, SENT);
    st128s(rs, zco, w + 8 < 2 * nb ? 262144 : OOB, SENT);
  }
  *(LAS v4u*)(L + L_PKI + w * 2048 + lane * 16) = wi0;
  *(LAS v4u*)(L + L_PKI + w * 2048 + 1024 + lane * 16) = wi1;
  asm volatile("" ::"v"(cpf), "v"(opf));
#pragma unroll
  for (int k = 0; k < 8; ++k) {
    const u32 row = 2 * k + (lane >> 5);
    glds16((const char*)wsp + ((64 * pi + 16 * w + row) * 4096 + 512 * pj + (((lane & 31) ^ row) * 16)), (u32)(size_t)L + L_PKW + w * 8192 + k * 1024);
  }
  CFENCE();
  float kscale;
  {
    const float L2E = 1.4426950408889634f;
    const float dt = rbf(__builtin_amdgcn_exp2f(((w >> 1) ? bfhi(p_ldt) : bflo(p_ldt)) * L2E));
    const float al2 = (-rbf(__builtin_amdgcn_exp2f(bflo(p_lar) * L2E)) * dt) * L2E;
    const float rev = (bflo(p_aim) * dt) * 0.15915494309189535f;
    u32 mx = max(max(p_cr & 0x7fffu, p_ci & 0x7fffu), max(p_cr2 & 0x7fffu, p_ci2 & 0x7fffu));
    mx = max(mx, dppu<0xB1>(mx)); mx = max(mx, dppu<0x4E>(mx)); mx = max(mx, dppu<0x141>(mx)); mx = max(mx, dppu<0x140>(mx));
    { auto r = __builtin_amdgcn_permlane16_swap(mx, mx, false, false); mx = max(r[0], r[1]); }
    { auto r = __builtin_amdgcn_permlane32_swap(mx, mx, false, false); mx = max(r[0], r[1]); }
    u32 ex = mx >> 7;
    ex = min(max(ex, 1u), 253u);
    const float dn = __uint_as_float((254u - ex) << 23), up = __uint_as_float(ex << 23);
    kscale = 2.0f * dt * up;
    LAS u32* tp = (LAS u32*)(L + L_TAB + (tid >> 7) * TAB_CH + (tid & 127) * 4);
    auto put = [&](int row, f2 v) { tp[row * (TABP / 4)] = pkh(v); };
    const cx w1 = mkcx(cexp_p(al2, rev, 1.f)), w16 = mkcx(cexp_p(al2, -rev, 16.f)), w256 = mkcx(cexp_p(al2, rev, 256.f));
    f2 U[17];
    U[0] = f2{bflo(p_cr) * dn, bflo(p_ci) * dn};
#pragma unroll
    for (int o = 1; o <= 16; ++o) U[o] = cmul(U[o - 1], w1);
#pragma unroll
    for (int o = 0; o < 16; ++o) put(o, U[o]);
    f2 y = f2{1.f, 0.f};
    put(47, y);
    y = w16.v;
    put(48, y);
#pragma unroll
    for (int m = 2; m < 16; ++m) { y = cmul(y, w16); put(47 + m, y); }
    f2 V = w256.v;
#pragma unroll
    for (int A = 1; A < 8; ++A) {
      if (A > 1) V = cmul(V, w256);
      const cx Vc = mkcx(V);
      const int st = A == 1 ? 2 : A < 4 ? 4 : 8, r0 = A == 1 ? 16 : A < 4 ? 25 + 5 * (A - 2) : 35 + 3 * (A - 4);
#pragma unroll
      for (int o = 0; o <= 16; o += st) put(r0 + o / st, cmul(U[o], Vc));
    }
  }
  const int mo = WS_MOD + ((lane & 3) * 1536 + 16 * (pi >> 1) + (lane >> 2)) * 16;
  v4u gsb = ld128(rs, mo + 768 * 16), gcb = ld128(rs, mo + 1024 * 16);
  __syncthreads();
  STAMP(2);
  {
    const u32 ch = w >> 1, hi = w & 1;
    lptr tb_ = L + L_TAB + ch * TAB_CH + 16 * q4;
    const u32 i2 = (c16 + ((c16 >> 1) & 1)) >> 1, i4 = (c16 + 1 + ((c16 >> 2) & 1)) >> 2, i8 = (c16 + 3 + ((c16 >> 3) & 1)) >> 3;
    const u32 row[4] = {hi ? 35 + i8 : c16, hi ? 38 + i8 : 16 + i2, hi ? 41 + i8 : 25 + i4, hi ? 44 + i8 : 30 + i4};
    v4u yf[8], xf[8][4];
    auto ldk = [&](int k, int i) {
      if (k < 8) { if (i < 4) xf[k][i] = *(LAS v4u*)(tb_ + row[i] * TABP + 64 * k); else yf[k] = *(LAS v4u*)(tb_ + (47 + c16) * TABP + 64 * k); }
    };
#pragma unroll
    for (int k = 0; k < 2; ++k)
#pragma unroll
      for (int i = 4; i >= 0; --i) ldk(k, i);
    SB();
    f4 acc[4];
#pragma unroll
    for (int k = 0; k < 8; ++k)
#pragma unroll
      for (int a = 3; a >= 0; --a) {
        acc[a] = mfma_h(xf[k][a], yf[k], k ? acc[a] : f4{0.f, 0.f, 0.f, 0.f});
        if (a == 3) ldk(k + 2, 4);
        ldk(k + 2, a);
        SB();
      }
    const int i0 = 2044 - (1024 * hi + 768 + 16 * c16 + 4 * q4);
    lptr t0 = L + L_TOE + ch * TOE_CH + toe_addr(i0), t1 = L + L_TOE + ch * TOE_CH + toe_addr(i0 + 4);
#pragma unroll
    for (int a = 0; a < 4; ++a) {
      const f4 v = acc[a] * kscale;
      const u32 P0 = pkbf(v.w, v.z), P1 = pkbf(v.y, v.x), M = pkbf(v.z, v.y);
      lptr p0 = t0 + 640 * (3 - a), p1 = t1 + 640 * (3 - a);
      *(LAS v2u*)(p1) = v2u{P0, P1};
      *(LAS u16*)(p0 + TOE_C + 6) = (u16)P0; *(LAS u32*)(p1 + TOE_C) = M; *(LAS u16*)(p1 + TOE_C + 4) = (u16)(P1 >> 16);
      *(LAS u32*)(p0 + 2 * TOE_C + 4) = P0; *(LAS u32*)(p1 + 2 * TOE_C) = P1;
      *(LAS u16*)(p0 + 3 * TOE_C + 2) = (u16)P0; *(LAS u32*)(p0 + 3 * TOE_C + 4) = M; *(LAS u16*)(p1 + 3 * TOE_C) = (u16)(P1 >> 16);
    }
  }
  STAMP(3);
  v4u gx[4], gs[4], gm;
  const int mo2 = WS_MOD + ((lane & 3) * 1536 + 256 * min(lane >> 2, 2u) + cp) * 16;
  auto s1_load = [&]() {
#pragma unroll
    for (int k = 0; k < 4; ++k) gx[k] = ld128(rs, WS_XG + cp * 16384 + (4 * w + k) * 1024 + lane * 16);
#pragma unroll
    for (int k = 0; k < 4; ++k) gs[k] = ld128(rs, WS_STAT1 + (4 * w + k) * 1024 + lane * 16);
    gm = ld128(rs, mo2);
  };
#pragma nounroll
  for (int sp = 0; sp < SPIN_CAP; ++sp) {
    if (!__any(((gsb.y ^ tag) | (gsb.w ^ tag) | (gcb.y ^ tag) | (gcb.w ^ tag)) != 0)) break;
    asm volatile("" ::: "memory");
    gsb = ld128(rs, mo + 768 * 16); gcb = ld128(rs, mo + 1024 * 16);
  }
  s1_load();
  CFENCE();
  v4u wpk[2];
  float bwp[2];
  {
    const u32 sbp = pkbf(quadsum(__uint_as_float(gsb.x)), quadsum(__uint_as_float(gsb.z)));
    const u32 cbp = pkbf(quadsum(__uint_as_float(gcb.x)), quadsum(__uint_as_float(gcb.z)));
    const u32 scp = pkbf(bf2f(cbp) + 1.0f);
    const f2 sc[4] = {bf2f(dppu<0x150>(scp)), bf2f(dppu<0x154>(scp)), bf2f(dppu<0x158>(scp)), bf2f(dppu<0x15C>(scp))};
    const f2 sb[4] = {bf2f(dppu<0x150>(sbp)), bf2f(dppu<0x154>(sbp)), bf2f(dppu<0x158>(sbp)), bf2f(dppu<0x15C>(sbp))};
#pragma unroll
    for (int f = 0; f < 2; ++f) {
      lptr p = L + L_PKI + w * 2048 + f * 1024 + (8 * q4 + (c16 >> 2)) * 32 + 8 * (c16 & 3);
      const v2u lo = trread(p), hi = trread(p + 128);
      const u32 r[4] = {lo.x, lo.y, hi.x, hi.y};
      f2 bw = {0.f, 0.f};
#pragma unroll
      for (int j = 0; j < 4; ++j) {
        const f2 v = bf2f(r[j]);
        wpk[f][j] = pkbf(v * sc[j]);
        bw = __builtin_elementwise_fma(v, sb[j], bw);
      }
      float t = bw.x + bw.y;
      t = sw16sum(t, t);
      bwp[f] = sw32sum(t, t);
    }
    const int bwo = lane < 16 ? WS_BWP + (((pj * 2 + (pi & 1)) * 6 + w) * 16 + (pi >> 1)) * 64 + lane * 4 : OOB;
    st128(rs, wio, wpk[0]);
    st128s(rs, wio, two ? 65536 : OOB, wpk[1]);
    __builtin_amdgcn_raw_buffer_store_b32(__float_as_uint(bwp[0]), rs, bwo, 0, SC1);
    __builtin_amdgcn_raw_buffer_store_b32(__float_as_uint(bwp[1]), rs, bwo, two ? 4096 : OOB, SC1);
  }
  CFENCE();
  STAMP(27);
#pragma nounroll
  for (int sp = 0; sp < SPIN_CAP; ++sp) {
    u32 bad = (gm.y ^ tag) | (gm.w ^ tag);
#pragma unroll
    for (int k = 0; k < 4; ++k) bad |= (gx[k].y ^ tag) | (gx[k].w ^ tag) | (gs[k].y ^ tag) | (gs[k].w ^ tag);
    if (!__any(bad != 0)) break;
    asm volatile("" ::: "memory");
    s1_load();
    CNT(29, 1);
  }
  DRAIN();
  __builtin_amdgcn_raw_buffer_store_b32(tag, rs, lane == 0 ? WS_CD + b * 64 + w * 4 : OOB, 0, SC1);
  STAMP(4);
  u32 xv[8];
  u32 h0[4], h1[4];
  u32 gav;
  {
    const u32 mv = pkbf(quadsum(__uint_as_float(gm.x)), quadsum(__uint_as_float(gm.z)));
    const u32 sav = __builtin_amdgcn_readlane(mv, 0), cav = __builtin_amdgcn_readlane(mv, 4);
    gav = __builtin_amdgcn_readlane(mv, 8);
    const u32 s1v = pkbf(bf2f(cav) + 1.0f);
    const float s10 = bflo(s1v), s11 = bfhi(s1v), sa0 = bflo(sav), sa1 = bfhi(sav);
#pragma unroll
    for (int k = 0; k < 4; ++k) {
      xv[2 * k] = gx[k].x; xv[2 * k + 1] = gx[k].z;
      const f2 m = {bflo(gs[k].x), bflo(gs[k].z)}, r = {__builtin_amdgcn_rcpf(bfhi(gs[k].x)), __builtin_amdgcn_rcpf(bfhi(gs[k].z))};
      h0[k] = pkbf((f2{bflo(gx[k].x), bflo(gx[k].z)} - m) * r * s10 + sa0);
      h1[k] = pkbf((f2{bfhi(gx[k].x), bfhi(gx[k].z)} - m) * r * s11 + sa1);
    }
    lptr up = L + L_U + 80 * (15 + (tid >> 2)) + 16 * (tid & 3);
    *(LAS v4u*)up = v4u{h0[0], h0[1], h0[2], h0[3]};
    *(LAS v4u*)(up + U_CH) = v4u{h1[0], h1[1], h1[2], h1[3]};
  }
  __syncthreads();
  STAMP(5);
  const int wpo = WS_WSPP + ((pi * 64 + 8 * pj) * 4 + w) * 1024 + lane * 16;
  lptr wql = L + L_PKW + w * 8192 + c16 * 512;
  v4u wq[8], cdv;
  auto side = [&](int s, int i) {
    if (s < 8 && i == 0) wq[s] = *(LAS v4u*)(wql + (((4 * s + q4) ^ c16) * 16));
    if (s >= 1 && s < 9 && i == 7) st128s(rs, wpo, (s - 1) * 4096, wq[s - 1]);
    if (s == 14 && i == 2) cdv = ld128(rs, WS_CD + tid * 64);
  };
  if (w & 1) conv2<1, 2>(L, w >> 1, lane, side);
  else conv2<0, 3>(L, w >> 1, lane, side);
  STAMP(21);
#pragma nounroll
  for (int sp = 0; sp < SPIN_CAP; ++sp) {
    if (!__any(((cdv.x ^ tag) | (cdv.y ^ tag) | (cdv.z ^ tag) | (cdv.w ^ tag)) != 0)) break;
    asm volatile("" ::: "memory");
    cdv = ld128(rs, WS_CD + tid * 64);
  }
  DRAIN();
  __syncthreads();
  STAMP(6);
  {
    f2 y[2][4];
    const float dk0 = bflo(p_dsk), dk1 = bfhi(p_dsk);
#pragma unroll
    for (int k = 0; k < 4; ++k) { y[0][k] = bf2f(h0[k]) * dk0; y[1][k] = bf2f(h1[k]) * dk1; }
#pragma unroll
    for (int B = 0; B < 4; ++B) {
      if (B <= (int)w) {
        const int R = 128 - 32 * B, YO = B == 0 ? 0 : B == 1 ? 8448 : B == 2 ? 14848 : 19200;
        const u32 tp = 8 * tid - 512 * B, a = tp / R;
        lptr yp = L + L_Y + YO + a * 16 + tp * 4;
#pragma unroll
        for (int c = 0; c < 2; ++c) {
          const f4 u = *(LAS f4*)(yp + c * Y_CH), v = *(LAS f4*)(yp + c * Y_CH + 16);
          y[c][0] += f2{u.x, u.y}; y[c][1] += f2{u.z, u.w}; y[c][2] += f2{v.x, v.y}; y[c][3] += f2{v.z, v.w};
        }
      }
    }
    v4u o0, o1;
    const float ga0 = bflo(gav), ga1 = bfhi(gav);
#pragma unroll
    for (int k = 0; k < 4; ++k) {
      o0[k] = san(pkbf(f2{bflo(xv[2 * k]), bflo(xv[2 * k + 1])} + y[0][k] * ga0));
      o1[k] = san(pkbf(f2{bfhi(xv[2 * k]), bfhi(xv[2 * k + 1])} + y[1][k] * ga1));
    }
    st128(rs, x1w, o0);
    st128(rs, x1w + 128, o1);
    const int pco = WS_PB + ((pi * 4 + w) * 44 + blk0) * 512 + lane * 16;
    st128(rs, pco, SENT);
    st128(rs, pco + 1024, SENT);
    st128(rs, 128 + lane < 32 * nb ? pco + 2048 : OOB, SENT);
  }
  CFENCE();
  STAMP(7);
  const u32 xw = 2 * (w & 1) + (w >> 1), blkS = 4 + (w >> 1);
  const bool hasS = blkS < nb;
  const u32 fo[4] = {((pj * 2 + 1) * 6 + w), ((pj * 2 + 1) * 6 + (hasS ? blkS : 4)), (pj * 12 + w), (pj * 12 + (hasS ? blkS : 4))};
  v4u ones = {0x3f803f80u, 0x3f803f80u, 0x3f803f80u, 0x3f803f80u};
  pin(ones);
  v4u wf[4][16];
  auto ldf = [&](int s, int k) { wf[s][k] = ld128s(rs, WS_WINP + lane * 16 + k * 1024, fo[s] * 16384); };
  f4 sW[4];
  {
#pragma unroll
    for (int k = 0; k < 16; ++k) { ldf(0, k); ldf(1, k); }
    CFENCE();
    v4u xr[16];
    const int xo = WS_X1 + pi * 65536 + w * 16384 + lane * 16;
#pragma unroll
    for (int k = 0; k < 16; ++k) xr[k] = ld128(rs, xo + k * 1024);
    CFENCE();
    __syncthreads();
    MM_HEAD();
#pragma unroll
    for (int k = 0; k < 16; ++k) {
      if (k) { m_av(sW[0], wf[0][k], ones); m_av(sW[1], wf[1][k], ones); } else { m_av0(sW[0], wf[0][k], ones); m_av0(sW[1], wf[1][k], ones); }
    }
    MM_TAIL();
    STAMP(8);
#pragma nounroll
    for (int sp = 0; sp < SPIN_CAP; ++sp) {
      u32 m = 0;
#pragma unroll
      for (int k = 0; k < 16; ++k) m = vmax(m, xr[k]);
      if (!__any(m == ~0u)) break;
      asm volatile("" ::: "memory");
#pragma unroll
      for (int k = 0; k < 16; ++k) xr[k] = ld128(rs, xo + k * 1024);
      CNT(29, 100);
    }
    STAMP(9);
    const u32 off = (lane >> 3) * 128 + (lane & 1) * 16;
#pragma unroll
    for (int k = 0; k < 16; ++k)
      *(LAS v4u*)(L + L_XIMG + (16 * w + k) * 1024 + off + ((((lane & 7) >> 1) ^ (((lane >> 4) & 1) | ((k & 1) << 1))) * 32)) = xr[k];
  }
  __syncthreads();
  STAMP(10);
  const u32 xsw = ((c16 >> 3) & 1) | ((q4 & 1) << 1), xbo = (8 * q4 + (c16 >> 2)) * 128 + 8 * (c16 & 3);
  lptr xb[4];
#pragma unroll
  for (int t = 0; t < 4; ++t) xb[t] = L + xbo + ((t ^ xw ^ xsw) * 32);
  v4u wsf[16][4];
  const int wso = WS_WSPP + ((pi * 64 + 16 * w) * 4) * 1024 + lane * 16;
  auto ldw = [&](int n) { wsf[n >> 2][n & 3] = ld128s(rs, wso + (n >> 2) * 4096, ((n & 3) ^ xw) * 1024); };
  f4 bW[4];
#pragma unroll
  for (int s = 0; s < 4; ++s) bW[s] = __builtin_bit_cast(f4, ld128s(rs, WS_BWP + c16 * 64 + q4 * 16, fo[s] * 1024));
  v2u bl[2][4], bh[2][4];
  auto ldx = [&](int k, int i) {
    if (k < 16) { if (i & 1) bh[k & 1][i >> 1] = trread(xb[i >> 1] + 4096 * k + 512); else bl[k & 1][i >> 1] = trread(xb[i >> 1] + 4096 * k); }
  };
  auto bx = [&](int k, int t) { return v4u{bl[k & 1][t].x, bl[k & 1][t].y, bh[k & 1][t].x, bh[k & 1][t].y}; };
  f4 z2a[6], st0, st1;
  {
#pragma unroll
    for (int i = 0; i < 8; ++i) ldx(0, i);
    MM_HEAD();
#pragma unroll
    for (int k = 0; k < 16; ++k)
#pragma unroll
      for (int i = 0; i < 8; ++i) {
        const int u = i == 2 ? 0 : i == 3 ? 4 : i == 4 ? 1 : i == 5 ? 5 : i - 4;
        if (i == 0) { if (k) m_vv(st0, ones, bx(k, 0)); else m_vv0(st0, ones, bx(k, 0)); }
        else if (i == 1) { if (k) m_vv(st1, bx(k, 0), bx(k, 0)); else m_vv0(st1, bx(k, 0), bx(k, 0)); }
        else { if (k) m_av(z2a[u], wf[u >> 2][k], bx(k, u & 3)); else m_av0(z2a[u], wf[u >> 2][k], bx(k, u & 3)); }
        ldx(k + 1, i);
        if (i == 1) ldf(2, k);
        if (i == 5) ldf(3, k);
        SB();
      }
    MM_TAIL();
  }
  STAMP(24);
  if (q4 == (c16 >> 2)) {
    const float ss = (c16 & 2) ? ((c16 & 1) ? st1.w : st1.z) : ((c16 & 1) ? st1.y : st1.x);
    const float m = st0.x * (1.f / 512), var = fmaxf(ss * (1.f / 512) - m * m, 0.f);
    const float r = __builtin_amdgcn_rcpf(ln_den(var));
    *(LAS f2*)(L + L_XST + (16 * xw + c16) * 8) = f2{r, -rbf(m) * r};
  }
#pragma unroll
  for (int s = 0; s < 2; ++s)
#pragma unroll
    for (int e = 0; e < 4; ++e) bW[s][e] = rowsum(bW[s][e]);
  __syncthreads();
  f2 rn[4];
#pragma unroll
  for (int t = 0; t < 4; ++t) rn[t] = *(LAS f2*)(L + L_XST + (16 * (t ^ xw) + c16) * 8);
  auto epi = [&](f4& a, int s, int t) {
    const f2 lo = gelu2(f2{a.x, a.y} * rn[t].x + (f2{sW[s].x, sW[s].y} * rn[t].y + f2{bW[s].x, bW[s].y}));
    const f2 hi = gelu2(f2{a.z, a.w} * rn[t].x + (f2{sW[s].z, sW[s].w} * rn[t].y + f2{bW[s].z, bW[s].w}));
    a = f4{lo.x, lo.y, hi.x, hi.y};
    pin(a);
  };
  {
    f2 zs[4], zq[4];
#pragma unroll
    for (int i = 0; i < 6; ++i) {
      epi(z2a[i], i >> 2, i & 3);
      ldw(2 * i); ldw(2 * i + 1);
      f2 lo = {z2a[i].x, z2a[i].y}, hi = {z2a[i].z, z2a[i].w};
      if (i < 4) { zs[i] = lo + hi; zq[i] = lo * lo + hi * hi; }
      else if (hasS) { zs[i & 3] += lo + hi; zq[i & 3] += lo * lo + hi * hi; }
      SB();
    }
    float u[4];
#pragma unroll
    for (int t = 0; t < 4; ++t) u[t] = sw32sum(zs[t].x + zs[t].y, zq[t].x + zq[t].y);
    LAS float* zp = (LAS float*)(L + L_ZST) + (w * 2 + (lane >> 5)) * 64 + c16;
    zp[16 * ((2 * (q4 & 1)) ^ xw)] = sw16sum(u[0], u[2]);
    zp[16 * ((2 * (q4 & 1) + 1) ^ xw)] = sw16sum(u[1], u[3]);
  }
  __syncthreads();
  if (w == 0) {
    LAS float* zp = (LAS float*)(L + L_ZST) + lane;
    const float s = (zp[0] + zp[128]) + (zp[256] + zp[384]), ss = (zp[64] + zp[192]) + (zp[320] + zp[448]);
    st128(rs, WS_ST2 + ((pi * 8 + pj) * 64 + lane) * 16, v4u{__float_as_uint(s), tag, __float_as_uint(ss), tag});
  }
  CFENCE();
  STAMP(11);
  f4 z1a[6];
  v2u xres[4];
  v4u g[8];
  auto s2b_load = [&]() {
#pragma unroll
    for (int n = 0; n < 8; ++n) g[n] = ld128s(rs, WS_ST2 + ((pi * 8 + 2 * q4 + (n & 1)) * 64 + c16) * 16, ((n >> 1) ^ xw) * 256);
  };
  {
#pragma unroll
    for (int i = 0; i < 8; ++i) ldx(0, i);
    MM_HEAD();
#pragma unroll
    for (int k = 0; k < 16; ++k)
#pragma unroll
      for (int i = 0; i < 8; ++i) {
        const int u = i == 2 ? 0 : i == 3 ? 4 : i == 4 ? 1 : i == 5 ? 5 : i - 4;
        if (i < 2) { if (k) m_av(sW[2 + i], wf[2 + i][k], ones); else m_av0(sW[2 + i], wf[2 + i][k], ones); }
        else { if (k) m_av(z1a[u], wf[2 + (u >> 2)][k], bx(k, u & 3)); else m_av0(z1a[u], wf[2 + (u >> 2)][k], bx(k, u & 3)); }
        ldx(k + 1, i);
        if (k < 12 && i == 1) ldw(12 + 2 * k);
        if (k < 12 && i == 5) ldw(13 + 2 * k);
        SB();
      }
    s2b_load();
    MM_TAIL();
#pragma unroll
    for (int n = 0; n < 4; ++n) {
      const u32 row = 16 * n + 4 * q4 + (c16 >> 2);
      xres[n] = trread(L + L_XIMG + pj * 8192 + row * 128 + ((w ^ (((row >> 1) & 1) | (((row >> 3) & 1) << 1))) * 32) + 8 * (c16 & 3));
    }
#pragma unroll
    for (int s = 2; s < 4; ++s)
#pragma unroll
      for (int e = 0; e < 4; ++e) bW[s][e] = rowsum(bW[s][e]);
  }
  STAMP(12);
  {
#pragma nounroll
    for (int sp = 0; sp < SPIN_CAP; ++sp) {
      u32 bad = 0;
#pragma unroll
      for (int n = 0; n < 8; ++n) bad |= (g[n].y ^ tag) | (g[n].w ^ tag);
      if (!__any(bad != 0)) break;
      asm volatile("" ::: "memory");
      s2b_load();
      CNT(29, 10000);
    }
    STAMP(13);
    lptr tw = L + L_TRS + w * TRS_W;
    float u[4];
#pragma unroll
    for (int t = 0; t < 4; ++t)
      u[t] = sw32sum(__uint_as_float(g[2 * t].x) + __uint_as_float(g[2 * t + 1].x), __uint_as_float(g[2 * t].z) + __uint_as_float(g[2 * t + 1].z));
#pragma unroll
    for (int t = 0; t < 4; ++t) {
      const float v = sw16sum(u[t], u[t]);
      auto e = __builtin_amdgcn_permlane32_swap(__float_as_uint(v), __float_as_uint(v), false, false);
      const float m = __uint_as_float(e[0]) * (1.f / 688), var = fmaxf(__uint_as_float(e[1]) * (1.f / 688) - m * m, 0.f);
      const float r = __builtin_amdgcn_rcpf(ln_den(var)), n = -rbf(m) * r;
      lptr p = tw + (16 * (t ^ xw) + c16) * TRS_P + 8 * q4;
      { const f4 z = z2a[t] * r + n; *(LAS v2u*)p = v2u{pkbf(z.x, z.y), pkbf(z.z, z.w)}; }
      if (t < 2) { const f4 z = z2a[4 + t] * r + n; *(LAS v2u*)(p + 32) = v2u{pkbf(z.x, z.y), pkbf(z.z, z.w)}; }
    }
    lptr p = tw + (8 * q4 + (c16 >> 2)) * TRS_P + 8 * (c16 & 3);
    const int zo = WS_Z2N + (pj * 6 * 64 + 2 * pi) * 1024 + lane * 16;
#pragma unroll
    for (int n = 0; n < 3; ++n) {
      lptr pp = n < 2 ? p + 32 * n * TRS_P : p + 32 * (w & 1) * TRS_P + 32;
      const v2u lo = trread(pp), hi = trread(pp + 4 * TRS_P);
      st128s(rs, zo, n < 2 ? w * 65536 + n * 1024 : (hasS ? blkS * 65536 + (w & 1) * 1024 : OOB), san4(v4u{lo.x, lo.y, hi.x, hi.y}));
    }
  }
  CFENCE();
  STAMP(14);
  const char* wsrc = (const char*)wout + ((8 * w + (lane >> 3)) * 1024 + 128 * pj + 16 * ((lane & 7) ^ ((((lane >> 4) & 1) | ((w & 1) << 1)) << 1)));
  u32 bsr[4];
  v4u gg[2];
#pragma unroll
  for (int t = 0; t < 4; ++t) bsr[t] = gld<u16>(bsp, (64 * pi + 16 * (t ^ xw) + c16) * 2);
#pragma unroll
  for (int m = 0; m < 2; ++m) gg[m] = ld128(rs, WS_MOD + ((lane & 3) * 1536 + 1280 + 32 * pj + 16 * m + 8 * (c16 >> 3) + 2 * q4 + ((c16 >> 2) & 1)) * 16);
  v2u z1[6];
#pragma unroll
  for (int i = 0; i < 6; ++i) {
    epi(z1a[i], 2 + (i >> 2), i & 3);
    z1[i] = v2u{pkbf(z1a[i].x, z1a[i].y), pkbf(z1a[i].z, z1a[i].w)};
    pin(z1[i]);
#pragma unroll
    for (int n = 0; n < 5; ++n) if (36 + 5 * i + n < 64) ldw(36 + 5 * i + n);
    SB();
  }
  for (u32 n = 0; n < 19; ++n) glds16(wsrc + n * 32768, (u32)(size_t)L + L_WO + (4 * n + w) * 1024);
  u32 gbp[2];
#pragma unroll
  for (int m = 0; m < 2; ++m) { gbp[m] = pkbf(quadsum(__uint_as_float(gg[m].x)), quadsum(__uint_as_float(gg[m].z))); pin(gbp[m]); }
  __syncthreads();
  STAMP(25);
  auto red_slot = [&](u32 blk, u32 snd) { return (blk < 4 ? blk * 3 + ((snd - blk - 1) & 3) : 4 * blk - 4 + snd) * 4096; };
  f4 ga[2][4];
#pragma unroll
  for (int t = 0; t < 4; ++t) ga[1][t] = f4{0.f, 0.f, 0.f, 0.f};
  {
    const int zo = WS_Z2N + (pj * 6 * 64 + 16 * w) * 1024 + lane * 16;
    const u32 rot = (pi & 3) == 3 ? 1 : 2 * (pi & 3) - (((pi & 3) == 2 && nb == 5) ? 1 : 0);
    auto blk_of = [&](u32 it) {
      u32 f = it + nb - 6 + rot;
      f = f >= nb - 1 ? f - (nb - 1) : f;
      u32 v = it == 5 ? w : w + 1 + f;
      return v >= nb ? v - nb : v;
    };
    auto soff = [&](u32 it) { return (u32)__builtin_amdgcn_readfirstlane((it < 6 && it + nb >= 6) ? blk_of(it) * 65536 : (u32)OOB); };
    v4u zr[2][16];
#pragma nounroll
    for (int sp = 0; sp < SPIN_CAP; ++sp) {
      u32 m = 0;
#pragma unroll
      for (int h = 0; h < 2; ++h)
#pragma unroll
        for (int k = 0; k < 16; ++k) zr[h][k] = ld128s(rs, zo + k * 1024, soff(h));
      if (nb == 6) {
#pragma unroll
        for (int k = 0; k < 16; ++k) m = vmax(m, zr[0][k]);
      } else {
#pragma unroll
        for (int k = 0; k < 16; ++k) m = vmax(m, zr[1][k]);
      }
      asm volatile("" ::: "memory");
      if (!__any(m == ~0u)) break;
      CNT(28, 100);
    }
    STAMP(15);
    for (u32 it2 = 0; it2 < 6; it2 += 2) {
#pragma unroll
      for (int h = 0; h < 2; ++h) {
        const u32 it = it2 + h, so = soff(it + 2);
        lptr rp = L + L_RED + red_slot(blk_of(it + nb > 6 ? it - 1 : 1u), w) + lane * 16;
        MM_HEAD();
#pragma unroll
        for (int k = 0; k < 16; ++k) {
#pragma unroll
          for (int t = 0; t < 4; ++t) { if (k) m_va(ga[h][t], zr[h][k], wsf[k][t]); else m_va0(ga[h][t], zr[h][k], wsf[k][t]); }
          zr[h][k] = ld128s(rs, zo + k * 1024, so);
          if (k >= 2 && k < 6) *(LAS f4*)(rp + (((k - 2) ^ xw) * 1024)) = ga[h ^ 1][k - 2];
          SB();
        }
        MM_TAIL();
        const float t = (ga[h][0].x + ga[h][0].y) + (ga[h][0].z + ga[h][0].w);
        if (__any(t != t) && it + nb >= 6) {
          CNT(28, 1);
#pragma unroll
          for (int k4 = 0; k4 < 16; k4 += 4) {
            v4u zz[4];
#pragma nounroll
            for (int sp = 0; sp < SPIN_CAP; ++sp) {
              u32 m = 0;
#pragma unroll
              for (int k = 0; k < 4; ++k) zz[k] = ld128s(rs, zo + (k4 + k) * 1024, soff(it));
#pragma unroll
              for (int k = 0; k < 4; ++k) m = vmax(m, zz[k]);
              asm volatile("" ::: "memory");
              if (!__any(m == ~0u)) break;
            }
            MM_HEAD();
#pragma unroll
            for (int k = 0; k < 4; ++k)
#pragma unroll
              for (int t = 0; t < 4; ++t) { if (k4 + k) m_va(ga[h][t], zz[k], wsf[k4 + k][t]); else m_va0(ga[h][t], zz[k], wsf[k4 + k][t]); }
          }
          MM_TAIL();
        }
      }
    }
  }
  STAMP(26);
  __syncthreads();
  STAMP(16);
  {
    f4 gq[6];
#pragma unroll
    for (int i = 0; i < 6; ++i) {
      const u32 tt = (i & 3) ^ xw;
      if (i < 4) {
        gq[i] = ga[1][i];
#pragma unroll
        for (int s = 1; s < 4; ++s) gq[i] += *(LAS f4*)(L + L_RED + ((w * 3 + s - 1) * 4 + tt) * 1024 + lane * 16);
      } else {
        lptr p = L + L_RED + ((blkS * 4 - 4) * 4 + tt) * 1024 + lane * 16;
        gq[i] = (*(LAS f4*)p + *(LAS f4*)(p + 4096)) + (*(LAS f4*)(p + 8192) + *(LAS f4*)(p + 12288));
      }
    }
    v2u P[6];
#pragma unroll
    for (int i = 0; i < 6; ++i) {
      const float bs = bflo(bsr[i & 3]);
      P[i] = v2u{pkbf(bf2f(z1[i].x) * (f2{gq[i].x, gq[i].y} + bs)), pkbf(bf2f(z1[i].y) * (f2{gq[i].z, gq[i].w} + bs))};
    }
#pragma unroll
    for (int h = 0; h < 3; ++h) {
      auto rx = __builtin_amdgcn_permlane16_swap(P[2 * h].x, P[2 * h + 1].x, false, false);
      auto ry = __builtin_amdgcn_permlane16_swap(P[2 * h].y, P[2 * h + 1].y, false, false);
      const u32 tt = (2 * (h & 1) + (q4 & 1)) ^ xw;
      st128s(rs, WS_PB + ((pi * 4 + tt) * 44 + blk0) * 512 + (q4 >> 1) * 256 + c16 * 16, h < 2 ? w * 512 : (hasS ? blkS * 512 : OOB),
             san4(v4u{rx[0], ry[0], rx[1], ry[1]}));
    }
  }
  STAMP(17);
  __syncthreads();
  glds16(wsrc + 19 * 32768, (u32)(size_t)L + L_WO2 + w * 1024);
  glds16(wsrc + 20 * 32768, (u32)(size_t)L + L_WO2 + (4 + w) * 1024);
  if (w < 2) glds16(wsrc + 21 * 32768, (u32)(size_t)L + L_WO2 + (8 + w) * 1024);
  else *(LAS v4u*)(L + L_WO2 + (8 + w) * 1024 + lane * 16) = v4u{0, 0, 0, 0};
  {
    const int po = WS_PB + (pi * 4 + w) * 44 * 512 + lane * 16;
    v4u pr[22];
    auto ldp = [&]() {
#pragma unroll
      for (int k = 0; k < 22; ++k) pr[k] = ld128(rs, (k == 21 && lane >= 32) ? OOB : po + k * 1024);
    };
    v4u wo[22][4];
    auto ldo = [&](int k) {
#pragma unroll
      for (int n = 0; n < 4; ++n) {
        lptr p = L + (k < 19 ? L_WO + 4096 * k : L_WO2 + 4096 * (k - 19)) + xbo + ((n ^ xsw) * 32);
        const v2u lo = trread(p), hi = trread(p + 512);
        wo[k][n] = v4u{lo.x, lo.y, hi.x, hi.y};
      }
    };
#pragma unroll
    for (int k = 0; k < 12; ++k) ldo(k);
    DRAIN();
    STAMP(18);
    ldp();
    CFENCE();
#pragma unroll
    for (int k = 12; k < 19; ++k) ldo(k);
    __syncthreads();
#pragma unroll
    for (int k = 19; k < 22; ++k) ldo(k);
    CFENCE();
    f4 o[4];
#pragma nounroll
    for (int sp = 0; sp < SPIN_CAP; ++sp) {
      MM_HEAD();
#pragma unroll
      for (int k = 0; k < 22; ++k)
#pragma unroll
        for (int n = 0; n < 4; ++n) {
          if (k == 0) m_av0(o[n], wo[k][n], pr[k]); else if (k < 16) m_av(o[n], wo[k][n], pr[k]); else m_vv(o[n], wo[k][n], pr[k]);
        }
      MM_TAIL();
      if (!__any(o[0].x != o[0].x)) break;
      u32 m = 0;
#pragma unroll
      for (int k = 0; k < 22; ++k) m = vmax(m, pr[k]);
      if (!__any(m == ~0u)) break;
      asm volatile("" ::: "memory");
      ldp();
      CNT(28, 10000);
    }
    STAMP(19);
    v2u O[4];
#pragma unroll
    for (int n = 0; n < 4; ++n) {
      const u32 g0 = (n & 1) ? dppu<0x158>(gbp[n >> 1]) : dppu<0x150>(gbp[n >> 1]), g1 = (n & 1) ? dppu<0x15C>(gbp[n >> 1]) : dppu<0x154>(gbp[n >> 1]);
      O[n] = v2u{pkbf(bf2f(xres[n].x) + bf2f(g0) * f2{o[n].x, o[n].y}), pkbf(bf2f(xres[n].y) + bf2f(g1) * f2{o[n].z, o[n].w})};
    }
#pragma unroll
    for (int h = 0; h < 2; ++h) {
      auto rx = __builtin_amdgcn_permlane16_swap(O[2 * h].x, O[2 * h + 1].x, false, false);
      auto ry = __builtin_amdgcn_permlane16_swap(O[2 * h].y, O[2 * h + 1].y, false, false);
      *(v4u*)(out + (64 * pi + 16 * w + c16) * 512 + 64 * pj + 16 * (2 * h + (q4 & 1)) + 8 * (q4 >> 1)) = v4u{rx[0], ry[0], rx[1], ry[1]};
    }
  }
  if (b == 0 && tid == 0) __builtin_amdgcn_raw_buffer_store_b32(tag, rs, WS_E, 0, SC1);
  STAMP(20);
  STAMP_FLUSH();
}

extern "C" void kernel_launch(void* const* d_in, const int* in_sizes, int n_in, void* d_out, int out_size, void* d_ws, size_t ws_size,
                              hipStream_t stream) {
  (void)in_sizes; (void)n_in; (void)out_size; (void)ws_size;
  (void)hipFuncSetAttribute((const void*)fused_kernel, hipFuncAttributeMaxDynamicSharedMemorySize, LDS_BYTES);
  fused_kernel<<<256, 256, LDS_BYTES, stream>>>((const u16*)d_in[0], (const u16*)d_in[1], (const u16*)d_in[2], (const u16*)d_in[3],
                                                (const u16*)d_in[4], (const u16*)d_in[5], (const u16*)d_in[6], (const u16*)d_in[8],
                                                (const u16*)d_in[9], (const u16*)d_in[10], (const u16*)d_in[11], (const u16*)d_in[12],
                                                (const u16*)d_in[13], (u16*)d_out, (unsigned char*)d_ws);
}
```

```cpp
#include <hip/hip_runtime.h>
#include <stdint.h>

typedef unsigned u32;
typedef unsigned short u16;
typedef unsigned long long u64;
typedef u32 v4u __attribute__((ext_vector_type(4)));
typedef u32 v2u __attribute__((ext_vector_type(2)));
typedef float f4 __attribute__((ext_vector_type(4)));
typedef float f2 __attribute__((ext_vector_type(2)));
typedef __bf16 bf8 __attribute__((ext_vector_type(8)));
typedef __bf16 bf2 __attribute__((ext_vector_type(2)));
typedef _Float16 h8 __attribute__((ext_vector_type(8)));
typedef _Float16 h2 __attribute__((ext_vector_type(2)));
typedef short s4 __attribute__((ext_vector_type(4)));
#define LAS __attribute__((address_space(3)))
#define GAS __attribute__((address_space(1)))
#define CAS __attribute__((address_space(4)))
#define DI __device__ __forceinline__
typedef __amdgpu_buffer_rsrc_t rsrc_t;
typedef LAS unsigned char* lptr;

#define XMAP 2

constexpr int WS_E = 0, WS_STAT1 = 4096, WS_CD = 32768, WS_ST2 = 65536, WS_MOD = 524288, WS_BWP = 655360;
constexpr int WS_XG = 1 << 20, WS_X1 = 5 << 20, WS_Z2N = 7 << 20, WS_PB = 10 << 20, WS_WINP = 13 << 20;
constexpr int WS_DBG = 24 << 20, WS_END = 25 << 20;
constexpr int OOB = 0x40000000;
constexpr int TABP = 544, TAB_CH = 63 * TABP;
constexpr int L_TAB = 0, L_Y = 0, Y_CH = 21504;
constexpr int L_SLB = 43008, L_SLA = 75776;
constexpr int L_PKI = 68544;
constexpr int L_TOE = 108544, TOE_C = 5312, TOE_CH = 4 * TOE_C;
constexpr int L_U = 151040, U_CH = 6320;
constexpr int L_XIMG = 0, L_TRS = 65536, TRS_W = 4096;
constexpr int L_RED = 0, L_WO = 81920, L_WO2 = 0;
constexpr int L_XST = 159744, L_ZST = L_XST + 1024;
constexpr int LDS_BYTES = 163840;
constexpr int SC1 = 16;
constexpr int SPIN_CAP = 1 << 15;
static_assert(LDS_BYTES <= 163840, "LDS");

DI float bflo(u32 v) { return __uint_as_float(v << 16); }
DI float bfhi(u32 v) { return __uint_as_float(v & 0xffff0000u); }
DI f2 bf2f(u32 v) { return f2{bflo(v), bfhi(v)}; }
DI u32 pkbf(float a, float b) { f2 v = {a, b}; return __builtin_bit_cast(u32, __builtin_convertvector(v, bf2)); }
DI u32 pkbf(f2 v) { return __builtin_bit_cast(u32, __builtin_convertvector(v, bf2)); }
DI u32 pkh(f2 v) { return __builtin_bit_cast(u32, __builtin_convertvector(v, h2)); }
DI float rbf(float a) { return bflo(pkbf(a, a)); }
DI u32 san(u32 v) { return v < 0xFFFFFFFEu ? v : 0xFFFFFFFEu; }
DI v4u san4(v4u v) { return v4u{san(v.x), san(v.y), san(v.z), san(v.w)}; }
DI u32 umax3(u32 a, u32 b, u32 c) { return max(max(a, b), c); }
DI u32 vmax(u32 m, v4u v) { return umax3(umax3(m, v.x, v.y), v.z, v.w); }
DI v4u ld128(rsrc_t rs, int off) { return __builtin_amdgcn_raw_buffer_load_b128(rs, off, 0, SC1); }
DI v4u ld128s(rsrc_t rs, int off, int soff) { return __builtin_amdgcn_raw_buffer_load_b128(rs, off, soff, SC1); }
DI void st128(rsrc_t rs, int off, v4u v) { __builtin_amdgcn_raw_buffer_store_b128(v, rs, off, 0, SC1); }
DI void st128s(rsrc_t rs, int off, int soff, v4u v) { __builtin_amdgcn_raw_buffer_store_b128(v, rs, off, soff, SC1); }
DI void st64(rsrc_t rs, int off, v2u v) { __builtin_amdgcn_raw_buffer_store_b64(v, rs, off, 0, SC1); }
#define DRAIN() asm volatile("s_waitcnt vmcnt(0)" ::: "memory")
#define SB() __builtin_amdgcn_sched_barrier(0)
#define CFENCE() do { asm volatile("" ::: "memory"); SB(); } while (0)
template <int C> DI u32 dppu(u32 v) { return (u32)__builtin_amdgcn_update_dpp(0, (int)v, C, 0xF, 0xF, true); }
template <int C> DI float dppadd(float v) { return v + __uint_as_float(dppu<C>(__float_as_uint(v))); }
DI float sw16sum(float a, float b) { auto r = __builtin_amdgcn_permlane16_swap(__float_as_uint(a), __float_as_uint(b), false, false); return __uint_as_float(r[0]) + __uint_as_float(r[1]); }
DI float sw32sum(float a, float b) { auto r = __builtin_amdgcn_permlane32_swap(__float_as_uint(a), __float_as_uint(b), false, false); return __uint_as_float(r[0]) + __uint_as_float(r[1]); }
DI float quadsum(float v) { v = dppadd<0xB1>(v); return dppadd<0x4E>(v); }
DI float rowsum(float v) { v = quadsum(v); v = dppadd<0x141>(v); return dppadd<0x140>(v); }
DI f4 mfma_bf(v4u a, v4u b, f4 c) { return __builtin_amdgcn_mfma_f32_16x16x32_bf16(__builtin_bit_cast(bf8, a), __builtin_bit_cast(bf8, b), c, 0, 0, 0); }
DI f4 mfma_h(v4u a, v4u b, f4 c) { return __builtin_amdgcn_mfma_f32_16x16x32_f16(__builtin_bit_cast(h8, a), __builtin_bit_cast(h8, b), c, 0, 0, 0); }
DI v2u trread(lptr p) { return __builtin_bit_cast(v2u, __builtin_amdgcn_ds_read_tr16_b64_v4i16((LAS s4*)p)); }
#define FORU(i, n) _Pragma("unroll") for (int i = 0; i < (n); ++i)
DI f2 cm1(f2 a, f2 b) { f2 t; asm("v_pk_mul_f32 %0, %1, %2 op_sel:[0,0] op_sel_hi:[0,1]" : "=v"(t) : "v"(a), "v"(b)); return t; }
DI f2 cm2(f2 a, f2 b, f2 t) { f2 r; asm("v_pk_fma_f32 %0, %1, %2, %3 op_sel:[1,1,0] op_sel_hi:[1,0,1] neg_lo:[0,1,0]" : "=v"(r) : "v"(a), "v"(b), "v"(t)); return r; }
DI f2 cexp_p(float al2, float rev, float p) {
  float m = __builtin_amdgcn_exp2f(al2 * p), f = __builtin_amdgcn_fractf(rev * p);
  return f2{m * __builtin_amdgcn_cosf(f), m * __builtin_amdgcn_sinf(f)};
}
template <int N> DI void gelu_n(f2 (&v)[N]) {
  const float c0 = -2.0f * 0.7978845608028654f * 1.4426950408889634f, c1 = c0 * 0.044715f;
  f2 a[N];
  FORU(i, N) a[i] = v[i] * v[i];
  SB();
  FORU(i, N) a[i] = __builtin_elementwise_fma(a[i], f2{c1, c1}, f2{c0, c0});
  SB();
  FORU(i, N) a[i] *= v[i];
  SB();
  FORU(i, N) a[i] = f2{__builtin_amdgcn_exp2f(a[i].x), __builtin_amdgcn_exp2f(a[i].y)};
  SB();
  FORU(i, N) a[i] += 1.0f;
  SB();
  FORU(i, N) a[i] = f2{__builtin_amdgcn_rcpf(a[i].x), __builtin_amdgcn_rcpf(a[i].y)};
  SB();
  FORU(i, N) v[i] *= a[i];
  SB();
}
DI float ln_den(float var) { return rbf(__builtin_amdgcn_sqrtf(rbf(rbf(var) + 1e-5f))); }
template <class T> DI T gld(const void* base, u32 off) { return *(const GAS T*)((const GAS char*)base + off); }
template <int LO, int SO> DI void dma16(rsrc_t r, u32 voff, u32 sb, u32 lb) {
  u32 t;
  asm volatile("s_add_u32 m0, %1, %5\n\ts_add_u32 %0, %2, %6\n\tbuffer_load_dwordx4 %3, %4, %0 offen lds"
               : "=&s"(t) : "s"(lb), "s"(sb), "v"(voff), "s"(r), "i"(LO), "i"(SO) : "memory", "scc");
}
template <int N> struct IC { static constexpr int v = N; };
template <int I, int N, class F> DI void sfor(F f) { if constexpr (I < N) { f(IC<I>{}); sfor<I + 1, N>(f); } }
#define MF "v_mfma_f32_16x16x32_bf16 "
DI void m_av(f4& c, v4u a, v4u b) { asm volatile(MF "%0, %1, %2, %0" : "+v"(c) : "a"(a), "v"(b)); }
DI void m_va(f4& c, v4u a, v4u b) { asm volatile(MF "%0, %1, %2, %0" : "+v"(c) : "v"(a), "a"(b)); }
DI void m_vv(f4& c, v4u a, v4u b) { asm volatile(MF "%0, %1, %2, %0" : "+v"(c) : "v"(a), "v"(b)); }
DI void m_av0(f4& c, v4u a, v4u b) { asm volatile(MF "%0, %1, %2, 0" : "=&v"(c) : "a"(a), "v"(b)); }
DI void m_va0(f4& c, v4u a, v4u b) { asm volatile(MF "%0, %1, %2, 0" : "=&v"(c) : "v"(a), "a"(b)); }
DI void m_vv0(f4& c, v4u a, v4u b) { asm volatile(MF "%0, %1, %2, 0" : "=&v"(c) : "v"(a), "v"(b)); }
template <class T> DI void pin(T& v) { asm volatile("" : "+v"(v)); }
#define MM_HEAD() do { SB(); asm volatile("s_nop 7"); SB(); } while (0)
#define MM_TAIL() do { SB(); asm volatile("s_nop 7"); SB(); } while (0)
DI int toe_addr(int pos) { return 2 * pos + 8 * (pos >> 4); }

#define STAMP(n)
#define STAMP_FLUSH()
#define STAMP_INIT()
#define CNT(n,v)

template <int BAND> struct Band {
  static constexpr int R = 128 - 32 * BAND, NT = R / 16, NS = 15 + NT / 2;
  static constexpr int YO = BAND == 0 ? 0 : BAND == 1 ? 8448 : BAND == 2 ? 14848 : 19200;
  lptr al, ah, bb, yp;
  f4 acc[NT];
  v2u lo[32], hi[32];
  v4u bf[NS];
  DI void init(lptr L, int ch, int lane) {
    const int r = lane & 15, q = lane >> 4, c = (3 - r) & 3;
    const int P = 2048 - 512 * BAND - 496 - 4 * (r >> 2) + 8 * q;
    al = L + L_TOE + ch * TOE_CH + c * TOE_C + toe_addr(P);
    ah = L + L_TOE + ch * TOE_CH + c * TOE_C + toe_addr(P + 4);
    bb = L + L_U + ch * U_CH + 80 * (R / 32) * r + 16 * q;
    yp = L + L_Y + ch * Y_CH + YO + r * (4 * R + 16) + 16 * q;
  }
  DI void load(int s, int i) {
    if (i < 4) {
      const int n = 2 * s + (i >> 1);
      if (n < 32) { if (i & 1) hi[n] = *(LAS v2u*)(ah + 40 * (31 - n)); else lo[n] = *(LAS v2u*)(al + 40 * (31 - n)); }
    } else if (s < NS) bf[s] = *(LAS v4u*)(bb + 80 * (NT / 2 - 1 - s + 15));
  }
  DI void mma(int s, int t) {
    const int n = t - NT + 2 + 2 * s;
    if (s < NS && n >= 0 && n < 32) {
      if (n < 2) m_vv0(acc[t], v4u{lo[n].x, lo[n].y, hi[n].x, hi[n].y}, bf[s]); else m_vv(acc[t], v4u{lo[n].x, lo[n].y, hi[n].x, hi[n].y}, bf[s]);
    }
  }
  DI void flush(int s, int t) {
    const int n = t - NT + 2 + 2 * (s - 1);
    if (n == 30 || n == 31) *(LAS f4*)(yp + 64 * t) = acc[t];
  }
};
template <int B1, int B2, class F> DI void conv2(lptr L, int ch, int lane, F side) {
  Band<B1> u; Band<B2> v;
  u.init(L, ch, lane); v.init(L, ch, lane);
  constexpr int N1 = Band<B1>::NT, NS = Band<B1>::NS;
#pragma unroll
  for (int s = 0; s < 2; ++s)
#pragma unroll
    for (int i = 0; i < 5; ++i) { u.load(s, i); v.load(s, i); SB(); }
  sfor<0, 10 * (NS + 1)>([&](auto z) {
    constexpr int s = z.v / 10, i = z.v % 10;
    if (s >= 16 && i == 0) asm volatile("s_nop 7");
    if (i < N1) { u.mma(s, i); u.flush(s, i); } else { v.mma(s, i - N1); v.flush(s, i - N1); }
    if (i < 5) u.load(s + 2, i); else v.load(s + 2, i - 5);
    side(IC<s>{}, IC<i>{});
    SB();
  });
}

__global__ void __launch_bounds__(256, 1)
fused_kernel(const u16* __restrict__ x, const u16* __restrict__ lar, const u16* __restrict__ aim, const u16* __restrict__ cre,
             const u16* __restrict__ cim, const u16* __restrict__ ldt, const u16* __restrict__ dsk, const u16* __restrict__ win,
             const u16* __restrict__ wsp, const u16* __restrict__ bsp, const u16* __restrict__ wout, const u16* __restrict__ cvec,
             const u16* __restrict__ wmod, u16* __restrict__ out, unsigned char* ws) {
  extern __shared__ __attribute__((aligned(16))) unsigned char smem[];
  lptr L = (lptr)smem;
  const u32 b = blockIdx.x, tid = threadIdx.x, lane = tid & 63;
  STAMP_INIT();
  STAMP(0);
  const u32 w = __builtin_amdgcn_readfirstlane(tid >> 6);
  const u32 xc = b & 7, rr = b >> 3, cp = xc * 32 + rr;
  const u32 c16 = lane & 15, q4 = lane >> 4;
  const u32 tag = *(const CAS u32*)(ws + WS_E) + 1u;
  const u32 tb = 512 * (b >> 6) + 32 * (b & 15) + 2 * ((b >> 4) & 3);
  const v4u sr0 = gld<v4u>(x, (tb + 8 * w) * 1024 + lane * 16), sr1 = gld<v4u>(x, (tb + 8 * w) * 1024 + 1024 + lane * 16);
  const u32 xgo = (tb + 8 * (lane & 3)) * 1024 + (16 * w + (lane >> 2)) * 16;
  const v4u xg0 = gld<v4u>(x, xgo), xg1 = gld<v4u>(x, xgo + 1024);
  const u32 mG = b % 48, mP = min(b / 48, 3u), mrow = mP * 128 + (lane >> 1);
  v4u wm[4];
  u32 cv[4];
#pragma unroll
  for (int n = 0; n < 4; ++n) wm[n] = gld<v4u>(wmod, (mrow + 32 * n) * 6144 + mG * 128 + w * 32 + (lane & 1) * 16);
#pragma unroll
  for (int n = 0; n < 4; ++n) cv[n] = gld<u16>(cvec, (mrow + 32 * n) * 2);
  const u32 pidx = (cp * 256 + tid) * 2;
  const u32 p_lar = gld<u16>(lar, pidx), p_aim = gld<u16>(aim, pidx), p_cr = gld<u16>(cre, pidx), p_ci = gld<u16>(cim, pidx);
  const u32 p_cr2 = gld<u16>(cre, pidx ^ 128), p_ci2 = gld<u16>(cim, pidx ^ 128);
  const u32 p_ldt = *(const CAS u32*)(ldt + 2 * cp), p_dsk = *(const CAS u32*)(dsk + 2 * cp);
  CFENCE();
  u32 pf0 = 0, pf1 = 0, pf2 = 0, pf3 = 0;
  {
    u64 pc;
    u32 len;
    asm volatile("s_getpc_b64 %0\nfk_text_a:\n\ts_mov_b32 %1, fk_text_b-fk_text_a" : "=s"(pc), "=s"(len));
    const u32 o = (rr * 16 + tid) * 128;
    if (tid < 16 && o < len) pf0 = gld<u32>((const void*)pc, o);
  }
  CFENCE();
  const u32 pi = (xc >> 1) * 8 + (rr & 7), pj = (xc & 1) * 4 + (rr >> 3);
  const u32 nb = pj < 3 ? 6 : 5, blk0 = 5 * pj + min(pj, 3u);
  const bool two = w + 4 < nb;
  const rsrc_t rs = __builtin_amdgcn_make_buffer_rsrc((void*)ws, 0, WS_END, 0x00020000);
  const v4u SENT = {~0u, ~0u, ~0u, ~0u};
  const int x1w = WS_X1 + ((tid >> 3) * 512 + 2 * cp) * 128 + (tid & 7) * 16;
  const u32 wio_src = ((32 * (pi >> 1) + (lane >> 1)) * 1376 + (pi & 1) * 688 + 16 * (blk0 + w)) * 2 + (lane & 1) * 16;
  const v4u wi0 = gld<v4u>(win, wio_src), wi1 = gld<v4u>(win, two ? wio_src + 128 : wio_src);
  CFENCE();
  if (tid < 40) *(LAS v4u*)(L + L_TOE + (tid / 5) * TOE_C + 5120 + (tid % 5) * 16) = v4u{0, 0, 0, 0};
  if (tid < 150) *(LAS v4u*)(L + L_U + (tid / 75) * U_CH + (tid % 75) * 16) = v4u{0, 0, 0, 0};
  STAMP(23);
  {
    f2 a[8];
#pragma unroll
    for (int e = 0; e < 4; ++e) { a[2 * e] = f2{bflo(sr0[e]), bflo(sr1[e])}; a[2 * e + 1] = f2{bfhi(sr0[e]), bfhi(sr1[e])}; }
    f2 s = ((a[0] + a[1]) + (a[2] + a[3])) + ((a[4] + a[5]) + (a[6] + a[7]));
    float t = rowsum(sw32sum(s.x, s.y));
    t = sw16sum(t, t) * (1.f / 512);
    auto mm = __builtin_amdgcn_permlane32_swap(__float_as_uint(t), __float_as_uint(t), false, false);
    const f2 m = {__uint_as_float(mm[0]), __uint_as_float(mm[1])};
    s = f2{0.f, 0.f};
#pragma unroll
    for (int e = 0; e < 8; ++e) { const f2 u = a[e] - m; s = __builtin_elementwise_fma(u, u, s); }
    float v = rowsum(sw32sum(s.x, s.y));
    v = sw16sum(v, v) * (1.f / 512);
    st64(rs, (lane & 31) == 0 ? WS_STAT1 + b * 64 + w * 16 + (lane >> 5) * 8 : OOB, v2u{pkbf(t, ln_den(v)), tag});
  }
  {
    const int o = WS_XG + (4 * (16 * w + (lane >> 2))) * 16384 + b * 64 + (lane & 3) * 16;
    st128(rs, o, v4u{xg0.x, tag, xg1.x, tag});
    st128s(rs, o, 16384, v4u{xg0.y, tag, xg1.y, tag});
    st128s(rs, o, 32768, v4u{xg0.z, tag, xg1.z, tag});
    st128s(rs, o, 49152, v4u{xg0.w, tag, xg1.w, tag});
  }
  CFENCE();
  STAMP(22);
  {
    f2 a[4];
#pragma unroll
    for (int n = 0; n < 4; ++n) {
      const float c = bflo(cv[n]);
      const float s = rbf(c * __builtin_amdgcn_rcpf(1.f + __builtin_amdgcn_exp2f(-1.4426950408889634f * c)));
#pragma unroll
      for (int e = 0; e < 4; ++e) a[e] = n ? a[e] + bf2f(wm[n][e]) * s : bf2f(wm[n][e]) * s;
    }
    const float u0 = sw32sum(a[0].x, a[2].x), u1 = sw32sum(a[0].y, a[2].y), u2 = sw32sum(a[1].x, a[3].x), u3 = sw32sum(a[1].y, a[3].y);
    float t0 = sw16sum(u0, u2), t1 = sw16sum(u1, u3);
    t0 = dppadd<0x4E>(dppadd<0x124>(dppadd<0x128>(t0)));
    t1 = dppadd<0x4E>(dppadd<0x124>(dppadd<0x128>(t1)));
    st128(rs, (c16 < 2 && b < 192) ? WS_MOD + (mP * 1536 + mG * 32 + w * 8 + c16 * 4 + q4) * 16 : OOB, v4u{__float_as_uint(t0), tag, __float_as_uint(t1), tag});
  }
  CFENCE();
  STAMP(1);
  const u32 xw = 2 * (w & 1) + (w >> 1);
  const rsrc_t rw = __builtin_amdgcn_make_buffer_rsrc((void*)wsp, 0, 2048 * 4096, 0x00020000);
  const u32 wvo = (lane >> 3) * 4096 + ((lane & 7) ^ (lane >> 3)) * 16, lbase = (u32)(size_t)L;
  v4u wsf[16][4];
  const u32 wsb[4] = {(64 * pi + 16 * xw) * 4096 + w * 1024, (64 * pi + 16 * (xw ^ 1)) * 4096 + w * 1024,
                      (64 * pi + 16 * (xw ^ 2)) * 4096 + w * 1024, (64 * pi + 16 * (xw ^ 3)) * 4096 + w * 1024};
  const u32 lw8 = lbase + w * 8192;
#define W_DMA(c, n, slot) dma16<(slot) + (n) * 1024, ((n) & 1) * 32768 + 128 * (c)>(rw, wvo, wsb[(n) >> 1], lw8)
  auto w_get = [&](int c, int i, int slot) {
    wsf[2 * c + (i & 1)][i >> 1] = *(LAS v4u*)(L + slot + w * 8192 + (i >> 1) * 2048 + c16 * 128 + (((4 * (i & 1) + q4) ^ (c16 & 7)) * 16));
  };
  const int wio = WS_WINP + (((pj * 2 + (pi & 1)) * 6 + w) * 16 + (pi >> 1)) * 1024 + lane * 16;
  *(LAS v4u*)(L + L_PKI + w * 1024 + lane * 16) = wi0;
  if (w < 2) *(LAS v4u*)(L + L_PKI + 4096 + w * 1024 + lane * 16) = wi1;
  CFENCE();
  st128(rs, x1w, SENT);
  st128(rs, x1w + 128, SENT);
  const u32 pfo = (64 * pi + 8 * pj + 2 * w + ((lane >> 4) & 1)) * 4096 + ((lane >> 2) & 3) * 1024 + (lane & 3) * 128;
  if (lane < 32) pf2 = gld<u32>(wsp, pfo);
  CFENCE();
  float kscale;
  {
    const float L2E = 1.4426950408889634f;
    const float dt = rbf(__builtin_amdgcn_exp2f(((w >> 1) ? bfhi(p_ldt) : bflo(p_ldt)) * L2E));
    const float al2 = (-rbf(__builtin_amdgcn_exp2f(bflo(p_lar) * L2E)) * dt) * L2E;
    const float rev = (bflo(p_aim) * dt) * 0.15915494309189535f;
    u32 mx = max(max(p_cr & 0x7fffu, p_ci & 0x7fffu), max(p_cr2 & 0x7fffu, p_ci2 & 0x7fffu));
    mx = max(mx, dppu<0xB1>(mx)); mx = max(mx, dppu<0x4E>(mx)); mx = max(mx, dppu<0x141>(mx)); mx = max(mx, dppu<0x140>(mx));
    { auto r = __builtin_amdgcn_permlane16_swap(mx, mx, false, false); mx = max(r[0], r[1]); }
    { auto r = __builtin_amdgcn_permlane32_swap(mx, mx, false, false); mx = max(r[0], r[1]); }
    u32 ex = mx >> 7;
    ex = min(max(ex, 1u), 253u);
    const float dn = __uint_as_float((254u - ex) << 23), up = __uint_as_float(ex << 23);
    kscale = 2.0f * dt * up;
    LAS u32* tp = (LAS u32*)(L + L_TAB + (tid >> 7) * TAB_CH + (tid & 127) * 4);
    f2 U[17], Y[16], V[8], X[31], wp[4], t[31];
    u32 pk[31];
    wp[0] = cexp_p(al2, rev, 1.f);
    Y[1] = cexp_p(al2, -rev, 16.f);
    V[1] = cexp_p(al2, rev, 256.f);
    Y[0] = f2{1.f, 0.f};
    U[0] = f2{bflo(p_cr) * dn, bflo(p_ci) * dn};
    SB();
    FORU(l, 4) {
      const int h = 1 << l;
      FORU(i, h) t[i] = cm1(U[i], wp[l]);
      if (l < 3) { t[8] = cm1(wp[l], wp[l]); t[9] = cm1(Y[h], Y[h]); }
      if (l < 2) t[10] = cm1(V[h], V[h]);
      FORU(i, h - 1) t[11 + i] = cm1(Y[1 + i], Y[h]);
      if (l < 3) { FORU(i, h - 1) t[18 + i] = cm1(V[1 + i], V[h]); }
      SB();
      FORU(i, h) U[h + i] = cm2(U[i], wp[l], t[i]);
      if (l < 3) { wp[l + 1] = cm2(wp[l], wp[l], t[8]); Y[2 * h] = cm2(Y[h], Y[h], t[9]); }
      if (l < 2) V[2 * h] = cm2(V[h], V[h], t[10]);
      FORU(i, h - 1) Y[h + 1 + i] = cm2(Y[1 + i], Y[h], t[11 + i]);
      if (l < 3) { FORU(i, h - 1) V[h + 1 + i] = cm2(V[1 + i], V[h], t[18 + i]); }
      SB();
    }
    t[0] = cm1(U[8], wp[3]);
    FORU(i, 16) pk[i] = pkh(Y[i]);
    SB();
    U[16] = cm2(U[8], wp[3], t[0]);
    FORU(i, 16) { tp[(47 + i) * (TABP / 4)] = pk[i]; SB(); }
    auto xa = [](int i) { return i < 9 ? 1 : i < 19 ? 2 + (i - 9) / 5 : 4 + (i - 19) / 3; };
    auto xo = [](int i) { return i < 9 ? 2 * i : i < 19 ? 4 * ((i - 9) % 5) : 8 * ((i - 19) % 3); };
    FORU(i, 16) pk[i] = pkh(U[i]);
    SB();
    FORU(i, 31) { t[i] = cm1(U[xo(i)], V[xa(i)]); if (i < 16) tp[i * (TABP / 4)] = pk[i]; SB(); }
    FORU(i, 31) X[i] = cm2(U[xo(i)], V[xa(i)], t[i]);
    SB();
    FORU(i, 33) { if (i < 31) pk[i] = pkh(X[i]); if (i >= 2) tp[(14 + i) * (TABP / 4)] = pk[i - 2]; SB(); }
  }
  const int mo = WS_MOD + ((lane & 3) * 1536 + 16 * (pi >> 1) + (lane >> 2)) * 16;
  v4u gsb = ld128(rs, mo + 768 * 16), gcb = ld128(rs, mo + 1024 * 16);
  __syncthreads();
  STAMP(2);
  {
    const u32 ch = w >> 1, hi = w & 1;
    lptr tb_ = L + L_TAB + ch * TAB_CH + 16 * q4;
    const u32 i2 = (c16 + ((c16 >> 1) & 1)) >> 1, i4 = (c16 + 1 + ((c16 >> 2) & 1)) >> 2, i8 = (c16 + 3 + ((c16 >> 3) & 1)) >> 3;
    const u32 row[4] = {hi ? 35 + i8 : c16, hi ? 38 + i8 : 16 + i2, hi ? 41 + i8 : 25 + i4, hi ? 44 + i8 : 30 + i4};
    v4u yf[8], xf[8][4];
    auto ldk = [&](int k, int i) {
      if (k < 8) { if (i < 4) xf[k][i] = *(LAS v4u*)(tb_ + row[i] * TABP + 64 * k); else yf[k] = *(LAS v4u*)(tb_ + (47 + c16) * TABP + 64 * k); }
    };
#pragma unroll
    for (int k = 0; k < 2; ++k)
#pragma unroll
      for (int i = 4; i >= 0; --i) ldk(k, i);
    SB();
    f4 acc[4];
#pragma unroll
    for (int k = 0; k < 8; ++k)
#pragma unroll
      for (int a = 3; a >= 0; --a) {
        if (k) asm volatile("v_mfma_f32_16x16x32_f16 %0, %1, %2, %0" : "+v"(acc[a]) : "v"(xf[k][a]), "v"(yf[k]));
        else asm volatile("v_mfma_f32_16x16x32_f16 %0, %1, %2, 0" : "=&v"(acc[a]) : "v"(xf[k][a]), "v"(yf[k]));
        if (a == 3) ldk(k + 2, 4);
        ldk(k + 2, a);
        SB();
      }
    MM_TAIL();
    const int i0 = 2044 - (1024 * hi + 768 + 16 * c16 + 4 * q4);
    lptr t0 = L + L_TOE + ch * TOE_CH + toe_addr(i0), t1 = L + L_TOE + ch * TOE_CH + toe_addr(i0 + 4);
#pragma unroll
    for (int a = 0; a < 4; ++a) {
      const f4 v = acc[a] * kscale;
      const u32 P0 = pkbf(v.w, v.z), P1 = pkbf(v.y, v.x), M = pkbf(v.z, v.y);
      lptr p0 = t0 + 640 * (3 - a), p1 = t1 + 640 * (3 - a);
      *(LAS v2u*)(p1) = v2u{P0, P1};
      *(LAS u16*)(p0 + TOE_C + 6) = (u16)P0; *(LAS u32*)(p1 + TOE_C) = M; *(LAS u16*)(p1 + TOE_C + 4) = (u16)(P1 >> 16);
      *(LAS u32*)(p0 + 2 * TOE_C + 4) = P0; *(LAS u32*)(p1 + 2 * TOE_C) = P1;
      *(LAS u16*)(p0 + 3 * TOE_C + 2) = (u16)P0; *(LAS u32*)(p0 + 3 * TOE_C + 4) = M; *(LAS u16*)(p1 + 3 * TOE_C) = (u16)(P1 >> 16);
    }
  }
  STAMP(3);
  v4u gx[4], gs[4], gm;
  const int mo2 = WS_MOD + ((lane & 3) * 1536 + 256 * min(lane >> 2, 2u) + cp) * 16;
  auto s1_load = [&]() {
#pragma unroll
    for (int k = 0; k < 4; ++k) gx[k] = ld128s(rs, lane * 16, WS_XG + cp * 16384 + (4 * w + k) * 1024);
#pragma unroll
    for (int k = 0; k < 4; ++k) gs[k] = ld128s(rs, lane * 16, WS_STAT1 + (4 * w + k) * 1024);
    gm = ld128(rs, mo2);
  };
  s1_load();
  CFENCE();
  asm volatile("" ::"v"(pf0), "v"(pf2));
  auto pk_bad = [&]() { return __any(((gsb.y ^ tag) | (gsb.w ^ tag) | (gcb.y ^ tag) | (gcb.w ^ tag)) != 0); };
  if (pk_bad()) {
#pragma nounroll
    for (int sp = 0; sp < SPIN_CAP; ++sp) {
      asm volatile("" ::: "memory");
      gsb = ld128(rs, mo + 768 * 16); gcb = ld128(rs, mo + 1024 * 16);
      if (!pk_bad()) break;
    }
  }
  v4u wpk[2];
  float bwp[2];
  {
    const u32 sbp = pkbf(quadsum(__uint_as_float(gsb.x)), quadsum(__uint_as_float(gsb.z)));
    const u32 cbp = pkbf(quadsum(__uint_as_float(gcb.x)), quadsum(__uint_as_float(gcb.z)));
    const u32 scp = pkbf(bf2f(cbp) + 1.0f);
    const f2 sc[4] = {bf2f(dppu<0x150>(scp)), bf2f(dppu<0x154>(scp)), bf2f(dppu<0x158>(scp)), bf2f(dppu<0x15C>(scp))};
    const f2 sb[4] = {bf2f(dppu<0x150>(sbp)), bf2f(dppu<0x154>(sbp)), bf2f(dppu<0x158>(sbp)), bf2f(dppu<0x15C>(sbp))};
#pragma unroll
    for (int f = 0; f < 2; ++f) {
      lptr p = L + L_PKI + (f ? 4096 + (w & 1) * 1024 : w * 1024) + (8 * q4 + (c16 >> 2)) * 32 + 8 * (c16 & 3);
      const v2u lo = trread(p), hi = trread(p + 128);
      const u32 r[4] = {lo.x, lo.y, hi.x, hi.y};
      f2 bw = {0.f, 0.f}, sw = {0.f, 0.f};
#pragma unroll
      for (int j = 0; j < 4; ++j) {
        const f2 v = bf2f(r[j]);
        wpk[f][j] = pkbf(v * sc[j]);
        bw = __builtin_elementwise_fma(v, sb[j], bw);
        sw += bf2f(wpk[f][j]);
      }
      const float t = sw16sum(bw.x + bw.y, sw.x + sw.y);
      bwp[f] = sw32sum(t, t);
    }
    const int bwo = lane < 32 ? WS_BWP + (((pj * 2 + (pi & 1)) * 6 + w) * 16 + (pi >> 1)) * 128 + lane * 4 : OOB;
    st128(rs, wio, wpk[0]);
    st128s(rs, wio, two ? 65536 : OOB, wpk[1]);
    __builtin_amdgcn_raw_buffer_store_b32(__float_as_uint(bwp[0]), rs, bwo, 0, SC1);
    __builtin_amdgcn_raw_buffer_store_b32(__float_as_uint(bwp[1]), rs, bwo, two ? 8192 : OOB, SC1);
  }
  CFENCE();
  STAMP(27);
  auto s1_bad = [&]() {
    u32 bad = (gm.y ^ tag) | (gm.w ^ tag);
    FORU(k, 4) bad |= (gx[k].y ^ tag) | (gx[k].w ^ tag) | (gs[k].y ^ tag) | (gs[k].w ^ tag);
    return __any(bad != 0);
  };
  if (s1_bad()) {
#pragma nounroll
    for (int sp = 0; sp < SPIN_CAP; ++sp) {
      asm volatile("" ::: "memory");
      s1_load();
      CNT(29, 1);
      if (!s1_bad()) break;
    }
  }
  STAMP(4);
  u32 xv[8];
  u32 h0[4], h1[4];
  u32 gav;
  {
    const u32 mv = pkbf(quadsum(__uint_as_float(gm.x)), quadsum(__uint_as_float(gm.z)));
    const u32 sav = __builtin_amdgcn_readlane(mv, 0), cav = __builtin_amdgcn_readlane(mv, 4);
    gav = __builtin_amdgcn_readlane(mv, 8);
    const u32 s1v = pkbf(bf2f(cav) + 1.0f);
    const float s10 = bflo(s1v), s11 = bfhi(s1v), sa0 = bflo(sav), sa1 = bfhi(sav);
    f2 m[4], r[4], a[4], c[4];
    FORU(k, 4) {
      xv[2 * k] = gx[k].x; xv[2 * k + 1] = gx[k].z;
      m[k] = f2{bflo(gs[k].x), bflo(gs[k].z)}; r[k] = f2{bfhi(gs[k].x), bfhi(gs[k].z)};
      a[k] = f2{bflo(gx[k].x), bflo(gx[k].z)}; c[k] = f2{bfhi(gx[k].x), bfhi(gx[k].z)};
    }
    SB();
    FORU(k, 4) { r[k] = f2{__builtin_amdgcn_rcpf(r[k].x), __builtin_amdgcn_rcpf(r[k].y)}; a[k] -= m[k]; c[k] -= m[k]; }
    SB();
    FORU(k, 4) { a[k] *= r[k]; c[k] *= r[k]; }
    SB();
    FORU(k, 4) { a[k] = a[k] * s10 + sa0; c[k] = c[k] * s11 + sa1; }
    SB();
    FORU(k, 4) { h0[k] = pkbf(a[k]); h1[k] = pkbf(c[k]); }
    lptr up = L + L_U + 80 * (15 + (tid >> 2)) + 16 * (tid & 3);
    *(LAS v4u*)up = v4u{h0[0], h0[1], h0[2], h0[3]};
    *(LAS v4u*)(up + U_CH) = v4u{h1[0], h1[1], h1[2], h1[3]};
  }
  DRAIN();
  __builtin_amdgcn_raw_buffer_store_b32(tag, rs, lane == 0 ? WS_CD + b * 64 + w * 4 : OOB, 0, SC1);
  __syncthreads();
  STAMP(5);
  v4u cdv;
  const int zco = WS_Z2N + ((pj * 6 + (w >> 1)) * 64 + 2 * pi + (w & 1)) * 1024, pco = WS_PB + ((pi * 4 + w) * 44 + blk0) * 512;
  auto side = [&](auto S, auto I) {
    constexpr int s = S.v, i = I.v;
    if constexpr (s < 4 && (i & 1) && i < 8) W_DMA(s >> 1, 4 * (s & 1) + (i >> 1), (s >> 1) ? L_SLB : L_SLA);
    if (s == 4 && i == 1) st128s(rs, lane * 16, zco, SENT);
    if (s == 4 && i == 6) st128s(rs, lane * 16, zco + 131072, SENT);
    if (s == 5 && i == 1) st128s(rs, lane * 16, w + 8 < 2 * nb ? zco + 262144 : OOB, SENT);
    if (s == 5 && i == 6) st128s(rs, lane * 16, pco, SENT);
    if (s == 6 && i == 1) st128s(rs, lane * 16, pco + 1024, SENT);
    if (s == 6 && i == 6) st128s(rs, 128 + lane < 32 * nb ? lane * 16 : OOB, pco + 2048, SENT);
    if (s == 8 && i == 0) asm volatile("s_waitcnt vmcnt(6)" ::: "memory");
    if ((s == 8 || s == 9) && i < 8) w_get(s - 8, i, s == 9 ? L_SLB : L_SLA);
    if (s == 9 && i == 9) pf3 = __builtin_amdgcn_raw_buffer_load_b32(rw, lane < 32 ? pfo : OOB, 512, 0);
    if constexpr (s >= 10 && s < 14 && (i & 1) && i >= 2) W_DMA(2 + ((s - 10) >> 1), 4 * ((s - 10) & 1) + (i >> 1) - 1, ((s - 10) >> 1) ? L_SLB : L_SLA);
    if (s == 14 && i == 2) cdv = ld128(rs, WS_CD + tid * 64);
  };
  if (w & 1) conv2<1, 2>(L, w >> 1, lane, side);
  else conv2<0, 3>(L, w >> 1, lane, side);
  STAMP(21);
  auto cd_bad = [&]() { return __any(((cdv.x ^ tag) | (cdv.y ^ tag) | (cdv.z ^ tag) | (cdv.w ^ tag)) != 0); };
  if (cd_bad()) {
#pragma nounroll
    for (int sp = 0; sp < SPIN_CAP; ++sp) {
      asm volatile("" ::: "memory");
      cdv = ld128(rs, WS_CD + tid * 64);
      if (!cd_bad()) break;
    }
  }
  asm volatile("" ::"v"(pf3));
  DRAIN();
  __syncthreads();
  STAMP(6);
  {
    f2 y[2][4];
    const float dk0 = bflo(p_dsk), dk1 = bfhi(p_dsk);
#pragma unroll
    for (int k = 0; k < 4; ++k) { y[0][k] = bf2f(h0[k]) * dk0; y[1][k] = bf2f(h1[k]) * dk1; }
#pragma unroll
    for (int B = 0; B < 4; ++B) {
      if (B <= (int)w) {
        const int R = 128 - 32 * B, YO = B == 0 ? 0 : B == 1 ? 8448 : B == 2 ? 14848 : 19200;
        const u32 tp = 8 * tid - 512 * B, a = tp / R;
        lptr yp = L + L_Y + YO + a * 16 + tp * 4;
#pragma unroll
        for (int c = 0; c < 2; ++c) {
          const f4 u = *(LAS f4*)(yp + c * Y_CH), v = *(LAS f4*)(yp + c * Y_CH + 16);
          y[c][0] += f2{u.x, u.y}; y[c][1] += f2{u.z, u.w}; y[c][2] += f2{v.x, v.y}; y[c][3] += f2{v.z, v.w};
        }
      }
    }
    v4u o0, o1;
    const float ga0 = bflo(gav), ga1 = bfhi(gav);
#pragma unroll
    for (int k = 0; k < 4; ++k) {
      o0[k] = san(pkbf(f2{bflo(xv[2 * k]), bflo(xv[2 * k + 1])} + y[0][k] * ga0));
      o1[k] = san(pkbf(f2{bfhi(xv[2 * k]), bfhi(xv[2 * k + 1])} + y[1][k] * ga1));
    }
    st128(rs, x1w, o0);
    st128(rs, x1w + 128, o1);
  }
  CFENCE();
  STAMP(7);
  const u32 blkS = 4 + (w >> 1);
  const bool hasS = blkS < nb;
  const u32 fo[4] = {((pj * 2 + 1) * 6 + w), ((pj * 2 + 1) * 6 + (hasS ? blkS : 4)), (pj * 12 + w), (pj * 12 + (hasS ? blkS : 4))};
  v4u ones = {0x3f803f80u, 0x3f803f80u, 0x3f803f80u, 0x3f803f80u};
  pin(ones);
  v4u wf[4][16];
  auto ldf = [&](int s, int k) { wf[s][k] = ld128s(rs, lane * 16 + (k & 3) * 1024, WS_WINP + fo[s] * 16384 + (k >> 2) * 4096); };
  f4 sW[4];
  f4 bW[4];
  auto ldb = [&](int s) {
    bW[s] = __builtin_bit_cast(f4, ld128s(rs, c16 * 128 + q4 * 16, WS_BWP + fo[s] * 2048));
    sW[s] = __builtin_bit_cast(f4, ld128s(rs, c16 * 128 + q4 * 16, WS_BWP + fo[s] * 2048 + 64));
  };
  auto redb = [&](int s) {
    FORU(e, 16) { f4& v = (e & 4) ? sW[s + (e >> 3)] : bW[s + (e >> 3)]; v[e & 3] = dppadd<0xB1>(v[e & 3]); }
    SB();
    FORU(e, 16) { f4& v = (e & 4) ? sW[s + (e >> 3)] : bW[s + (e >> 3)]; v[e & 3] = dppadd<0x4E>(v[e & 3]); }
    SB();
    FORU(e, 16) { f4& v = (e & 4) ? sW[s + (e >> 3)] : bW[s + (e >> 3)]; v[e & 3] = dppadd<0x141>(v[e & 3]); }
    SB();
    FORU(e, 16) { f4& v = (e & 4) ? sW[s + (e >> 3)] : bW[s + (e >> 3)]; v[e & 3] = dppadd<0x140>(v[e & 3]); }
    SB();
  };
  {
    ldb(0); ldb(1);
#pragma unroll
    for (int k = 0; k < 16; ++k) { ldf(0, k); ldf(1, k); }
    CFENCE();
    v4u xr[16];
    const int xo = WS_X1 + pi * 65536 + w * 16384;
#pragma unroll
    for (int k = 0; k < 16; ++k) xr[k] = ld128s(rs, lane * 16, xo + k * 1024);
    CFENCE();
#pragma unroll
    for (int k = 0; k < 16; ++k) w_get(2 + (k >> 3), k & 7, k >> 3 ? L_SLB : L_SLA);
    __syncthreads();
    redb(0);
    pin(bW[0]); pin(bW[1]); pin(sW[0]); pin(sW[1]);
    STAMP(8);
    lptr xi = L + L_XIMG + w * 16384 + (lane >> 3) * 128 + (lane & 1) * 16;
    const u32 xs0 = (((lane & 7) >> 1) ^ ((lane >> 4) & 1)) * 32;
    u32 m = 0;
    FORU(k, 16) { *(LAS v4u*)(xi + k * 1024 + (xs0 ^ ((k & 1) * 64))) = xr[k]; m = vmax(m, xr[k]); SB(); }
    if (__any(m == ~0u)) {
#pragma nounroll
      for (int sp = 0; sp < SPIN_CAP; ++sp) {
        asm volatile("" ::: "memory");
        FORU(k, 16) xr[k] = ld128s(rs, lane * 16, xo + k * 1024);
        SB();
        m = 0;
        FORU(k, 16) { *(LAS v4u*)(xi + k * 1024 + (xs0 ^ ((k & 1) * 64))) = xr[k]; m = vmax(m, xr[k]); SB(); }
        CNT(29, 100);
        if (!__any(m == ~0u)) break;
      }
    }
    STAMP(9);
  }
  __syncthreads();
  STAMP(10);
  const u32 xsw = ((c16 >> 3) & 1) | ((q4 & 1) << 1), xbo = (8 * q4 + (c16 >> 2)) * 128 + 8 * (c16 & 3);
  lptr xb[4];
#pragma unroll
  for (int t = 0; t < 4; ++t) xb[t] = L + xbo + ((t ^ xw ^ xsw) * 32);
  const rsrc_t ro = __builtin_amdgcn_make_buffer_rsrc((void*)wout, 0, 688 * 1024, 0x00020000);
  const u32 ovo = (lane >> 3) * 1024 + 16 * ((lane & 7) ^ ((((lane >> 4) & 1) | ((w & 1) << 1)) << 1));
  const u32 osb = w * 8192 + 128 * pj, lw1 = lbase + w * 1024;
#define O_DMA(n) dma16<((n) < 19 ? L_WO + (n) * 4096 : L_WO2 + ((n) - 19) * 4096), (n) * 32768>(ro, ovo, osb, lw1)
  v2u bl[2][4], bh[2][4];
  auto ldx = [&](int k, int i) {
    if (k < 16) { if (i & 1) bh[k & 1][i >> 1] = trread(xb[i >> 1] + 4096 * k + 512); else bl[k & 1][i >> 1] = trread(xb[i >> 1] + 4096 * k); }
  };
  auto bx = [&](int k, int t) { return v4u{bl[k & 1][t].x, bl[k & 1][t].y, bh[k & 1][t].x, bh[k & 1][t].y}; };
  f4 z2a[6];
  f2 rn[4];
  {
    f4 st0, st1;
    v2u sl[16], sh[16];
    FORU(k, 4) { sl[k] = trread(xb[0] + 4096 * k); sh[k] = trread(xb[0] + 4096 * k + 512); }
    MM_HEAD();
    FORU(k, 16) {
      const v4u v = {sl[k].x, sl[k].y, sh[k].x, sh[k].y};
      if (k) m_vv(st0, ones, v); else m_vv0(st0, ones, v);
      if (k + 4 < 16) sl[k + 4] = trread(xb[0] + 4096 * (k + 4)); else ldx(0, 2 * (k - 12));
      SB();
      if (k) m_vv(st1, v, v); else m_vv0(st1, v, v);
      if (k + 4 < 16) sh[k + 4] = trread(xb[0] + 4096 * (k + 4) + 512); else ldx(0, 2 * (k - 12) + 1);
      SB();
    }
    float sv0 = 0.f, sv1 = 0.f;
    FORU(k, 16) FORU(i, 6) {
      const int u = i == 0 ? 0 : i == 1 ? 4 : i == 2 ? 1 : i == 3 ? 5 : i - 2;
      if (k) m_vv(z2a[u], wf[u >> 2][k], bx(k, u & 3)); else m_vv0(z2a[u], wf[u >> 2][k], bx(k, u & 3));
      if (i < 2) { ldx(k + 1, 2 * i); ldx(k + 1, 2 * i + 1); } else ldx(k + 1, i + 2);
      if (i == 2 && k) ldf(2, k - 1);
      if (i == 4 && (k & 1)) ldf(3, k >> 1);
      if (k == 0 && i == 3) pf1 = __builtin_amdgcn_raw_buffer_load_b32(ro, tid < 22 ? (b * 22 + tid) * 128 : OOB, 0, 0);
      if (i == 3 || i == 5) {
        const int z = 2 * k + (i == 5);
        if (z == 4) sv1 = (c16 & 1) ? st1.y : st1.x;
        if (z == 5) sv0 = (c16 & 1) ? st1.w : st1.z;
        if (z == 6) sv1 = (c16 & 2) ? sv0 : sv1;
        if (z == 7) sv0 = st0.x * (1.f / 512);
        if (z == 8) sv1 *= (1.f / 512);
        if (z == 9) sv1 = fmaxf(__builtin_fmaf(-sv0, sv0, sv1), 0.f);
        if (z == 10) sv1 = rbf(sv1);
        if (z == 11) sv0 = rbf(sv0);
        if (z == 12) sv1 = rbf(sv1 + 1e-5f);
        if (z == 13) sv1 = __builtin_amdgcn_sqrtf(sv1);
        if (z == 14) sv1 = rbf(sv1);
        if (z == 15) sv1 = __builtin_amdgcn_rcpf(sv1);
        if (z == 16) sv0 *= -sv1;
        if (z == 17) *(volatile LAS f2*)(L + (q4 == (c16 >> 2) ? L_XST + (16 * xw + c16) * 8 : L_XST + 512 + lane * 8)) = f2{sv1, sv0};
        if (z >= 4 && z <= 16) { pin(sv0); pin(sv1); }
        if (z == 22) __builtin_amdgcn_s_barrier();
        if (z >= 24 && z < 28) rn[z - 24] = *(volatile LAS f2*)(L + L_XST + (16 * ((z - 24) ^ xw) + c16) * 8);
      }
      SB();
    }
    MM_TAIL();
  }
  asm volatile("" ::"v"(pf1));
  STAMP(24);
  auto epi = [&](f4 (&a)[6], int s0, auto fill) {
    f2 v[12];
    FORU(i, 12) {
      const f4 &sw = sW[s0 + (i >> 3)], &bw = bW[s0 + (i >> 3)];
      v[i] = ((i & 1) ? f2{sw.z, sw.w} : f2{sw.x, sw.y}) * rn[(i >> 1) & 3].y + ((i & 1) ? f2{bw.z, bw.w} : f2{bw.x, bw.y});
    }
    SB();
    FORU(i, 12) v[i] = ((i & 1) ? f2{a[i >> 1].z, a[i >> 1].w} : f2{a[i >> 1].x, a[i >> 1].y}) * rn[(i >> 1) & 3].x + v[i];
    SB();
    fill();
    gelu_n<12>(v);
    FORU(i, 6) { a[i] = f4{v[2 * i].x, v[2 * i].y, v[2 * i + 1].x, v[2 * i + 1].y}; pin(a[i]); }
  };
  {
    epi(z2a, 0, [&]() { ldf(2, 15); FORU(k, 8) ldf(3, 8 + k); SB(); });
    if (!hasS) { z2a[4] = f4{0.f, 0.f, 0.f, 0.f}; z2a[5] = z2a[4]; }
    f2 zs[6], zq[6];
    FORU(i, 6) { zs[i] = f2{z2a[i].x, z2a[i].y} + f2{z2a[i].z, z2a[i].w}; zq[i] = f2{z2a[i].x, z2a[i].y} * f2{z2a[i].x, z2a[i].y}; }
    SB();
    FORU(i, 6) zq[i] = __builtin_elementwise_fma(f2{z2a[i].z, z2a[i].w}, f2{z2a[i].z, z2a[i].w}, zq[i]);
    FORU(i, 2) zs[i] += zs[4 + i];
    SB();
    float us[4], uq[4], u[4];
    FORU(i, 2) zq[i] += zq[4 + i];
    FORU(t, 4) us[t] = zs[t].x + zs[t].y;
    SB();
    FORU(t, 4) uq[3 - t] = zq[3 - t].x + zq[3 - t].y;
    SB();
    FORU(t, 4) u[3 - t] = sw32sum(us[3 - t], uq[3 - t]);
    LAS float* zp = (LAS float*)(L + L_ZST) + (w * 2 + (lane >> 5)) * 64 + c16;
    zp[16 * ((2 * (q4 & 1) + 1) ^ xw)] = sw16sum(u[1], u[3]);
    zp[16 * ((2 * (q4 & 1)) ^ xw)] = sw16sum(u[0], u[2]);
  }
  __syncthreads();
  if (w == 0) {
    LAS float* zp = (LAS float*)(L + L_ZST) + lane;
    const float s = (zp[0] + zp[128]) + (zp[256] + zp[384]), ss = (zp[64] + zp[192]) + (zp[320] + zp[448]);
    st128(rs, WS_ST2 + ((pi * 8 + pj) * 64 + lane) * 16, v4u{__float_as_uint(s), tag, __float_as_uint(ss), tag});
  }
  CFENCE();
  STAMP(11);
  f4 z1a[6];
  v2u xres[4];
  v4u g[8];
  auto s2b_ld = [&](int n) { g[n] = ld128s(rs, (2 * q4 * 64 + c16) * 16 + (n & 1) * 1024, WS_ST2 + pi * 8192 + ((n >> 1) ^ xw) * 256); };
  FORU(k, 16) { pin(wf[2][k]); pin(wf[3][k]); }
  {
#pragma unroll
    for (int i = 0; i < 8; ++i) ldx(0, i);
    MM_HEAD();
    sfor<0, 96>([&](auto z) {
      constexpr int k = z.v / 6, i = z.v % 6, u = i == 0 ? 0 : i == 1 ? 4 : i == 2 ? 1 : i == 3 ? 5 : i - 2;
      if (k) m_vv(z1a[u], wf[2 + (u >> 2)][k], bx(k, u & 3)); else m_vv0(z1a[u], wf[2 + (u >> 2)][k], bx(k, u & 3));
      if (i < 2) { ldx(k + 1, 2 * i); ldx(k + 1, 2 * i + 1); } else ldx(k + 1, i + 2);
      if constexpr (k < 8 && (i == 3 || i == 5)) { constexpr int n = 2 * k + (i == 5); W_DMA(4 + (n >> 3), n & 7, L_WO + (n >> 3) * 32768); }
      if (k == 8 && (i == 2 || i == 4)) ldb(1 + (i >> 1));
      if (k >= 10 && k < 14 && (i == 3 || i == 5)) s2b_ld(2 * (k - 10) + (i == 5));
      SB();
    });
    MM_TAIL();
#pragma unroll
    for (int n = 0; n < 4; ++n) {
      const u32 row = 16 * n + 4 * q4 + (c16 >> 2);
      xres[n] = trread(L + L_XIMG + pj * 8192 + row * 128 + ((w ^ (((row >> 1) & 1) | (((row >> 3) & 1) << 1))) * 32) + 8 * (c16 & 3));
    }
  }
  STAMP(12);
  {
    auto bad = [&]() { u32 v = 0; FORU(n, 8) v |= (g[n].y ^ tag) | (g[n].w ^ tag); return __any(v != 0); };
    if (bad()) {
#pragma nounroll
      for (int sp = 0; sp < SPIN_CAP; ++sp) {
        asm volatile("" ::: "memory");
        FORU(n, 8) s2b_ld(n);
        CNT(29, 10000);
        if (!bad()) break;
      }
    }
    STAMP(13);
    lptr tw = L + L_TRS + w * TRS_W;
    float a[4], c[4];
    FORU(t, 4) { a[t] = __uint_as_float(g[2 * t].x) + __uint_as_float(g[2 * t + 1].x); c[t] = __uint_as_float(g[2 * t].z) + __uint_as_float(g[2 * t + 1].z); }
    SB();
    const float a0 = sw32sum(a[0], a[2]), a1 = sw32sum(a[1], a[3]), c0 = sw32sum(c[0], c[2]), c1 = sw32sum(c[1], c[3]);
    const float m = sw16sum(a0, a1) * (1.f / 688), var = fmaxf(sw16sum(c0, c1) * (1.f / 688) - m * m, 0.f);
    const float r = __builtin_amdgcn_rcpf(ln_den(var)), n0 = -rbf(m) * r;
    auto gather = [&](float v, float (&o)[4]) {
      auto e = __builtin_amdgcn_permlane16_swap(__float_as_uint(v), __float_as_uint(v), false, false);
      auto e0 = __builtin_amdgcn_permlane32_swap(e[0], e[0], false, false);
      auto e1 = __builtin_amdgcn_permlane32_swap(e[1], e[1], false, false);
      o[0] = __uint_as_float(e0[0]); o[2] = __uint_as_float(e0[1]); o[1] = __uint_as_float(e1[0]); o[3] = __uint_as_float(e1[1]);
    };
    gather(r, a); gather(n0, c);
    SB();
    FORU(i, 6) z2a[i] = z2a[i] * a[i & 3] + c[i & 3];
    SB();
    FORU(i, 6) *(LAS v2u*)(tw + (16 * ((i & 3) ^ xw) + c16) * 64 + 8 * (((i & 4) + q4) ^ (c16 >> 1))) = v2u{pkbf(z2a[i].x, z2a[i].y), pkbf(z2a[i].z, z2a[i].w)};
    const u32 r0 = 8 * q4 + (c16 >> 2);
    const int zo = WS_Z2N + (pj * 6 * 64 + 2 * pi) * 1024 + lane * 16;
#pragma unroll
    for (int n = 0; n < 3; ++n) {
      lptr pp = tw + (n < 2 ? n : (w & 1)) * 2048 + r0 * 64;
      const u32 un = (n < 2 ? 0 : 4) + (c16 & 3);
      const v2u lo = trread(pp + 8 * (un ^ ((r0 >> 1) & 7))), hi = trread(pp + 256 + 8 * (un ^ (((r0 + 4) >> 1) & 7)));
      st128s(rs, zo, n < 2 ? w * 65536 + n * 1024 : (hasS ? blkS * 65536 + (w & 1) * 1024 : OOB), san4(v4u{lo.x, lo.y, hi.x, hi.y}));
    }
  }
  CFENCE();
  STAMP(14);
  __syncthreads();
  sfor<0, 16>([&](auto z) { W_DMA(6 + (z.v >> 3), z.v & 7, (z.v >> 3) * 32768); });
  FORU(n, 16) w_get(4 + (n >> 3), n & 7, L_WO + (n >> 3) * 32768);
  u32 bsr[4];
  v4u gg[2];
#pragma unroll
  for (int t = 0; t < 4; ++t) bsr[t] = gld<u16>(bsp, (64 * pi + 16 * (t ^ xw) + c16) * 2);
#pragma unroll
  for (int m = 0; m < 2; ++m) gg[m] = ld128(rs, WS_MOD + ((lane & 3) * 1536 + 1280 + 32 * pj + 16 * m + 8 * (c16 >> 3) + 2 * q4 + ((c16 >> 2) & 1)) * 16);
  redb(2);
  v2u z1[6];
  epi(z1a, 2, []() {});
  FORU(i, 6) { z1[i] = v2u{pkbf(z1a[i].x, z1a[i].y), pkbf(z1a[i].z, z1a[i].w)}; pin(z1[i]); }
  u32 gbp[2];
#pragma unroll
  for (int m = 0; m < 2; ++m) { gbp[m] = pkbf(quadsum(__uint_as_float(gg[m].x)), quadsum(__uint_as_float(gg[m].z))); pin(gbp[m]); }
  CFENCE();
#pragma unroll
  for (int n = 0; n < 16; ++n) w_get(6 + (n >> 3), n & 7, (n >> 3) * 32768);
  __syncthreads();
  STAMP(25);
  auto red_slot = [&](u32 blk, u32 snd) { return (blk < 4 ? blk * 3 + ((snd - blk - 1) & 3) : 4 * blk - 4 + snd) * 4096; };
  f4 ga[2][4];
  {
    const u32 zo = WS_Z2N + (pj * 6 * 64 + 16 * w) * 1024;
    const u32 rot = (pi & 3) == 3 ? 1 : 2 * (pi & 3) - (((pi & 3) == 2 && nb == 5) ? 1 : 0);
    auto blk_of = [&](u32 it) {
      u32 f = it + nb - 6 + rot;
      f = f >= nb - 1 ? f - (nb - 1) : f;
      u32 v = it == 5 ? w : w + 1 + f;
      return v >= nb ? v - nb : v;
    };
    auto soff = [&](u32 it) { return (u32)__builtin_amdgcn_readfirstlane((it < 6 && it + nb >= 6) ? zo + blk_of(it) * 65536 : (u32)OOB); };
    v4u zr[2][16];
    {
      const u32 s0 = soff(6 - nb);
#pragma nounroll
      for (int sp = 0; sp < SPIN_CAP; ++sp) {
        u32 m = 0;
        FORU(k, 16) zr[0][k] = ld128s(rs, lane * 16, s0 + k * 1024);
        FORU(k, 16) m = vmax(m, zr[0][k]);
        asm volatile("" ::: "memory");
        if (!__any(m == ~0u)) break;
        CNT(28, 100);
      }
      if (nb == 6) { FORU(k, 16) zr[1][k] = ld128s(rs, lane * 16, soff(1) + k * 1024); }
      else { FORU(k, 16) { zr[1][k] = zr[0][k]; zr[0][k] = v4u{0, 0, 0, 0}; } }
    }
    STAMP(15);
    u32 badm = 0;
    u32 so[6], rsl[6];
    FORU(it, 6) { so[it] = soff(it); rsl[it] = __builtin_amdgcn_readfirstlane(red_slot(blk_of(it + nb > 6 ? it - 1 : 1u), w)); }
    float ck = 0.f;
    u32 ra = 0;
    MM_HEAD();
    sfor<0, 96 * 4>([&](auto z) {
      constexpr int it = z.v / 64, k = (z.v / 4) % 16, t = z.v % 4, h = it & 1;
      if (k) m_va(ga[h][t], zr[h][k], wsf[k][t]); else m_va0(ga[h][t], zr[h][k], wsf[k][t]);
      constexpr int pz = z.v / 4 - 1, pit = pz / 16, pk = pz % 16;
      if constexpr (t == 1 && pz >= 0) {
        if constexpr (pit < 4) zr[pit & 1][pk] = ld128s(rs, lane * 16 + (pk & 3) * 1024, so[pit + 2] + (pk >> 2) * 4096);
        else if constexpr (pz - 64 < 19) O_DMA(pz - 64);
      }
      if constexpr (it > 0) {
        if (k == 2 && t == 2) { ck = ga[h ^ 1][0].x + ga[h ^ 1][0].y; pin(ck); }
        if (k == 2 && t == 3) { ck += ga[h ^ 1][0].z; pin(ck); }
        if (k == 3 && t == 2) { ck += ga[h ^ 1][0].w; pin(ck); }
        if (k == 3 && t == 3) badm |= __any(ck != ck) ? 1u << it : 0u;
        if (k >= 4 && k < 12 && !(k & 1) && t == 2) { ra = rsl[it] + lane * 16 + ((((k - 4) >> 1) ^ xw) * 1024); pin(ra); }
        if constexpr (k >= 4 && k < 12 && !(k & 1) && t == 3) *(LAS f4*)(L + L_RED + ra) = ga[h ^ 1][(k - 4) >> 1];
      }
      SB();
    });
    MM_TAIL();
    { const float t = (ga[1][0].x + ga[1][0].y) + (ga[1][0].z + ga[1][0].w); badm |= __any(t != t) ? 64u : 0u; }
    badm >>= 1;
    if (badm) {
#pragma nounroll
      for (u32 it = 6 - nb; it < 6; ++it) {
        if (!((badm >> it) & 1)) continue;
        CNT(28, 1);
        const u32 so = soff(it);
#pragma nounroll
        for (int sp = 0; sp < SPIN_CAP; ++sp) {
          u32 m = 0;
#pragma unroll
          for (int k = 0; k < 16; ++k) zr[0][k] = ld128s(rs, lane * 16, so + k * 1024);
#pragma unroll
          for (int k = 0; k < 16; ++k) m = vmax(m, zr[0][k]);
          asm volatile("" ::: "memory");
          if (!__any(m == ~0u)) break;
        }
        MM_HEAD();
#pragma unroll
        for (int k = 0; k < 16; ++k)
#pragma unroll
          for (int t = 0; t < 4; ++t) { if (k) m_va(ga[0][t], zr[0][k], wsf[k][t]); else m_va0(ga[0][t], zr[0][k], wsf[k][t]); }
        MM_TAIL();
#pragma unroll
        for (int t = 0; t < 4; ++t) {
          if (it < 5) *(LAS f4*)(L + L_RED + red_slot(blk_of(it), w) + lane * 16 + ((t ^ xw) * 1024)) = ga[0][t];
          else ga[1][t] = ga[0][t];
        }
      }
    }
  }
  DRAIN();
  STAMP(26);
  __syncthreads();
  STAMP(16);
  {
    f4 gq[6];
#pragma unroll
    for (int i = 0; i < 6; ++i) {
      const u32 tt = (i & 3) ^ xw;
      if (i < 4) {
        gq[i] = ga[1][i];
#pragma unroll
        for (int s = 1; s < 4; ++s) gq[i] += *(LAS f4*)(L + L_RED + ((w * 3 + s - 1) * 4 + tt) * 1024 + lane * 16);
      } else {
        lptr p = L + L_RED + ((blkS * 4 - 4) * 4 + tt) * 1024 + lane * 16;
        gq[i] = (*(LAS f4*)p + *(LAS f4*)(p + 4096)) + (*(LAS f4*)(p + 8192) + *(LAS f4*)(p + 12288));
      }
    }
    v2u P[6];
#pragma unroll
    for (int i = 0; i < 6; ++i) {
      const float bs = bflo(bsr[i & 3]);
      P[i] = v2u{pkbf(bf2f(z1[i].x) * (f2{gq[i].x, gq[i].y} + bs)), pkbf(bf2f(z1[i].y) * (f2{gq[i].z, gq[i].w} + bs))};
    }
#pragma unroll
    for (int h = 0; h < 3; ++h) {
      auto rx = __builtin_amdgcn_permlane16_swap(P[2 * h].x, P[2 * h + 1].x, false, false);
      auto ry = __builtin_amdgcn_permlane16_swap(P[2 * h].y, P[2 * h + 1].y, false, false);
      const u32 tt = (2 * (h & 1) + (q4 & 1)) ^ xw;
      st128s(rs, WS_PB + ((pi * 4 + tt) * 44 + blk0) * 512 + (q4 >> 1) * 256 + c16 * 16, h < 2 ? w * 512 : (hasS ? blkS * 512 : OOB),
             san4(v4u{rx[0], ry[0], rx[1], ry[1]}));
    }
  }
  STAMP(17);
  __syncthreads();
  O_DMA(19); O_DMA(20);
  if (w < 2) O_DMA(21);
  else *(LAS v4u*)(L + L_WO2 + (8 + w) * 1024 + lane * 16) = v4u{0, 0, 0, 0};
  {
    const int po = WS_PB + (pi * 4 + w) * 44 * 512;
    v4u pr[22];
    auto ldp = [&](int k) { pr[k] = ld128s(rs, (k == 21 && lane >= 32) ? OOB : lane * 16, po + k * 1024); };
    v4u wo[22][4];
    auto ldo = [&](int k) {
#pragma unroll
      for (int n = 0; n < 4; ++n) {
        lptr p = L + (k < 19 ? L_WO + 4096 * k : L_WO2 + 4096 * (k - 19)) + xbo + ((n ^ xsw) * 32);
        const v2u lo = trread(p), hi = trread(p + 512);
        wo[k][n] = v4u{lo.x, lo.y, hi.x, hi.y};
      }
    };
#pragma unroll
    for (int k = 0; k < 19; ++k) ldo(k);
    DRAIN();
    __syncthreads();
#pragma unroll
    for (int k = 19; k < 22; ++k) ldo(k);
    STAMP(18);
    f4 o[4];
#pragma nounroll
    for (int sp = 0; sp < SPIN_CAP; ++sp) {
#pragma unroll
      for (int k = 0; k < 14; ++k) ldp(k);
      MM_HEAD();
#pragma unroll
      for (int k = 0; k < 22; ++k) {
#pragma unroll
        for (int n = 0; n < 4; ++n) {
          if (k == 0) m_av0(o[n], wo[k][n], pr[k]); else if (k < 16) m_av(o[n], wo[k][n], pr[k]); else m_vv(o[n], wo[k][n], pr[k]);
        }
        if (k + 14 < 22) ldp(k + 14);
        SB();
      }
      MM_TAIL();
      if (!__any(o[0].x != o[0].x)) break;
      u32 m = 0;
#pragma unroll
      for (int k = 0; k < 22; ++k) m = vmax(m, pr[k]);
      if (!__any(m == ~0u)) break;
      asm volatile("" ::: "memory");
      CNT(28, 10000);
    }
    STAMP(19);
    v2u O[4];
#pragma unroll
    for (int n = 0; n < 4; ++n) {
      const u32 g0 = (n & 1) ? dppu<0x158>(gbp[n >> 1]) : dppu<0x150>(gbp[n >> 1]), g1 = (n & 1) ? dppu<0x15C>(gbp[n >> 1]) : dppu<0x154>(gbp[n >> 1]);
      O[n] = v2u{pkbf(bf2f(xres[n].x) + bf2f(g0) * f2{o[n].x, o[n].y}), pkbf(bf2f(xres[n].y) + bf2f(g1) * f2{o[n].z, o[n].w})};
    }
#pragma unroll
    for (int h = 0; h < 2; ++h) {
      auto rx = __builtin_amdgcn_permlane16_swap(O[2 * h].x, O[2 * h + 1].x, false, false);
      auto ry = __builtin_amdgcn_permlane16_swap(O[2 * h].y, O[2 * h + 1].y, false, false);
      *(v4u*)(out + (64 * pi + 16 * w + c16) * 512 + 64 * pj + 16 * (2 * h + (q4 & 1)) + 8 * (q4 >> 1)) = v4u{rx[0], ry[0], rx[1], ry[1]};
    }
  }
  if (b == 0 && tid == 0) __builtin_amdgcn_raw_buffer_store_b32(tag, rs, WS_E, 0, SC1);
  asm volatile("fk_text_b:");
  STAMP(20);
  STAMP_FLUSH();
}

extern "C" void kernel_launch(void* const* d_in, const int* in_sizes, int n_in, void* d_out, int out_size, void* d_ws, size_t ws_size,
                              hipStream_t stream) {
  (void)in_sizes; (void)n_in; (void)out_size; (void)ws_size;
  (void)hipFuncSetAttribute((const void*)fused_kernel, hipFuncAttributeMaxDynamicSharedMemorySize, LDS_BYTES);
  fused_kernel<<<256, 256, LDS_BYTES, stream>>>((const u16*)d_in[0], (const u16*)d_in[1], (const u16*)d_in[2], (const u16*)d_in[3],
                                                (const u16*)d_in[4], (const u16*)d_in[5], (const u16*)d_in[6], (const u16*)d_in[8],
                                                (const u16*)d_in[9], (const u16*)d_in[10], (const u16*)d_in[11], (const u16*)d_in[12],
                                                (const u16*)d_in[13], (u16*)d_out, (unsigned char*)d_ws);
}
```

```cpp
#include <hip/hip_runtime.h>
#include <stdint.h>

typedef unsigned u32;
typedef unsigned short u16;
typedef unsigned long long u64;
typedef u32 v4u __attribute__((ext_vector_type(4)));
typedef u32 v2u __attribute__((ext_vector_type(2)));
typedef float f4 __attribute__((ext_vector_type(4)));
typedef float f2 __attribute__((ext_vector_type(2)));
typedef __bf16 bf8 __attribute__((ext_vector_type(8)));
typedef __bf16 bf2 __attribute__((ext_vector_type(2)));
typedef _Float16 h8 __attribute__((ext_vector_type(8)));
typedef _Float16 h2 __attribute__((ext_vector_type(2)));
typedef short s4 __attribute__((ext_vector_type(4)));
#define LAS __attribute__((address_space(3)))
#define GAS __attribute__((address_space(1)))
#define CAS __attribute__((address_space(4)))
#define DI __device__ __forceinline__
typedef __amdgpu_buffer_rsrc_t rsrc_t;
typedef LAS unsigned char* lptr;

#define XMAP 2

constexpr int WS_E = 0, WS_STAT1 = 4096, WS_CD = 32768, WS_ST2 = 65536, WS_F3 = 327680, WS_MOD = 524288, WS_BWP = 655360;
constexpr int WS_XG = 1 << 20, WS_X1 = 5 << 20, WS_Z2N = 7 << 20, WS_PB = 10 << 20, WS_WINP = 13 << 20, WS_WOP = 29 << 19;
constexpr int WS_DBG = 24 << 20, WS_END = 25 << 20;
constexpr int OOB = 0x40000000;
constexpr int TABP = 544, TAB_CH = 63 * TABP;
constexpr int L_TAB = 0, L_Y = 0, Y_CH = 21504;
constexpr int L_PKW = 75776, L_SHF = 81920;
constexpr int L_PKI = 68544;
constexpr int L_TOE = 108544, TOE_C = 5312, TOE_CH = 4 * TOE_C;
constexpr int L_U = 151040, U_CH = 6320;
constexpr int L_XIMG = 0, L_TRS = 65536, TRS_W = 4096;
constexpr int L_RED = 0, L_RED2 = 81920;
constexpr int L_XST = 159744, L_ZST = L_XST + 1024;
constexpr int LDS_BYTES = 163840;
constexpr int SC1 = 16;
constexpr int SPIN_CAP = 1 << 15;
static_assert(LDS_BYTES <= 163840, "LDS");

DI float bflo(u32 v) { return __uint_as_float(v << 16); }
DI float bfhi(u32 v) { return __uint_as_float(v & 0xffff0000u); }
DI f2 bf2f(u32 v) { return f2{bflo(v), bfhi(v)}; }
DI u32 pkbf(float a, float b) { f2 v = {a, b}; return __builtin_bit_cast(u32, __builtin_convertvector(v, bf2)); }
DI u32 pkbf(f2 v) { return __builtin_bit_cast(u32, __builtin_convertvector(v, bf2)); }
DI u32 pkh(f2 v) { return __builtin_bit_cast(u32, __builtin_convertvector(v, h2)); }
DI float rbf(float a) { return bflo(pkbf(a, a)); }
DI u32 san(u32 v) { return v < 0xFFFFFFFEu ? v : 0xFFFFFFFEu; }
DI v4u san4(v4u v) { return v4u{san(v.x), san(v.y), san(v.z), san(v.w)}; }
DI u32 umax3(u32 a, u32 b, u32 c) { return max(max(a, b), c); }
DI u32 umin3(u32 a, u32 b, u32 c) { return min(min(a, b), c); }
DI u32 vmax(u32 m, v4u v) { return umax3(umax3(m, v.x, v.y), v.z, v.w); }
DI v4u ld128(rsrc_t rs, int off) { return __builtin_amdgcn_raw_buffer_load_b128(rs, off, 0, SC1); }
DI v4u ld128s(rsrc_t rs, int off, int soff) { return __builtin_amdgcn_raw_buffer_load_b128(rs, off, soff, SC1); }
DI void st128(rsrc_t rs, int off, v4u v) { __builtin_amdgcn_raw_buffer_store_b128(v, rs, off, 0, SC1); }
DI void st128s(rsrc_t rs, int off, int soff, v4u v) { __builtin_amdgcn_raw_buffer_store_b128(v, rs, off, soff, SC1); }
DI void st64(rsrc_t rs, int off, v2u v) { __builtin_amdgcn_raw_buffer_store_b64(v, rs, off, 0, SC1); }
#define DRAIN() asm volatile("s_waitcnt vmcnt(0)" ::: "memory")
#define SB() __builtin_amdgcn_sched_barrier(0)
#define CFENCE() do { asm volatile("" ::: "memory"); SB(); } while (0)
template <int C> DI u32 dppu(u32 v) { return (u32)__builtin_amdgcn_update_dpp(0, (int)v, C, 0xF, 0xF, true); }
template <int C> DI float dppadd(float v) { return v + __uint_as_float(dppu<C>(__float_as_uint(v))); }
DI float sw16sum(float a, float b) { auto r = __builtin_amdgcn_permlane16_swap(__float_as_uint(a), __float_as_uint(b), false, false); return __uint_as_float(r[0]) + __uint_as_float(r[1]); }
DI float sw32sum(float a, float b) { auto r = __builtin_amdgcn_permlane32_swap(__float_as_uint(a), __float_as_uint(b), false, false); return __uint_as_float(r[0]) + __uint_as_float(r[1]); }
DI float quadsum(float v) { v = dppadd<0xB1>(v); return dppadd<0x4E>(v); }
DI float rowsum(float v) { v = quadsum(v); v = dppadd<0x141>(v); return dppadd<0x140>(v); }
DI f4 mfma_bf(v4u a, v4u b, f4 c) { return __builtin_amdgcn_mfma_f32_16x16x32_bf16(__builtin_bit_cast(bf8, a), __builtin_bit_cast(bf8, b), c, 0, 0, 0); }
DI f4 mfma_h(v4u a, v4u b, f4 c) { return __builtin_amdgcn_mfma_f32_16x16x32_f16(__builtin_bit_cast(h8, a), __builtin_bit_cast(h8, b), c, 0, 0, 0); }
DI v2u trread(lptr p) { return __builtin_bit_cast(v2u, __builtin_amdgcn_ds_read_tr16_b64_v4i16((LAS s4*)p)); }
template <int N> struct IC { static constexpr int v = N; };
template <int I, int N, class F> DI void sfor(F f) { if constexpr (I < N) { f(IC<I>{}); sfor<I + 1, N>(f); } }
#define FORU(i, n) _Pragma("unroll") for (int i = 0; i < (n); ++i)
DI f2 cm1(f2 a, f2 b) { f2 t; asm("v_pk_mul_f32 %0, %1, %2 op_sel:[0,0] op_sel_hi:[0,1]" : "=v"(t) : "v"(a), "v"(b)); return t; }
DI f2 cm2(f2 a, f2 b, f2 t) { f2 r; asm("v_pk_fma_f32 %0, %1, %2, %3 op_sel:[1,1,0] op_sel_hi:[1,0,1] neg_lo:[0,1,0]" : "=v"(r) : "v"(a), "v"(b), "v"(t)); return r; }
DI f2 cexp_p(float al2, float rev, float p) {
  float m = __builtin_amdgcn_exp2f(al2 * p), f = __builtin_amdgcn_fractf(rev * p);
  return f2{m * __builtin_amdgcn_cosf(f), m * __builtin_amdgcn_sinf(f)};
}
template <int N, class F> DI void gelu_n(f2 (&v)[N], F fill) {
  const float c0 = -2.0f * 0.7978845608028654f * 1.4426950408889634f, c1 = c0 * 0.044715f;
  f2 a[N];
  sfor<0, 2>([&](auto z) { FORU(i, 6) a[6 * z.v + i] = v[6 * z.v + i] * v[6 * z.v + i]; fill(IC<4 + z.v>{}); });
  sfor<0, 2>([&](auto z) { FORU(i, 6) a[6 * z.v + i] = __builtin_elementwise_fma(a[6 * z.v + i], f2{c1, c1}, f2{c0, c0}); fill(IC<6 + z.v>{}); });
  sfor<0, 2>([&](auto z) { FORU(i, 6) a[6 * z.v + i] *= v[6 * z.v + i]; fill(IC<8 + z.v>{}); });
  sfor<0, 6>([&](auto z) { FORU(i, 2) a[2 * z.v + i] = f2{__builtin_amdgcn_exp2f(a[2 * z.v + i].x), __builtin_amdgcn_exp2f(a[2 * z.v + i].y)}; fill(IC<10 + z.v>{}); });
  sfor<0, 2>([&](auto z) { FORU(i, 6) a[6 * z.v + i] += 1.0f; fill(IC<16 + z.v>{}); });
  sfor<0, 6>([&](auto z) { FORU(i, 2) a[2 * z.v + i] = f2{__builtin_amdgcn_rcpf(a[2 * z.v + i].x), __builtin_amdgcn_rcpf(a[2 * z.v + i].y)}; fill(IC<18 + z.v>{}); });
  sfor<0, 2>([&](auto z) { FORU(i, 6) v[6 * z.v + i] *= a[6 * z.v + i]; fill(IC<24 + z.v>{}); });
}
DI float ln_den(float var) { return rbf(__builtin_amdgcn_sqrtf(rbf(rbf(var) + 1e-5f))); }
template <class T> DI T gld(const void* base, u32 off) { return *(const GAS T*)((const GAS char*)base + off); }
#define MF "v_mfma_f32_16x16x32_bf16 "
DI void m_av(f4& c, v4u a, v4u b) { asm volatile(MF "%0, %1, %2, %0" : "+v"(c) : "a"(a), "v"(b)); }
DI void m_va(f4& c, v4u a, v4u b) { asm volatile(MF "%0, %1, %2, %0" : "+v"(c) : "v"(a), "a"(b)); }
DI void m_vv(f4& c, v4u a, v4u b) { asm volatile(MF "%0, %1, %2, %0" : "+v"(c) : "v"(a), "v"(b)); }
DI void m_av0(f4& c, v4u a, v4u b) { asm volatile(MF "%0, %1, %2, 0" : "=&v"(c) : "a"(a), "v"(b)); }
DI void m_va0(f4& c, v4u a, v4u b) { asm volatile(MF "%0, %1, %2, 0" : "=&v"(c) : "v"(a), "a"(b)); }
DI void m_vv0(f4& c, v4u a, v4u b) { asm volatile(MF "%0, %1, %2, 0" : "=&v"(c) : "v"(a), "v"(b)); }
DI v4u aget(v4u a) {
  v4u r;
  _Pragma("unroll") for (int e = 0; e < 4; ++e) { u32 t; asm volatile("v_accvgpr_read_b32 %0, %1" : "=v"(t) : "a"(a[e])); r[e] = t; }
  return r;
}
DI u32 ssel(bool c, u32 a, u32 b) { asm("" : "+s"(a)); asm("" : "+s"(b)); return c ? a : b; }
DI u32 smod(u32 v, u32 m) { u32 t = v - m; asm("" : "+s"(t)); return (int)t >= 0 ? t : v; }
template <class T> DI void pin(T& v) { asm volatile("" : "+v"(v)); }
#define MM_HEAD() do { SB(); asm volatile("s_nop 7"); SB(); } while (0)
#define MM_TAIL() do { SB(); asm volatile("s_nop 7"); SB(); } while (0)
DI int toe_addr(int pos) { return 2 * pos + 8 * (pos >> 4); }

#define STAMP(n)
#define STAMP_FLUSH()
#define STAMP_INIT()
#define CNT(n,v)

template <int BAND> struct Band {
  static constexpr int R = 128 - 32 * BAND, NT = R / 16, NS = 15 + NT / 2;
  static constexpr int YO = BAND == 0 ? 0 : BAND == 1 ? 8448 : BAND == 2 ? 14848 : 19200;
  lptr al, ah, bb, yp;
  f4 acc[NT];
  v2u lo[32], hi[32];
  v4u bf[NS];
  DI void init(lptr L, int ch, int lane) {
    const int r = lane & 15, q = lane >> 4, c = (3 - r) & 3;
    const int P = 2048 - 512 * BAND - 496 - 4 * (r >> 2) + 8 * q;
    al = L + L_TOE + ch * TOE_CH + c * TOE_C + toe_addr(P);
    ah = L + L_TOE + ch * TOE_CH + c * TOE_C + toe_addr(P + 4);
    bb = L + L_U + ch * U_CH + 80 * (R / 32) * r + 16 * q;
    yp = L + L_Y + ch * Y_CH + YO + r * (4 * R + 16) + 16 * q;
  }
  DI void load(int s, int i) {
    if (i < 4) {
      const int n = 2 * s + (i >> 1);
      if (n < 32) { if (i & 1) hi[n] = *(LAS v2u*)(ah + 40 * (31 - n)); else lo[n] = *(LAS v2u*)(al + 40 * (31 - n)); }
    } else if (s < NS) bf[s] = *(LAS v4u*)(bb + 80 * (NT / 2 - 1 - s + 15));
  }
  DI void mma(int s, int t) {
    const int n = t - NT + 2 + 2 * s;
    if (s < NS && n >= 0 && n < 32) {
      if (n < 2) m_vv0(acc[t], v4u{lo[n].x, lo[n].y, hi[n].x, hi[n].y}, bf[s]); else m_vv(acc[t], v4u{lo[n].x, lo[n].y, hi[n].x, hi[n].y}, bf[s]);
    }
  }
  DI void flush(int s, int t) {
    const int n = t - NT + 2 + 2 * (s - 1);
    if (n == 30 || n == 31) *(LAS f4*)(yp + 64 * t) = acc[t];
  }
};
template <int B1, int B2, class F> DI void conv2(lptr L, int ch, int lane, F side) {
  Band<B1> u; Band<B2> v;
  u.init(L, ch, lane); v.init(L, ch, lane);
  constexpr int N1 = Band<B1>::NT, NS = Band<B1>::NS;
#pragma unroll
  for (int s = 0; s < 2; ++s)
#pragma unroll
    for (int i = 0; i < 5; ++i) { u.load(s, i); v.load(s, i); SB(); }
  sfor<0, 10 * (NS + 1)>([&](auto z) {
    constexpr int s = z.v / 10, i = z.v % 10;
    if (s >= 17 && i == 0) asm volatile("s_nop %0" ::"i"(s == NS ? 7 : s == NS - 1 ? 5 : 2));
    if (i < N1) { u.mma(s, i); u.flush(s, i); } else { v.mma(s, i - N1); v.flush(s, i - N1); }
    if (i < 5) u.load(s + 2, i); else v.load(s + 2, i - 5);
    side(IC<s>{}, IC<i>{});
    SB();
  });
}

__global__ void __launch_bounds__(256, 1)
fused_kernel(const u16* __restrict__ x, const u16* __restrict__ lar, const u16* __restrict__ aim, const u16* __restrict__ cre,
             const u16* __restrict__ cim, const u16* __restrict__ ldt, const u16* __restrict__ dsk, const u16* __restrict__ win,
             const u16* __restrict__ wsp, const u16* __restrict__ bsp, const u16* __restrict__ wout, const u16* __restrict__ cvec,
             const u16* __restrict__ wmod, u16* __restrict__ out, unsigned char* ws) {
  extern __shared__ __attribute__((aligned(16))) unsigned char smem[];
  lptr L = (lptr)smem;
  const u32 b = blockIdx.x, tid = threadIdx.x, lane = tid & 63;
  STAMP_INIT();
  STAMP(0);
  const u32 w = __builtin_amdgcn_readfirstlane(tid >> 6);
  const u32 xc = b & 7, rr = b >> 3, cp = xc * 32 + rr;
  const u32 c16 = lane & 15, q4 = lane >> 4;
  const u32 tag = *(const CAS u32*)(ws + WS_E) + 1u;
  const u32 tb = 512 * (b >> 6) + 32 * (b & 15) + 2 * ((b >> 4) & 3);
  const v4u sr0 = gld<v4u>(x, (tb + 8 * w) * 1024 + lane * 16), sr1 = gld<v4u>(x, (tb + 8 * w) * 1024 + 1024 + lane * 16);
  const u32 xgo = (tb + 8 * (lane & 3)) * 1024 + (16 * w + (lane >> 2)) * 16;
  const v4u xg0 = gld<v4u>(x, xgo), xg1 = gld<v4u>(x, xgo + 1024);
  const u32 mG = b % 48, mP = min(b / 48, 3u), mrow = mP * 128 + (lane >> 1);
  v4u wm[4];
  u32 cv[4];
#pragma unroll
  for (int n = 0; n < 4; ++n) wm[n] = gld<v4u>(wmod, (mrow + 32 * n) * 6144 + mG * 128 + w * 32 + (lane & 1) * 16);
#pragma unroll
  for (int n = 0; n < 4; ++n) cv[n] = gld<u16>(cvec, (mrow + 32 * n) * 2);
  const u32 pidx = (cp * 256 + tid) * 2;
  const u32 p_lar = gld<u16>(lar, pidx), p_aim = gld<u16>(aim, pidx), p_cr = gld<u16>(cre, pidx), p_ci = gld<u16>(cim, pidx);
  const u32 p_cr2 = gld<u16>(cre, pidx ^ 128), p_ci2 = gld<u16>(cim, pidx ^ 128);
  const u32 p_ldt = *(const CAS u32*)(ldt + 2 * cp), p_dsk = *(const CAS u32*)(dsk + 2 * cp);
  CFENCE();
  u32 pf0 = 0, pf1 = 0;
  {
    u64 pc;
    u32 len;
    asm volatile("s_getpc_b64 %0\nfk_text_a:\n\ts_mov_b32 %1, fk_text_b-fk_text_a" : "=s"(pc), "=s"(len));
    const u32 o = (rr * 16 + tid) * 128;
    if (tid < 16 && o < len) pf0 = gld<u32>((const void*)pc, o);
  }
  CFENCE();
  const u32 pi = (xc >> 1) * 8 + (rr & 7), pj = (xc & 1) * 4 + (rr >> 3);
  const u32 nb = pj < 3 ? 6 : 5, blk0 = 5 * pj + min(pj, 3u);
  const bool two = w + 4 < nb;
  const rsrc_t rs = __builtin_amdgcn_make_buffer_rsrc((void*)ws, 0, WS_END, 0x00020000);
  const v4u SENT = {~0u, ~0u, ~0u, ~0u};
  const int x1w = WS_X1 + ((tid >> 3) * 512 + 2 * cp) * 128 + (tid & 7) * 16;
  const u32 wio_src = ((32 * (pi >> 1) + (lane >> 1)) * 1376 + (pi & 1) * 688 + 16 * (blk0 + w)) * 2 + (lane & 1) * 16;
  const v4u wi0 = gld<v4u>(win, wio_src), wi1 = gld<v4u>(win, two ? wio_src + 128 : wio_src);
  const rsrc_t ro = __builtin_amdgcn_make_buffer_rsrc((void*)wout, 0, 688 * 1024, 0x00020000);
  const v4u wo0 = __builtin_amdgcn_raw_buffer_load_b128(ro, (lane >> 1) * 1024 + (lane & 1) * 16, b < 176 ? (b % 22) * 32768 + (b / 22) * 128 + w * 32 : OOB, 0);
  CFENCE();
  if (tid < 40) *(LAS v4u*)(L + L_TOE + (tid / 5) * TOE_C + 5120 + (tid % 5) * 16) = v4u{0, 0, 0, 0};
  if (tid < 150) *(LAS v4u*)(L + L_U + (tid / 75) * U_CH + (tid % 75) * 16) = v4u{0, 0, 0, 0};
  STAMP(23);
  {
    f2 a[8];
#pragma unroll
    for (int e = 0; e < 4; ++e) { a[2 * e] = f2{bflo(sr0[e]), bflo(sr1[e])}; a[2 * e + 1] = f2{bfhi(sr0[e]), bfhi(sr1[e])}; }
    f2 s = ((a[0] + a[1]) + (a[2] + a[3])) + ((a[4] + a[5]) + (a[6] + a[7]));
    float t = rowsum(sw32sum(s.x, s.y));
    t = sw16sum(t, t) * (1.f / 512);
    auto mm = __builtin_amdgcn_permlane32_swap(__float_as_uint(t), __float_as_uint(t), false, false);
    const f2 m = {__uint_as_float(mm[0]), __uint_as_float(mm[1])};
    s = f2{0.f, 0.f};
#pragma unroll
    for (int e = 0; e < 8; ++e) { const f2 u = a[e] - m; s = __builtin_elementwise_fma(u, u, s); }
    float v = rowsum(sw32sum(s.x, s.y));
    v = sw16sum(v, v) * (1.f / 512);
    st64(rs, (lane & 31) == 0 ? WS_STAT1 + b * 64 + w * 16 + (lane >> 5) * 8 : OOB, v2u{pkbf(t, ln_den(v)), tag});
  }
  {
    const int o = WS_XG + (4 * (16 * w + (lane >> 2))) * 16384 + b * 64 + (lane & 3) * 16;
    st128(rs, o, v4u{xg0.x, tag, xg1.x, tag});
    st128s(rs, o, 16384, v4u{xg0.y, tag, xg1.y, tag});
    st128s(rs, o, 32768, v4u{xg0.z, tag, xg1.z, tag});
    st128s(rs, o, 49152, v4u{xg0.w, tag, xg1.w, tag});
  }
  CFENCE();
  STAMP(22);
  {
    f2 a[4];
#pragma unroll
    for (int n = 0; n < 4; ++n) {
      const float c = bflo(cv[n]);
      const float s = rbf(c * __builtin_amdgcn_rcpf(1.f + __builtin_amdgcn_exp2f(-1.4426950408889634f * c)));
#pragma unroll
      for (int e = 0; e < 4; ++e) a[e] = n ? a[e] + bf2f(wm[n][e]) * s : bf2f(wm[n][e]) * s;
    }
    const float u0 = sw32sum(a[0].x, a[2].x), u1 = sw32sum(a[0].y, a[2].y), u2 = sw32sum(a[1].x, a[3].x), u3 = sw32sum(a[1].y, a[3].y);
    float t0 = sw16sum(u0, u2), t1 = sw16sum(u1, u3);
    t0 = dppadd<0x4E>(dppadd<0x124>(dppadd<0x128>(t0)));
    t1 = dppadd<0x4E>(dppadd<0x124>(dppadd<0x128>(t1)));
    st128(rs, (c16 < 2 && b < 192) ? WS_MOD + (mP * 1536 + mG * 32 + w * 8 + c16 * 4 + q4) * 16 : OOB, v4u{__float_as_uint(t0), tag, __float_as_uint(t1), tag});
  }
  CFENCE();
  STAMP(1);
  const u32 xw = 2 * (w & 1) + (w >> 1);
  const int wio = WS_WINP + (((pj * 2 + (pi & 1)) * 6 + w) * 16 + (pi >> 1)) * 1024 + lane * 16;
  const u32 pf2 = gld<u32>(wsp, (64 * pi + 8 * pj) * 4096 + tid * 128);
  st128(rs, x1w, SENT);
  st128(rs, x1w + 128, SENT);
  CFENCE();
  const rsrc_t rw = __builtin_amdgcn_make_buffer_rsrc((void*)wsp, 0, 2048 * 4096, 0x00020000);
  const u32 wvo = (lane >> 3) * 4096 + (lane & 7) * 16;
  const u32 wsb[4] = {(64 * pi + 16 * xw) * 4096 + w * 1024, (64 * pi + 16 * (xw ^ 1)) * 4096 + w * 1024,
                      (64 * pi + 16 * (xw ^ 2)) * 4096 + w * 1024, (64 * pi + 16 * (xw ^ 3)) * 4096 + w * 1024};
  v4u wraw[64], wsf[16][4];
  auto rq = [&](int g) { return (g + pj) & 3; };
  auto ldw = [&](int n) { wraw[n] = __builtin_amdgcn_raw_buffer_load_b128(rw, (wvo + rq(n >> 4) * 256) + ((n >> 3) & 1) * 128, wsb[(n & 7) >> 1] + (n & 1) * 32768, 0); };
  const u32 wwa = (u32)(size_t)L + L_SHF + w * 8192 + (lane >> 3) * 128 + ((lane & 7) ^ (lane >> 3)) * 16;
  auto shuf = [&](auto Z) {
    constexpr int c = Z.v >> 4, j = Z.v & 15, i = j & 7;
    if constexpr (j < 8) asm volatile("ds_write_b128 %0, %1 offset:%2" ::"v"(wwa), "a"(wraw[8 * c + i]), "i"(i * 1024) : "memory");
    else wsf[2 * c + (i & 1)][i >> 1] = *(LAS v4u*)(L + L_SHF + w * 8192 + (i >> 1) * 2048 + c16 * 128 + (((4 * (i & 1) + q4) ^ (c16 & 7)) * 16));
  };
  float kscale;
  {
    const float L2E = 1.4426950408889634f;
    const float dt = rbf(__builtin_amdgcn_exp2f(((w >> 1) ? bfhi(p_ldt) : bflo(p_ldt)) * L2E));
    const float al2 = (-rbf(__builtin_amdgcn_exp2f(bflo(p_lar) * L2E)) * dt) * L2E;
    const float rev = (bflo(p_aim) * dt) * 0.15915494309189535f;
    u32 mx = max(max(p_cr & 0x7fffu, p_ci & 0x7fffu), max(p_cr2 & 0x7fffu, p_ci2 & 0x7fffu));
    mx = max(mx, dppu<0xB1>(mx)); mx = max(mx, dppu<0x4E>(mx)); mx = max(mx, dppu<0x141>(mx)); mx = max(mx, dppu<0x140>(mx));
    { auto r = __builtin_amdgcn_permlane16_swap(mx, mx, false, false); mx = max(r[0], r[1]); }
    { auto r = __builtin_amdgcn_permlane32_swap(mx, mx, false, false); mx = max(r[0], r[1]); }
    u32 ex = mx >> 7;
    ex = min(max(ex, 1u), 253u);
    const float dn = __uint_as_float((254u - ex) << 23), up = __uint_as_float(ex << 23);
    kscale = 2.0f * dt * up;
    LAS u32* tp = (LAS u32*)(L + L_TAB + (tid >> 7) * TAB_CH + (tid & 127) * 4);
    f2 U[17], Y[16], V[8], X[31], wp[4], t[31];
    u32 pk[31];
    wp[0] = cexp_p(al2, rev, 1.f);
    Y[1] = cexp_p(al2, -rev, 16.f);
    V[1] = cexp_p(al2, rev, 256.f);
    Y[0] = f2{1.f, 0.f};
    U[0] = f2{bflo(p_cr) * dn, bflo(p_ci) * dn};
    SB();
    FORU(l, 4) {
      const int h = 1 << l;
      FORU(i, h) t[i] = cm1(U[i], wp[l]);
      if (l < 3) { t[8] = cm1(wp[l], wp[l]); t[9] = cm1(Y[h], Y[h]); }
      if (l < 2) t[10] = cm1(V[h], V[h]);
      FORU(i, h - 1) t[11 + i] = cm1(Y[1 + i], Y[h]);
      if (l < 3) { FORU(i, h - 1) t[18 + i] = cm1(V[1 + i], V[h]); }
      SB();
      FORU(i, h) U[h + i] = cm2(U[i], wp[l], t[i]);
      if (l < 3) { wp[l + 1] = cm2(wp[l], wp[l], t[8]); Y[2 * h] = cm2(Y[h], Y[h], t[9]); }
      if (l < 2) V[2 * h] = cm2(V[h], V[h], t[10]);
      FORU(i, h - 1) Y[h + 1 + i] = cm2(Y[1 + i], Y[h], t[11 + i]);
      if (l < 3) { FORU(i, h - 1) V[h + 1 + i] = cm2(V[1 + i], V[h], t[18 + i]); }
      SB();
    }
    auto xa = [](int i) { return i < 9 ? 1 : i < 19 ? 2 + (i - 9) / 5 : 4 + (i - 19) / 3; };
    auto xo = [](int i) { return i < 9 ? 2 * i : i < 19 ? 4 * ((i - 9) % 5) : 8 * ((i - 19) % 3); };
    u32 py[16], pu[16];
    t[0] = cm1(U[8], wp[3]);
    FORU(i, 16) { py[i] = pkh(Y[i]); if (i == 2) U[16] = cm2(U[8], wp[3], t[0]); if (i & 1) tp[(47 + (i >> 1)) * (TABP / 4)] = py[i >> 1]; SB(); }
    FORU(i, 16) { pu[i] = pkh(U[i]); if (i & 1) tp[(55 + (i >> 1)) * (TABP / 4)] = py[8 + (i >> 1)]; SB(); }
    FORU(i, 31) { t[i] = cm1(U[xo(i)], V[xa(i)]); if (!(i & 1)) tp[(i >> 1) * (TABP / 4)] = pu[i >> 1]; SB(); }
    FORU(i, 35) {
      if (i < 31) X[i] = cm2(U[xo(i)], V[xa(i)], t[i]);
      if (i >= 2 && i < 33) pk[i - 2] = pkh(X[i - 2]);
      if (i >= 4) tp[(12 + i) * (TABP / 4)] = pk[i - 4];
      SB();
    }
  }
  *(LAS v4u*)(L + L_PKI + w * 1024 + lane * 16) = wi0;
  if (w < 2) *(LAS v4u*)(L + L_PKI + 4096 + w * 1024 + lane * 16) = wi1;
  *(LAS v4u*)(L + L_PKW + w * 8192 + lane * 16) = wo0;
  v4u gx[4], gs[4], gm[3];
  const int mo = WS_MOD + ((lane & 3) * 1536 + 16 * (pi >> 1) + (lane >> 2)) * 16;
  const int mo2 = WS_MOD + ((lane & 3) * 1536 + 256 * min(lane >> 2, 2u) + cp) * 16;
  auto s1_ld = [&](int n) {
    if (n < 4) gx[n] = ld128s(rs, lane * 16 + n * 1024, WS_XG + cp * 16384 + w * 4096);
    else if (n < 8) gs[n - 4] = ld128s(rs, lane * 16 + (n - 4) * 1024, WS_STAT1 + w * 4096);
    else gm[n - 8] = n == 8 ? ld128(rs, mo2) : ld128s(rs, mo, (n == 9 ? 768 : 1024) * 16);
  };
  FORU(n, 3) s1_ld(n);
  __syncthreads();
  STAMP(2);
  f4 kacc[4];
  v2u pl[3], ph[3];
  {
    const u32 hi = w & 1;
    lptr tb_ = L + L_TAB + (w >> 1) * TAB_CH + 16 * q4;
    const u32 i2 = (c16 + ((c16 >> 1) & 1)) >> 1, i4 = (c16 + 1 + ((c16 >> 2) & 1)) >> 2, i8 = (c16 + 3 + ((c16 >> 3) & 1)) >> 3;
    const u32 row[4] = {hi ? 35 + i8 : c16, hi ? 38 + i8 : 16 + i2, hi ? 41 + i8 : 25 + i4, hi ? 44 + i8 : 30 + i4};
    v4u yf[8], xf[8][4];
    auto ldk = [&](int k, int i) {
      if (k < 8) { if (i < 4) xf[k][i] = *(LAS v4u*)(tb_ + row[i] * TABP + 64 * k); else yf[k] = *(LAS v4u*)(tb_ + (47 + c16) * TABP + 64 * k); }
    };
    FORU(k, 2) FORU(i, 5) ldk(k, 4 - i);
    SB();
    const u32 po = (8 * q4 + (c16 >> 2)) * 32 + 8 * (c16 & 3);
    FORU(k, 8) FORU(a, 4) {
      if (k) asm volatile("v_mfma_f32_16x16x32_f16 %0, %1, %2, %0" : "+v"(kacc[3 - a]) : "v"(xf[k][3 - a]), "v"(yf[k]));
      else asm volatile("v_mfma_f32_16x16x32_f16 %0, %1, %2, 0" : "=&v"(kacc[3 - a]) : "v"(xf[k][3 - a]), "v"(yf[k]));
      if (a == 0) ldk(k + 2, 4);
      ldk(k + 2, 3 - a);
      if (k < 4 && (a & 1)) s1_ld(3 + 2 * k + (a >> 1));
      if (k >= 6 && a < 3) {
        lptr p = L + (a == 0 ? L_PKI + w * 1024 : a == 1 ? L_PKI + 4096 + (w & 1) * 1024 : L_PKW + w * 8192) + po;
        if (k == 6) pl[a] = trread(p); else ph[a] = trread(p + 128);
      }
      SB();
    }
  }
  MM_TAIL();
  f2 pv[8];
  {
    const int i0 = 2044 - (1024 * (w & 1) + 768 + 16 * c16 + 4 * q4);
    lptr t0 = L + L_TOE + (w >> 1) * TOE_CH + toe_addr(i0), t1 = L + L_TOE + (w >> 1) * TOE_CH + toe_addr(i0 + 4);
    u32 P0[4], P1[4], M[4];
    FORU(a, 4) kacc[a] *= kscale;
    SB();
    FORU(a, 4) { P0[a] = pkbf(kacc[a].w, kacc[a].z); P1[a] = pkbf(kacc[a].y, kacc[a].x); M[a] = pkbf(kacc[a].z, kacc[a].y); }
    SB();
    FORU(a, 4) {
      lptr p0 = t0 + 640 * (3 - a), p1 = t1 + 640 * (3 - a);
      *(LAS v2u*)(p1) = v2u{P0[a], P1[a]};
      *(LAS u16*)(p0 + TOE_C + 6) = (u16)P0[a]; *(LAS u32*)(p1 + TOE_C) = M[a]; *(LAS u16*)(p1 + TOE_C + 4) = (u16)(P1[a] >> 16);
      *(LAS u32*)(p0 + 2 * TOE_C + 4) = P0[a]; *(LAS u32*)(p1 + 2 * TOE_C) = P1[a];
      *(LAS u16*)(p0 + 3 * TOE_C + 2) = (u16)P0[a]; *(LAS u32*)(p0 + 3 * TOE_C + 4) = M[a]; *(LAS u16*)(p1 + 3 * TOE_C) = (u16)(P1[a] >> 16);
      FORU(j, 2) { pv[2 * a + j] = bf2f((a & 1) ? ph[a >> 1][j] : pl[a >> 1][j]); pin(pv[2 * a + j]); }
      SB();
    }
    st128s(rs, lane * 16, b < 176 ? WS_WOP + (b * 4 + w) * 1024 : OOB, v4u{pl[2].x, pl[2].y, ph[2].x, ph[2].y});
  }
  asm volatile("" ::"v"(pf0), "v"(pf2));
  STAMP(3);
  auto s1_bad = [&]() {
    u32 mx = umax3(gm[0].y, gm[0].w, tag), mn = umin3(gm[0].y, gm[0].w, tag);
    FORU(k, 2) { mx = umax3(mx, gm[1 + k].y, gm[1 + k].w); mn = umin3(mn, gm[1 + k].y, gm[1 + k].w); }
    FORU(k, 4) {
      mx = umax3(umax3(mx, gx[k].y, gx[k].w), gs[k].y, gs[k].w);
      mn = umin3(umin3(mn, gx[k].y, gx[k].w), gs[k].y, gs[k].w);
    }
    return __any(mx != mn);
  };
  if (s1_bad()) {
#pragma nounroll
    for (int sp = 0; sp < SPIN_CAP; ++sp) {
      asm volatile("" ::: "memory");
      FORU(n, 11) s1_ld(n);
      CNT(29, 1);
      if (!s1_bad()) break;
    }
  }
  STAMP(4);
  u32 xv[8];
  u32 h0[4], h1[4];
  u32 gav;
  {
    float qs[6];
    f2 m[4], r[4], a[4], c[4];
    int wn = 0;
    auto fill = [&]() { SB(); ldw(wn); ++wn; SB(); };
    FORU(i, 6) qs[i] = dppadd<0xB1>(__uint_as_float((i & 1) ? gm[i >> 1].z : gm[i >> 1].x));
    FORU(k, 4) { xv[2 * k] = gx[k].x; xv[2 * k + 1] = gx[k].z; r[k] = f2{bfhi(gs[k].x), bfhi(gs[k].z)}; }
    fill();
    FORU(i, 6) qs[i] = dppadd<0x4E>(qs[i]);
    FORU(k, 4) m[k] = f2{bflo(gs[k].x), bflo(gs[k].z)};
    fill();
    const u32 mv = pkbf(qs[0], qs[1]), sbp = pkbf(qs[2], qs[3]), cbp = pkbf(qs[4], qs[5]);
    FORU(k, 4) r[k] = f2{__builtin_amdgcn_rcpf(r[k].x), __builtin_amdgcn_rcpf(r[k].y)};
    fill();
    const u32 sav = __builtin_amdgcn_readlane(mv, 0), cav = __builtin_amdgcn_readlane(mv, 4);
    gav = __builtin_amdgcn_readlane(mv, 8);
    const u32 scp = pkbf(bf2f(cbp) + 1.0f);
    FORU(k, 4) { a[k] = f2{bflo(gx[k].x), bflo(gx[k].z)}; c[k] = f2{bfhi(gx[k].x), bfhi(gx[k].z)}; }
    fill();
    const u32 s1v = pkbf(bf2f(cav) + 1.0f);
    FORU(k, 4) { a[k] -= m[k]; c[k] -= m[k]; }
    const u32 scb[4] = {dppu<0x150>(scp), dppu<0x154>(scp), dppu<0x158>(scp), dppu<0x15C>(scp)};
    fill();
    FORU(k, 4) { a[k] *= r[k]; c[k] *= r[k]; }
    f2 sc[4], sb[4], t[8], bw[2], sw[2];
    FORU(j, 4) sc[j] = bf2f(scb[j]);
    fill();
    FORU(k, 4) { a[k] = a[k] * bflo(s1v) + bflo(sav); c[k] = c[k] * bfhi(s1v) + bfhi(sav); }
    FORU(j, 8) t[j] = pv[j] * sc[j & 3];
    fill();
    v4u wpk[2];
    FORU(k, 4) { h0[k] = pkbf(a[k]); h1[k] = pkbf(c[k]); }
    FORU(j, 8) wpk[j >> 2][j & 3] = pkbf(t[j]);
    fill();
    lptr up = L + L_U + 80 * (15 + (tid >> 2)) + 16 * (tid & 3);
    *(LAS v4u*)up = v4u{h0[0], h0[1], h0[2], h0[3]};
    *(LAS v4u*)(up + U_CH) = v4u{h1[0], h1[1], h1[2], h1[3]};
    st128(rs, wio, wpk[0]);
    st128s(rs, wio, two ? 65536 : OOB, wpk[1]);
    const u32 sbb[4] = {dppu<0x150>(sbp), dppu<0x154>(sbp), dppu<0x158>(sbp), dppu<0x15C>(sbp)};
    FORU(j, 8) t[j] = bf2f(wpk[j >> 2][j & 3]);
    SB();
    FORU(j, 4) sb[j] = bf2f(sbb[j]);
    FORU(f, 2) sw[f] = (t[4 * f] + t[4 * f + 1]) + (t[4 * f + 2] + t[4 * f + 3]);
    SB();
    FORU(j, 8) pv[j] *= sb[j & 3];
    SB();
    FORU(f, 2) bw[f] = (pv[4 * f] + pv[4 * f + 1]) + (pv[4 * f + 2] + pv[4 * f + 3]);
    const float e0 = sw16sum(bw[0].x + bw[0].y, sw[0].x + sw[0].y), e1 = sw16sum(bw[1].x + bw[1].y, sw[1].x + sw[1].y);
    const int bwo = WS_BWP + (((pj * 2 + (pi & 1)) * 6 + w) * 16 + (pi >> 1)) * 128 + (lane & 31) * 4;
    __builtin_amdgcn_raw_buffer_store_b32(__float_as_uint(sw32sum(e0, e1)), rs, lane < 32 ? bwo : two ? bwo + 8192 : OOB, 0, SC1);
  }
  __syncthreads();
  STAMP(5);
  v4u cdv;
  const u32 fpo = lane < 48 ? WS_WINP + pj * 196608 + (((pi & 7) * 4 + w) * 48 + lane) * 128 : OOB;
  auto side = [&](auto S, auto I) {
    constexpr int s = S.v, i = I.v, z = 10 * s + i;
    if constexpr (z % 4 == 1 && z / 4 < 40) ldw(8 + z / 4);
    if (z == 43) {
      asm volatile("s_waitcnt vmcnt(11)" ::: "memory");
      __builtin_amdgcn_raw_buffer_store_b32(tag, rs, lane == 0 ? WS_CD + b * 64 + w * 4 : OOB, 0, SC1);
    }
    if (z == 123) pf1 = __builtin_amdgcn_raw_buffer_load_b32(rs, fpo, 0, SC1);
    if (z == 163) cdv = ld128(rs, WS_CD + tid * 64);
  };
  if (w & 1) conv2<1, 2>(L, w >> 1, lane, side);
  else conv2<0, 3>(L, w >> 1, lane, side);
  STAMP(21);
  {
    f2 y[2][4], t[2][4];
    const float ga0 = bflo(gav), ga1 = bfhi(gav);
    FORU(k, 4) { t[0][k] = bf2f(h0[k]); t[1][k] = bf2f(h1[k]); }
    FORU(k, 4) { y[0][k] = f2{bflo(xv[2 * k]), bflo(xv[2 * k + 1])}; y[1][k] = f2{bfhi(xv[2 * k]), bfhi(xv[2 * k + 1])}; }
    lptr yp[4];
    FORU(B, 4) {
      const u32 tp = 8 * tid - 512 * B;
      yp[B] = L + L_Y + (B == 0 ? 0 : B == 1 ? 8448 : B == 2 ? 14848 : 19200) + ((tp & 2047) / (128 - 32 * B)) * 16 + tp * 4;
      pin(yp[B]);
    }
    SB();
    FORU(k, 4) { t[0][k] *= bflo(p_dsk); t[1][k] *= bfhi(p_dsk); pin(y[0][k]); pin(y[1][k]); pin(t[0][k]); pin(t[1][k]); }
    __syncthreads();
    STAMP(6);
    f4 q[4][4];
    FORU(B, 4) {
      if (B <= (int)w) { FORU(e, 4) q[B][e] = *(LAS f4*)(yp[B] + (e >> 1) * Y_CH + (e & 1) * 16); }
      else { FORU(e, 4) q[B][e] = f4{0.f, 0.f, 0.f, 0.f}; }
    }
    FORU(B, 4) { FORU(e, 4) { t[e >> 1][2 * (e & 1)] += f2{q[B][e].x, q[B][e].y}; t[e >> 1][2 * (e & 1) + 1] += f2{q[B][e].z, q[B][e].w}; } SB(); }
    FORU(k, 4) { y[0][k] += t[0][k] * ga0; y[1][k] += t[1][k] * ga1; }
    SB();
    v4u o0, o1;
    FORU(k, 4) { o0[k] = pkbf(y[0][k]); o1[k] = pkbf(y[1][k]); }
    SB();
    st128(rs, x1w, san4(o0));
    st128(rs, x1w + 128, san4(o1));
  }
  CFENCE();
  asm volatile("" ::"v"(pf1));
  auto cd_bad = [&]() { return __any(umax3(umax3(cdv.x, cdv.y, cdv.z), cdv.w, tag) != umin3(umin3(cdv.x, cdv.y, cdv.z), cdv.w, tag)); };
  if (cd_bad()) {
#pragma nounroll
    for (int sp = 0; sp < SPIN_CAP; ++sp) {
      asm volatile("" ::: "memory");
      cdv = ld128(rs, WS_CD + tid * 64);
      if (!cd_bad()) break;
    }
  }
  __syncthreads();
  STAMP(7);
  const u32 blkS = 4 + (w >> 1);
  const bool hasS = blkS < nb;
  const u32 fo[4] = {((pj * 2 + 1) * 6 + w), ((pj * 2 + 1) * 6 + (hasS ? blkS : 4)), (pj * 12 + w), (pj * 12 + (hasS ? blkS : 4))};
  v4u ones = {0x3f803f80u, 0x3f803f80u, 0x3f803f80u, 0x3f803f80u};
  pin(ones);
  v4u wf[4][16];
  auto ldf = [&](int s, int k) { wf[s][k] = ld128s(rs, lane * 16 + (k & 3) * 1024, WS_WINP + fo[s] * 16384 + (k >> 2) * 4096); };
  f4 sW[4];
  f4 bW[4];
  auto ldb = [&](int s) {
    bW[s] = __builtin_bit_cast(f4, ld128s(rs, c16 * 128 + q4 * 16, WS_BWP + fo[s] * 2048));
    sW[s] = __builtin_bit_cast(f4, ld128s(rs, c16 * 128 + q4 * 16, WS_BWP + fo[s] * 2048 + 64));
  };
  auto redb = [&](int s) {
    FORU(e, 16) { f4& v = (e & 4) ? sW[s + (e >> 3)] : bW[s + (e >> 3)]; v[e & 3] = dppadd<0xB1>(v[e & 3]); }
    SB();
    FORU(e, 16) { f4& v = (e & 4) ? sW[s + (e >> 3)] : bW[s + (e >> 3)]; v[e & 3] = dppadd<0x4E>(v[e & 3]); }
    SB();
    FORU(e, 16) { f4& v = (e & 4) ? sW[s + (e >> 3)] : bW[s + (e >> 3)]; v[e & 3] = dppadd<0x141>(v[e & 3]); }
    SB();
    FORU(e, 16) { f4& v = (e & 4) ? sW[s + (e >> 3)] : bW[s + (e >> 3)]; v[e & 3] = dppadd<0x140>(v[e & 3]); }
    SB();
  };
  {
    ldb(0); ldb(1);
    v4u xr[16];
    const int xo = WS_X1 + pi * 65536 + w * 16384;
    sfor<0, 32>([&](auto z) {
      constexpr int n = z.v;
      if constexpr (n < 16) ldf(n & 1, n >> 1); else xr[n - 16] = ld128s(rs, lane * 16 + (n & 3) * 1024, xo + rq((n - 16) >> 2) * 4096);
      shuf(IC<2 * n>{}); shuf(IC<2 * n + 1>{});
      SB();
    });
    redb(0);
    pin(bW[0]); pin(bW[1]); pin(sW[0]); pin(sW[1]);
    STAMP(8);
    lptr xi = L + L_XIMG + w * 16384 + (lane >> 3) * 128 + (lane & 1) * 16;
    const u32 xs0 = (((lane & 7) >> 1) ^ ((lane >> 4) & 1)) * 32;
    u32 m = 0;
    FORU(k, 16) { *(LAS v4u*)(xi + rq(k >> 2) * 4096 + (k & 3) * 1024 + (xs0 ^ ((k & 1) * 64))) = xr[k]; m = vmax(m, xr[k]); ldf(k & 1, 8 + (k >> 1)); SB(); }
    if (__any(m == ~0u)) {
#pragma nounroll
      for (int sp = 0; sp < SPIN_CAP; ++sp) {
        asm volatile("" ::: "memory");
        FORU(k, 16) xr[k] = ld128s(rs, lane * 16, xo + rq(k >> 2) * 4096 + (k & 3) * 1024);
        SB();
        m = 0;
        FORU(k, 16) { *(LAS v4u*)(xi + rq(k >> 2) * 4096 + (k & 3) * 1024 + (xs0 ^ ((k & 1) * 64))) = xr[k]; m = vmax(m, xr[k]); SB(); }
        CNT(29, 100);
        if (!__any(m == ~0u)) break;
      }
    }
    STAMP(9);
  }
  __syncthreads();
  STAMP(10);
  const u32 xsw = ((c16 >> 3) & 1) | ((q4 & 1) << 1), xbo = (8 * q4 + (c16 >> 2)) * 128 + 8 * (c16 & 3);
  lptr xb[4];
#pragma unroll
  for (int t = 0; t < 4; ++t) xb[t] = L + xbo + ((t ^ xw ^ xsw) * 32);
  v2u bl[2][4], bh[2][4];
  auto ldx = [&](int k, int i) {
    if (k < 16) { if (i & 1) bh[k & 1][i >> 1] = trread(xb[i >> 1] + 4096 * k + 512); else bl[k & 1][i >> 1] = trread(xb[i >> 1] + 4096 * k); }
  };
  auto bx = [&](int k, int t) { return v4u{bl[k & 1][t].x, bl[k & 1][t].y, bh[k & 1][t].x, bh[k & 1][t].y}; };
  const int pco = WS_PB + ((pi * 4 + w) * 44 + blk0) * 512, pc2 = 128 + lane < 32 * nb ? lane * 16 : OOB;
  const int zco = WS_Z2N + ((pj * 6 + (w >> 1)) * 64 + 2 * pi + (w & 1)) * 1024, zc2 = w + 8 < 2 * nb ? zco + 262144 : OOB;
  f4 z2a[6];
  f2 rn[4];
  {
    f4 st0, st1;
    v2u sl[16], sh[16];
    FORU(k, 4) { sl[k] = trread(xb[0] + 4096 * k); sh[k] = trread(xb[0] + 4096 * k + 512); }
    MM_HEAD();
    sfor<0, 16>([&](auto z) {
      constexpr int k = z.v;
      const v4u v = {sl[k].x, sl[k].y, sh[k].x, sh[k].y};
      if (k) m_vv(st0, ones, v); else m_vv0(st0, ones, v);
      if constexpr (k + 4 < 16) sl[k + 4] = trread(xb[0] + 4096 * (k + 4)); else ldx(0, 2 * (k - 12));
      SB();
      if (k) m_vv(st1, v, v); else m_vv0(st1, v, v);
      if constexpr (k + 4 < 16) sh[k + 4] = trread(xb[0] + 4096 * (k + 4) + 512); else ldx(0, 2 * (k - 12) + 1);
      if (k == 1) st128s(rs, lane * 16, pco, SENT);
      if (k == 5) st128s(rs, lane * 16, pco + 1024, SENT);
      if (k == 9) st128s(rs, pc2, pco + 2048, SENT);
      if (k == 3) st128s(rs, lane * 16, zco, SENT);
      if (k == 7) st128s(rs, lane * 16, zco + 131072, SENT);
      if (k == 11) st128s(rs, lane * 16, zc2, SENT);
      SB();
    });
    float sv0 = 0.f, sv1 = 0.f;
    FORU(k, 16) FORU(i, 6) {
      const int u = i == 0 ? 0 : i == 1 ? 4 : i == 2 ? 1 : i == 3 ? 5 : i - 2;
      if (k) m_vv(z2a[u], wf[u >> 2][k], bx(k, u & 3)); else m_vv0(z2a[u], wf[u >> 2][k], bx(k, u & 3));
      if (i < 2) { ldx(k + 1, 2 * i); ldx(k + 1, 2 * i + 1); } else ldx(k + 1, i + 2);
      if (i == 2 && k) ldf(2 + ((k - 1) & 1), (k - 1) >> 1);
      if (i == 3 || i == 5) {
        const int z = 2 * k + (i == 5);
        if (z == 4) sv1 = (c16 & 1) ? st1.y : st1.x;
        if (z == 5) sv0 = (c16 & 1) ? st1.w : st1.z;
        if (z == 6) sv1 = (c16 & 2) ? sv0 : sv1;
        if (z == 7) { pin(st0); sv0 = st0.x * (1.f / 512); }
        if (z == 8) sv1 *= (1.f / 512);
        if (z == 9) sv1 = fmaxf(__builtin_fmaf(-sv0, sv0, sv1), 0.f);
        if (z == 10) sv1 = rbf(sv1);
        if (z == 11) sv0 = rbf(sv0);
        if (z == 12) sv1 = rbf(sv1 + 1e-5f);
        if (z == 13) sv1 = __builtin_amdgcn_sqrtf(sv1);
        if (z == 14) sv1 = rbf(sv1);
        if (z == 15) sv1 = __builtin_amdgcn_rcpf(sv1);
        if (z == 16) sv0 *= -sv1;
        if (z == 17) *(volatile LAS f2*)(L + (q4 == (c16 >> 2) ? L_XST + (16 * xw + c16) * 8 : L_XST + 512 + lane * 8)) = f2{sv1, sv0};
        if (z >= 4 && z <= 16) { pin(sv0); pin(sv1); }
        if (z == 22) __builtin_amdgcn_s_barrier();
        if (z >= 24 && z < 28) rn[z - 24] = *(volatile LAS f2*)(L + L_XST + (16 * ((z - 24) ^ xw) + c16) * 8);
      }
      SB();
    }
    MM_TAIL();
  }
  STAMP(24);
  auto shuf2 = [&](auto C, auto N) {
    constexpr int n = N.v % 12, z = 16 * (C.v + N.v / 12);
    if constexpr (N.v < 24) { if constexpr (n < 8) shuf(IC<z + n>{}); else { shuf(IC<z + 2 * n - 8>{}); shuf(IC<z + 2 * n - 7>{}); } }
  };
  auto epi = [&](f4 (&a)[6], int s0, auto fill) {
    f2 v[12];
    sfor<0, 2>([&](auto z) {
      FORU(j, 6) {
        const int i = 6 * z.v + j;
        const f4 &sw = sW[s0 + (i >> 3)], &bw = bW[s0 + (i >> 3)];
        v[i] = ((i & 1) ? f2{sw.z, sw.w} : f2{sw.x, sw.y}) * rn[(i >> 1) & 3].y + ((i & 1) ? f2{bw.z, bw.w} : f2{bw.x, bw.y});
      }
      fill(IC<z.v>{});
    });
    sfor<0, 2>([&](auto z) {
      FORU(j, 6) { const int i = 6 * z.v + j; v[i] = ((i & 1) ? f2{a[i >> 1].z, a[i >> 1].w} : f2{a[i >> 1].x, a[i >> 1].y}) * rn[(i >> 1) & 3].x + v[i]; }
      fill(IC<2 + z.v>{});
    });
    gelu_n<12>(v, fill);
    FORU(i, 6) { a[i] = f4{v[2 * i].x, v[2 * i].y, v[2 * i + 1].x, v[2 * i + 1].y}; pin(a[i]); }
  };
  {
    epi(z2a, 0, [&](auto N) {
      constexpr int n = N.v;
      SB();
      if constexpr ((n & 1) && n < 22) ldf(2 + ((15 + n / 2) & 1), (15 + n / 2) >> 1);
      if constexpr (n >= 22) ldw(26 + n);
      shuf2(IC<4>{}, N);
      SB();
    });
    if (!hasS) { z2a[4] = f4{0.f, 0.f, 0.f, 0.f}; z2a[5] = z2a[4]; }
    f2 zs[6], zq[6];
    FORU(i, 6) { zs[i] = f2{z2a[i].x, z2a[i].y} + f2{z2a[i].z, z2a[i].w}; zq[i] = f2{z2a[i].x, z2a[i].y} * f2{z2a[i].x, z2a[i].y}; }
    SB();
    FORU(i, 6) zq[i] = __builtin_elementwise_fma(f2{z2a[i].z, z2a[i].w}, f2{z2a[i].z, z2a[i].w}, zq[i]);
    FORU(i, 2) zs[i] += zs[4 + i];
    SB();
    float us[4], uq[4], u[4];
    FORU(i, 2) zq[i] += zq[4 + i];
    FORU(t, 4) us[t] = zs[t].x + zs[t].y;
    SB();
    FORU(t, 4) uq[3 - t] = zq[3 - t].x + zq[3 - t].y;
    SB();
    FORU(t, 4) u[3 - t] = sw32sum(us[3 - t], uq[3 - t]);
    LAS float* zp = (LAS float*)(L + L_ZST) + (w * 2 + (lane >> 5)) * 64 + c16;
    zp[16 * ((2 * (q4 & 1) + 1) ^ xw)] = sw16sum(u[1], u[3]);
    zp[16 * ((2 * (q4 & 1)) ^ xw)] = sw16sum(u[0], u[2]);
  }
  asm volatile("s_waitcnt vmcnt(20)" ::: "memory");
  __syncthreads();
  if (w == 0) {
    LAS float* zp = (LAS float*)(L + L_ZST) + lane;
    const float s = (zp[0] + zp[128]) + (zp[256] + zp[384]), ss = (zp[64] + zp[192]) + (zp[320] + zp[448]);
    st128(rs, WS_ST2 + ((pi * 8 + pj) * 64 + lane) * 16, v4u{__float_as_uint(s), tag, __float_as_uint(ss), tag});
  }
  __builtin_amdgcn_raw_buffer_store_b32(tag, rs, tid == 64 ? WS_F3 + (pj * 32 + pi) * 128 : OOB, 0, SC1);
  CFENCE();
  STAMP(11);
  f4 z1a[6];
  v2u xres[4];
  v4u g[8];
  u32 fv;
  auto f_ld = [&]() { fv = __builtin_amdgcn_raw_buffer_load_b32(rs, (lane & 7) * 128, WS_F3 + (pj * 32 + 8 * w) * 128, SC1); };
  auto s2b_ld = [&](int n) { g[n] = ld128s(rs, (2 * q4 * 64 + c16) * 16 + (n & 1) * 1024, WS_ST2 + pi * 8192 + ((n >> 1) ^ xw) * 256); };
  {
#pragma unroll
    for (int i = 0; i < 8; ++i) ldx(0, i);
    MM_HEAD();
    sfor<0, 96>([&](auto z) {
      constexpr int k = z.v / 6, i = z.v % 6, u = i == 0 ? 0 : i == 1 ? 4 : i == 2 ? 1 : i == 3 ? 5 : i - 2;
      if (k) m_vv(z1a[u], wf[2 + (u >> 2)][k], bx(k, u & 3)); else m_vv0(z1a[u], wf[2 + (u >> 2)][k], bx(k, u & 3));
      if (i < 2) { ldx(k + 1, 2 * i); ldx(k + 1, 2 * i + 1); } else ldx(k + 1, i + 2);
      if (k < 3 && (i == 2 || i == 4)) ldf(1 + (i >> 1), 13 + k);
      if (k == 3 && (i == 2 || i == 4)) ldb(1 + (i >> 1));
      if (k >= 4 && k < 10 && (i == 2 || i == 4)) ldw(52 + 2 * (k - 4) + (i >> 2));
      if (k >= 10 && k < 14 && (i == 3 || i == 5)) s2b_ld(2 * (k - 10) + (i == 5));
      if (k == 14 && i == 3) f_ld();
      SB();
    });
    MM_TAIL();
#pragma unroll
    for (int n = 0; n < 4; ++n) {
      const u32 row = 16 * n + 4 * q4 + (c16 >> 2);
      xres[n] = trread(L + L_XIMG + pj * 8192 + row * 128 + ((w ^ (((row >> 1) & 1) | (((row >> 3) & 1) << 1))) * 32) + 8 * (c16 & 3));
    }
  }
  STAMP(12);
  {
    auto bad = [&]() { u32 mx = tag, mn = tag; FORU(n, 8) { mx = umax3(mx, g[n].y, g[n].w); mn = umin3(mn, g[n].y, g[n].w); } return __any(mx != mn); };
    if (bad()) {
#pragma nounroll
      for (int sp = 0; sp < SPIN_CAP; ++sp) {
        asm volatile("" ::: "memory");
        FORU(n, 8) s2b_ld(n);
        CNT(29, 10000);
        if (!bad()) break;
      }
    }
    STAMP(13);
    lptr tw = L + L_TRS + w * TRS_W;
    float a[4], c[4];
    FORU(t, 4) { a[t] = __uint_as_float(g[2 * t].x) + __uint_as_float(g[2 * t + 1].x); c[t] = __uint_as_float(g[2 * t].z) + __uint_as_float(g[2 * t + 1].z); }
    SB();
    const float a0 = sw32sum(a[0], a[2]), a1 = sw32sum(a[1], a[3]), c0 = sw32sum(c[0], c[2]), c1 = sw32sum(c[1], c[3]);
    const float m = sw16sum(a0, a1) * (1.f / 688), var = fmaxf(sw16sum(c0, c1) * (1.f / 688) - m * m, 0.f);
    const float r = __builtin_amdgcn_rcpf(ln_den(var)), n0 = -rbf(m) * r;
    auto gather = [&](float v, float (&o)[4]) {
      auto e = __builtin_amdgcn_permlane16_swap(__float_as_uint(v), __float_as_uint(v), false, false);
      auto e0 = __builtin_amdgcn_permlane32_swap(e[0], e[0], false, false);
      auto e1 = __builtin_amdgcn_permlane32_swap(e[1], e[1], false, false);
      o[0] = __uint_as_float(e0[0]); o[2] = __uint_as_float(e0[1]); o[1] = __uint_as_float(e1[0]); o[3] = __uint_as_float(e1[1]);
    };
    gather(r, a); gather(n0, c);
    SB();
    FORU(i, 6) z2a[i] = z2a[i] * a[i & 3] + c[i & 3];
    SB();
    FORU(i, 6) *(LAS v2u*)(tw + (16 * ((i & 3) ^ xw) + c16) * 64 + 8 * (((i & 4) + q4) ^ (c16 >> 1))) = v2u{pkbf(z2a[i].x, z2a[i].y), pkbf(z2a[i].z, z2a[i].w)};
    const u32 r0 = 8 * q4 + (c16 >> 2);
    const int zo = WS_Z2N + (pj * 6 * 64 + 2 * pi) * 1024 + lane * 16;
#pragma unroll
    for (int n = 0; n < 3; ++n) {
      lptr pp = tw + (n < 2 ? n : (w & 1)) * 2048 + r0 * 64;
      const u32 un = (n < 2 ? 0 : 4) + (c16 & 3);
      const v2u lo = trread(pp + 8 * (un ^ ((r0 >> 1) & 7))), hi = trread(pp + 256 + 8 * (un ^ (((r0 + 4) >> 1) & 7)));
      st128s(rs, zo, n < 2 ? w * 65536 + n * 1024 : (hasS ? blkS * 65536 + (w & 1) * 1024 : OOB), san4(v4u{lo.x, lo.y, hi.x, hi.y}));
    }
  }
  CFENCE();
  STAMP(14);
  u32 bsr[4], gbp[2];
  v4u gg[2];
  FORU(t, 4) bsr[t] = gld<u16>(bsp, (64 * pi + 16 * (t ^ xw) + c16) * 2);
  FORU(m, 2) gg[m] = ld128(rs, WS_MOD + ((lane & 3) * 1536 + 1280 + 32 * pj + 16 * m + 8 * (c16 >> 3) + 2 * q4 + ((c16 >> 2) & 1)) * 16);
  if (__any(fv != tag)) {
#pragma nounroll
    for (int sp = 0; sp < SPIN_CAP; ++sp) {
      asm volatile("" ::: "memory");
      f_ld();
      if (!__any(fv != tag)) break;
    }
  }
  CFENCE();
  v2u z1[6];
  f4 ga[2][4];
  v4u wo[6][4];
  const u32 wob = WS_WOP + (pj * 22 + 6 * w) * 4096, wobh = ssel(w < 3, wob, OOB);
  auto ldo = [&](int n) { wo[n >> 2][n & 3] = ld128s(rs, lane * 16 + (n & 3) * 1024, (n < 16 ? wob : wobh) + (n >> 2) * 4096); };
  {
    const u32 zo = WS_Z2N + (pj * 6 * 64 + 16 * w) * 1024;
    const u32 rot = ((nb == 6 ? 0x1420u : 0x1320u) >> (4 * (pi & 3))) & 15;
    v4u zr[16];
    u32 zv[4];
    FORU(g, 4) zv[g] = lane * 16 + rq(g) * 4096;
    u32 so[6], rsl[6];
    FORU(it, 5) {
      const u32 blk = smod(w + 1 + smod((it ? it + nb - 6 : 0) + rot, nb - 1), nb);
      so[it] = zo + blk * 65536;
      rsl[it + 1] = ssel(blk < 4, blk * 3 + ((w - blk - 1) & 3), 4 * blk - 4 + w) * 4096;
    }
    so[5] = zo + w * 65536;
    auto ldz = [&](int k, u32 s0) { zr[k] = ld128s(rs, zv[k >> 2] + (k & 3) * 1024, s0); };
    redb(2);
    epi(z1a, 2, [&](auto N) {
      constexpr int n = N.v;
      SB();
      shuf2(IC<6>{}, N);
      if constexpr (n >= 6 && n < 22) ldz(n - 6, so[0]);
      SB();
    });
    FORU(i, 6) { z1[i] = v2u{pkbf(z1a[i].x, z1a[i].y), pkbf(z1a[i].z, z1a[i].w)}; pin(z1[i]); }
    FORU(m, 2) { gbp[m] = pkbf(quadsum(__uint_as_float(gg[m].x)), quadsum(__uint_as_float(gg[m].z))); pin(gbp[m]); }
    STAMP(25);
    __syncthreads();
    {
      auto bad = [&]() { u32 m = 0; FORU(k, 16) m = vmax(m, zr[k]); return __any(m == ~0u); };
      if (bad()) {
#pragma nounroll
        for (int sp = 0; sp < SPIN_CAP; ++sp) {
          asm volatile("" ::: "memory");
          FORU(k, 16) ldz(k, so[0]);
          CNT(28, 100);
          if (!bad()) break;
        }
      }
    }
    STAMP(15);
    u32 ra = 0, badm = 0;
    float ck = 0.f;
    MM_HEAD();
    sfor<0, 96 * 4>([&](auto z) {
      constexpr int it = z.v / 64, k = (z.v / 4) % 16, t = z.v % 4, h = it & 1;
      if (k) m_va(ga[h][t], zr[k], wsf[k][t]); else m_va0(ga[h][t], zr[k], wsf[k][t]);
      constexpr int pz = z.v / 4 - 1, pit = pz / 16, pk = pz % 16;
      if constexpr (t == 1 && pz >= 0) {
        if constexpr (pit < 5) ldz(pk, so[pit + 1]);
        else ldo(pk);
      }
      if constexpr (t == 3 && it == 5 && k >= 7) ldo(8 + k);
      if constexpr (it > 0) {
        if (k == 2 && t == 2) { ck = ga[h ^ 1][0].x + ga[h ^ 1][0].y; pin(ck); }
        if (k == 2 && t == 3) { ck += ga[h ^ 1][0].z; pin(ck); }
        if (k == 3 && t == 2) { ck += ga[h ^ 1][0].w; pin(ck); }
        if (k == 3 && t == 3) badm |= __any(ck != ck) ? 1u << (it - 1) : 0u;
        if (k >= 4 && k < 12 && !(k & 1) && t == 2) { ra = rsl[it] + lane * 16 + ((((k - 4) >> 1) ^ xw) * 1024); pin(ra); }
        if constexpr (k >= 4 && k < 12 && !(k & 1) && t == 3) *(LAS f4*)(L + L_RED + ra) = ga[h ^ 1][(k - 4) >> 1];
      }
      SB();
    });
    MM_TAIL();
    { const float t = (ga[1][0].x + ga[1][0].y) + (ga[1][0].z + ga[1][0].w); badm |= __any(t != t) ? 32u : 0u; }
    if (badm) {
#pragma nounroll
      for (u32 it = 0; it < 6; ++it) {
        if (!((badm >> it) & 1)) continue;
        CNT(28, 1);
        const u32 s0 = it == 0 ? so[0] : it == 1 ? so[1] : it == 2 ? so[2] : it == 3 ? so[3] : it == 4 ? so[4] : so[5];
        const u32 r0 = it == 0 ? rsl[1] : it == 1 ? rsl[2] : it == 2 ? rsl[3] : it == 3 ? rsl[4] : rsl[5];
#pragma nounroll
        for (int sp = 0; sp < SPIN_CAP; ++sp) {
          u32 m = 0;
          FORU(k, 16) ldz(k, s0);
          FORU(k, 16) m = vmax(m, zr[k]);
          asm volatile("" ::: "memory");
          if (!__any(m == ~0u)) break;
        }
        MM_HEAD();
        FORU(k, 16) FORU(t, 4) { if (k) m_va(ga[0][t], zr[k], wsf[k][t]); else m_va0(ga[0][t], zr[k], wsf[k][t]); }
        MM_TAIL();
        FORU(t, 4) {
          if (it < 5) *(LAS f4*)(L + L_RED + r0 + lane * 16 + ((t ^ xw) * 1024)) = ga[0][t];
          else ga[1][t] = ga[0][t];
        }
      }
    }
  }
  STAMP(26);
  {
    f2 zf[12], gs2[12];
    FORU(i, 12) { zf[i] = bf2f(z1[i >> 1][i & 1]); pin(zf[i]); }
    FORU(i, 12) {
      const float bs = bflo(bsr[(i >> 1) & 3]);
      gs2[i] = i < 8 ? ((i & 1) ? f2{ga[1][i >> 1].z, ga[1][i >> 1].w} : f2{ga[1][i >> 1].x, ga[1][i >> 1].y}) + bs : f2{bs, bs};
      pin(gs2[i]);
    }
    __syncthreads();
    STAMP(16);
    f4 q[4][6];
    FORU(s, 4) FORU(i, 6) {
      if (i < 4 && s < 3) q[s][i] = *(LAS f4*)(L + L_RED + ((w * 3 + s) * 4 + (i ^ xw)) * 1024 + lane * 16);
      if (i >= 4) q[s][i] = *(LAS f4*)(L + L_RED + ((blkS * 4 - 4 + s) * 4 + ((i & 3) ^ xw)) * 1024 + lane * 16);
    }
    SB();
    FORU(s, 4) { FORU(i, 6) if (i >= 4 || s < 3) { gs2[2 * i] += f2{q[s][i].x, q[s][i].y}; gs2[2 * i + 1] += f2{q[s][i].z, q[s][i].w}; } SB(); }
    FORU(i, 12) gs2[i] *= zf[i];
    SB();
    v2u P[6];
    FORU(i, 12) P[i >> 1][i & 1] = pkbf(gs2[i]);
    SB();
#pragma unroll
    for (int h = 0; h < 3; ++h) {
      auto rx = __builtin_amdgcn_permlane16_swap(P[2 * h].x, P[2 * h + 1].x, false, false);
      auto ry = __builtin_amdgcn_permlane16_swap(P[2 * h].y, P[2 * h + 1].y, false, false);
      const u32 tt = (2 * (h & 1) + (q4 & 1)) ^ xw;
      st128s(rs, WS_PB + ((pi * 4 + tt) * 44 + blk0) * 512 + (q4 >> 1) * 256 + c16 * 16, h < 2 ? w * 512 : (hasS ? blkS * 512 : OOB),
             san4(v4u{rx[0], ry[0], rx[1], ry[1]}));
    }
  }
  STAMP(17);
  {
    f4 oa[2][4];
    v4u pr[4][6];
    u32 pb[4], pbh[4];
    u32 rd[3];
    FORU(u, 4) {
      const u32 d = u < 3 ? 1 + smod(u + smod(pj & 3, 3), 3) : 4;
      pb[u] = WS_PB + ((pi * 4 + ((w + d) & 3)) * 44 + 12 * w) * 512; pbh[u] = ssel(w < 3, pb[u] + 4096, OOB);
      if (u < 3) rd[u] = L_RED2 + (((w + d) & 3) * 3 + 3 - d) * 4096 + lane * 16;
    }
    const u32 pv3 = (w == 3 && lane >= 32) ? OOB : lane * 16 + 3072;
    auto ldp = [&](int n) {
      const int u = n / 6, k = n % 6;
      pr[u][k] = ld128s(rs, k == 3 ? pv3 : lane * 16 + (k & 3) * 1024, k < 4 ? pb[u] : pbh[u]);
    };
    const u32 bk = 12 * w + lane % 12, po = (lane < 48 && bk < 43) ? WS_PB + ((pi * 4 + lane / 12) * 44 + bk) * 512 + 508 : OOB;
    DRAIN();
    STAMP(18);
#pragma nounroll
    for (int sp = 0; sp < SPIN_CAP; ++sp) {
      asm volatile("" ::: "memory");
      const u32 pv = __builtin_amdgcn_raw_buffer_load_b32(rs, po, 0, SC1);
      FORU(n, 12) { ldp(n); SB(); }
      if (__any(pv == ~0u)) { CNT(28, 10000); continue; }
      float ck = 0.f;
      MM_HEAD();
      sfor<0, 96>([&](auto z) {
        constexpr int u = z.v / 24, k = (z.v / 4) % 6, n = z.v % 4;
        if (k) m_va(oa[u & 1][n], wo[k][n], pr[u][k]); else m_va0(oa[u & 1][n], wo[k][n], pr[u][k]);
        if constexpr (n == 1 && z.v / 4 < 12) ldp(12 + z.v / 4);
        if constexpr (u > 0 && k == 2 && n == 0) { ck += oa[(u & 1) ^ 1][0].x; pin(ck); }
        if constexpr (u > 0 && k >= 2 && n == 2) *(LAS f4*)(L + rd[u - 1] + (k - 2) * 1024) = oa[(u & 1) ^ 1][k - 2];
        SB();
      });
      MM_TAIL();
      ck += oa[1][0].x;
      if (!__any(ck != ck)) break;
      u32 m = 0;
      FORU(n, 24) m = vmax(m, aget(pr[n / 6][n % 6]));
      if (!__any(m == ~0u)) break;
      CNT(28, 1000000);
    }
    __syncthreads();
    f4 o[4];
    FORU(n, 4) {
      lptr p = L + L_RED2 + (w * 12 + n) * 1024 + lane * 16;
      o[n] = (oa[1][n] + *(LAS f4*)p) + (*(LAS f4*)(p + 4096) + *(LAS f4*)(p + 8192));
    }
    STAMP(19);
    v2u O[4];
#pragma unroll
    for (int n = 0; n < 4; ++n) {
      const u32 g0 = (n & 1) ? dppu<0x158>(gbp[n >> 1]) : dppu<0x150>(gbp[n >> 1]), g1 = (n & 1) ? dppu<0x15C>(gbp[n >> 1]) : dppu<0x154>(gbp[n >> 1]);
      O[n] = v2u{pkbf(bf2f(xres[n].x) + bf2f(g0) * f2{o[n].x, o[n].y}), pkbf(bf2f(xres[n].y) + bf2f(g1) * f2{o[n].z, o[n].w})};
    }
#pragma unroll
    for (int h = 0; h < 2; ++h) {
      auto rx = __builtin_amdgcn_permlane16_swap(O[2 * h].x, O[2 * h + 1].x, false, false);
      auto ry = __builtin_amdgcn_permlane16_swap(O[2 * h].y, O[2 * h + 1].y, false, false);
      *(v4u*)(out + (64 * pi + 16 * w + c16) * 512 + 64 * pj + 16 * (2 * h + (q4 & 1)) + 8 * (q4 >> 1)) = v4u{rx[0], ry[0], rx[1], ry[1]};
    }
  }
  if (b == 0 && tid == 0) __builtin_amdgcn_raw_buffer_store_b32(tag, rs, WS_E, 0, SC1);
  asm volatile("fk_text_b:");
  STAMP(20);
  STAMP_FLUSH();
}

extern "C" void kernel_launch(void* const* d_in, const int* in_sizes, int n_in, void* d_out, int out_size, void* d_ws, size_t ws_size,
                              hipStream_t stream) {
  (void)in_sizes; (void)n_in; (void)out_size; (void)ws_size;
  (void)hipFuncSetAttribute((const void*)fused_kernel, hipFuncAttributeMaxDynamicSharedMemorySize, LDS_BYTES);
  fused_kernel<<<256, 256, LDS_BYTES, stream>>>((const u16*)d_in[0], (const u16*)d_in[1], (const u16*)d_in[2], (const u16*)d_in[3],
                                                (const u16*)d_in[4], (const u16*)d_in[5], (const u16*)d_in[6], (const u16*)d_in[8],
                                                (const u16*)d_in[9], (const u16*)d_in[10], (const u16*)d_in[11], (const u16*)d_in[12],
                                                (const u16*)d_in[13], (u16*)d_out, (unsigned char*)d_ws);
}
```

```cpp
#include <hip/hip_runtime.h>
#include <stdint.h>

typedef unsigned u32;
typedef unsigned short u16;
typedef unsigned long long u64;
typedef u32 v4u __attribute__((ext_vector_type(4)));
typedef u32 v2u __attribute__((ext_vector_type(2)));
typedef float f4 __attribute__((ext_vector_type(4)));
typedef float f2 __attribute__((ext_vector_type(2)));
typedef __bf16 bf8 __attribute__((ext_vector_type(8)));
typedef __bf16 bf2 __attribute__((ext_vector_type(2)));
typedef _Float16 h8 __attribute__((ext_vector_type(8)));
typedef _Float16 h2 __attribute__((ext_vector_type(2)));
typedef short s4 __attribute__((ext_vector_type(4)));
#define LAS __attribute__((address_space(3)))
#define GAS __attribute__((address_space(1)))
#define CAS __attribute__((address_space(4)))
#define DI __device__ __forceinline__
typedef __amdgpu_buffer_rsrc_t rsrc_t;
typedef LAS unsigned char* lptr;

#define XMAP 2

constexpr int WS_E = 0, WS_STAT1 = 4096, WS_CD = 32768, WS_ST2 = 65536, WS_F3 = 327680, WS_MOD = 524288, WS_BWP = 655360;
constexpr int WS_XG = 1 << 20, WS_X1 = 5 << 20, WS_Z2N = 7 << 20, WS_PB = 10 << 20, WS_WINP = 13 << 20, WS_WOP = 29 << 19;
constexpr int WS_DBG = 24 << 20, WS_END = 25 << 20;
constexpr int OOB = 0x40000000;
constexpr int TABP = 544, TAB_CH = 63 * TABP;
constexpr int L_TAB = 0, L_Y = 0, Y_CH = 21504;
constexpr int L_PKW = 75776, L_SHF = 81920;
constexpr int L_PKI = 68544;
constexpr int L_TOE = 108544, TOE_C = 5312, TOE_CH = 4 * TOE_C;
constexpr int L_U = 151040, U_CH = 6320;
constexpr int L_XIMG = 0, L_TRS = 65536, TRS_W = 4096;
constexpr int L_RED = 0, L_RED2 = 81920;
constexpr int L_XST = 159744, L_ZST = L_XST + 1024;
constexpr int LDS_BYTES = 163840;
constexpr int SC1 = 16;
constexpr int SPIN_CAP = 1 << 15;
static_assert(LDS_BYTES <= 163840, "LDS");

DI float bflo(u32 v) { return __uint_as_float(v << 16); }
DI float bfhi(u32 v) { return __uint_as_float(v & 0xffff0000u); }
DI f2 bf2f(u32 v) { return f2{bflo(v), bfhi(v)}; }
DI u32 pkbf(float a, float b) { f2 v = {a, b}; return __builtin_bit_cast(u32, __builtin_convertvector(v, bf2)); }
DI u32 pkbf(f2 v) { return __builtin_bit_cast(u32, __builtin_convertvector(v, bf2)); }
DI u32 pkh(f2 v) { return __builtin_bit_cast(u32, __builtin_convertvector(v, h2)); }
DI float rbf(float a) { return bflo(pkbf(a, a)); }
DI u32 san(u32 v) { return v < 0xFFFFFFFEu ? v : 0xFFFFFFFEu; }
DI v4u san4(v4u v) { return v4u{san(v.x), san(v.y), san(v.z), san(v.w)}; }
DI u32 umax3(u32 a, u32 b, u32 c) { return max(max(a, b), c); }
DI u32 umin3(u32 a, u32 b, u32 c) { return min(min(a, b), c); }
DI u32 vmax(u32 m, v4u v) { return umax3(umax3(m, v.x, v.y), v.z, v.w); }
DI v4u ld128(rsrc_t rs, int off) { return __builtin_amdgcn_raw_buffer_load_b128(rs, off, 0, SC1); }
DI v4u ld128s(rsrc_t rs, int off, int soff) { return __builtin_amdgcn_raw_buffer_load_b128(rs, off, soff, SC1); }
DI void st128(rsrc_t rs, int off, v4u v) { __builtin_amdgcn_raw_buffer_store_b128(v, rs, off, 0, SC1); }
DI void st128s(rsrc_t rs, int off, int soff, v4u v) { __builtin_amdgcn_raw_buffer_store_b128(v, rs, off, soff, SC1); }
DI void st64(rsrc_t rs, int off, v2u v) { __builtin_amdgcn_raw_buffer_store_b64(v, rs, off, 0, SC1); }
#define DRAIN() asm volatile("s_waitcnt vmcnt(0)" ::: "memory")
#define SB() __builtin_amdgcn_sched_barrier(0)
#define CFENCE() do { asm volatile("" ::: "memory"); SB(); } while (0)
template <int C> DI u32 dppu(u32 v) { return (u32)__builtin_amdgcn_update_dpp(0, (int)v, C, 0xF, 0xF, true); }
template <int C> DI float dppadd(float v) { return v + __uint_as_float(dppu<C>(__float_as_uint(v))); }
DI float sw16sum(float a, float b) { auto r = __builtin_amdgcn_permlane16_swap(__float_as_uint(a), __float_as_uint(b), false, false); return __uint_as_float(r[0]) + __uint_as_float(r[1]); }
DI float sw32sum(float a, float b) { auto r = __builtin_amdgcn_permlane32_swap(__float_as_uint(a), __float_as_uint(b), false, false); return __uint_as_float(r[0]) + __uint_as_float(r[1]); }
DI float quadsum(float v) { v = dppadd<0xB1>(v); return dppadd<0x4E>(v); }
DI float rowsum(float v) { v = quadsum(v); v = dppadd<0x141>(v); return dppadd<0x140>(v); }
DI f4 mfma_bf(v4u a, v4u b, f4 c) { return __builtin_amdgcn_mfma_f32_16x16x32_bf16(__builtin_bit_cast(bf8, a), __builtin_bit_cast(bf8, b), c, 0, 0, 0); }
DI f4 mfma_h(v4u a, v4u b, f4 c) { return __builtin_amdgcn_mfma_f32_16x16x32_f16(__builtin_bit_cast(h8, a), __builtin_bit_cast(h8, b), c, 0, 0, 0); }
DI v2u trread(lptr p) { return __builtin_bit_cast(v2u, __builtin_amdgcn_ds_read_tr16_b64_v4i16((LAS s4*)p)); }
template <int N> struct IC { static constexpr int v = N; };
template <int I, int N, class F> DI void sfor(F f) { if constexpr (I < N) { f(IC<I>{}); sfor<I + 1, N>(f); } }
#define FORU(i, n) _Pragma("unroll") for (int i = 0; i < (n); ++i)
DI f2 cm1(f2 a, f2 b) { f2 t; asm("v_pk_mul_f32 %0, %1, %2 op_sel:[0,0] op_sel_hi:[0,1]" : "=v"(t) : "v"(a), "v"(b)); return t; }
DI f2 cm2(f2 a, f2 b, f2 t) { f2 r; asm("v_pk_fma_f32 %0, %1, %2, %3 op_sel:[1,1,0] op_sel_hi:[1,0,1] neg_lo:[0,1,0]" : "=v"(r) : "v"(a), "v"(b), "v"(t)); return r; }
DI f2 cexp_p(float al2, float rev, float p) {
  float m = __builtin_amdgcn_exp2f(al2 * p), f = __builtin_amdgcn_fractf(rev * p);
  return f2{m * __builtin_amdgcn_cosf(f), m * __builtin_amdgcn_sinf(f)};
}
template <int N, class F> DI void gelu_n(f2 (&v)[N], F fill) {
  const float c0 = -2.0f * 0.7978845608028654f * 1.4426950408889634f, c1 = c0 * 0.044715f;
  f2 a[N];
  sfor<0, 2>([&](auto z) { FORU(i, 6) a[6 * z.v + i] = v[6 * z.v + i] * v[6 * z.v + i]; fill(IC<4 + z.v>{}); });
  sfor<0, 2>([&](auto z) { FORU(i, 6) a[6 * z.v + i] = __builtin_elementwise_fma(a[6 * z.v + i], f2{c1, c1}, f2{c0, c0}); fill(IC<6 + z.v>{}); });
  sfor<0, 2>([&](auto z) { FORU(i, 6) a[6 * z.v + i] *= v[6 * z.v + i]; fill(IC<8 + z.v>{}); });
  sfor<0, 6>([&](auto z) { FORU(i, 2) a[2 * z.v + i] = f2{__builtin_amdgcn_exp2f(a[2 * z.v + i].x), __builtin_amdgcn_exp2f(a[2 * z.v + i].y)}; fill(IC<10 + z.v>{}); });
  sfor<0, 2>([&](auto z) { FORU(i, 6) a[6 * z.v + i] += 1.0f; fill(IC<16 + z.v>{}); });
  sfor<0, 6>([&](auto z) { FORU(i, 2) a[2 * z.v + i] = f2{__builtin_amdgcn_rcpf(a[2 * z.v + i].x), __builtin_amdgcn_rcpf(a[2 * z.v + i].y)}; fill(IC<18 + z.v>{}); });
  sfor<0, 2>([&](auto z) { FORU(i, 6) v[6 * z.v + i] *= a[6 * z.v + i]; fill(IC<24 + z.v>{}); });
}
DI float ln_den(float var) { return rbf(__builtin_amdgcn_sqrtf(rbf(rbf(var) + 1e-5f))); }
template <class T> DI T gld(const void* base, u32 off) { return *(const GAS T*)((const GAS char*)base + off); }
#define MF "v_mfma_f32_16x16x32_bf16 "
DI void m_av(f4& c, v4u a, v4u b) { asm volatile(MF "%0, %1, %2, %0" : "+v"(c) : "a"(a), "v"(b)); }
DI void m_va(f4& c, v4u a, v4u b) { asm volatile(MF "%0, %1, %2, %0" : "+v"(c) : "v"(a), "a"(b)); }
DI void m_vv(f4& c, v4u a, v4u b) { asm volatile(MF "%0, %1, %2, %0" : "+v"(c) : "v"(a), "v"(b)); }
DI void m_av0(f4& c, v4u a, v4u b) { asm volatile(MF "%0, %1, %2, 0" : "=&v"(c) : "a"(a), "v"(b)); }
DI void m_va0(f4& c, v4u a, v4u b) { asm volatile(MF "%0, %1, %2, 0" : "=&v"(c) : "v"(a), "a"(b)); }
DI void m_vv0(f4& c, v4u a, v4u b) { asm volatile(MF "%0, %1, %2, 0" : "=&v"(c) : "v"(a), "v"(b)); }
DI v4u aget(v4u a) {
  v4u r;
  _Pragma("unroll") for (int e = 0; e < 4; ++e) { u32 t; asm volatile("v_accvgpr_read_b32 %0, %1" : "=v"(t) : "a"(a[e])); r[e] = t; }
  return r;
}
DI u32 ssel(bool c, u32 a, u32 b) { asm("" : "+s"(a)); asm("" : "+s"(b)); return c ? a : b; }
DI u32 smod(u32 v, u32 m) { u32 t = v - m; asm("" : "+s"(t)); return (int)t >= 0 ? t : v; }
template <class T> DI void pin(T& v) { asm volatile("" : "+v"(v)); }
#define MM_HEAD() do { SB(); asm volatile("s_nop 7"); SB(); } while (0)
#define MM_TAIL() do { SB(); asm volatile("s_nop 7"); SB(); } while (0)
DI int toe_addr(int pos) { return 2 * pos + 8 * (pos >> 4); }

#define DA 0
#define DB 1
#define STAMP(n) do { if ((n) == DA || (n) == DB) { SB(); u32 t_ = (u32)__builtin_amdgcn_s_memrealtime(); if ((n) == DA) dta = t_; else dtb = t_; SB(); } } while (0)
#define STAMP_FLUSH() do { u32 d_ = min(2u * (dtb - dta), 20000u), s_ = (u32)__builtin_amdgcn_s_memrealtime(); for (int q_ = 0; q_ < 20000; ++q_) { if ((u32)__builtin_amdgcn_s_memrealtime() - s_ >= d_) break; __builtin_amdgcn_s_sleep(1); } } while (0)
#define CNT(n,v)
#define STAMP_INIT() u32 dta = 0, dtb = 0

template <int BAND> struct Band {
  static constexpr int R = 128 - 32 * BAND, NT = R / 16, NS = 15 + NT / 2;
  static constexpr int YO = BAND == 0 ? 0 : BAND == 1 ? 8448 : BAND == 2 ? 14848 : 19200;
  lptr al, ah, bb, yp;
  f4 acc[NT];
  v2u lo[32], hi[32];
  v4u bf[NS];
  DI void init(lptr L, int ch, int lane) {
    const int r = lane & 15, q = lane >> 4, c = (3 - r) & 3;
    const int P = 2048 - 512 * BAND - 496 - 4 * (r >> 2) + 8 * q;
    al = L + L_TOE + ch * TOE_CH + c * TOE_C + toe_addr(P);
    ah = L + L_TOE + ch * TOE_CH + c * TOE_C + toe_addr(P + 4);
    bb = L + L_U + ch * U_CH + 80 * (R / 32) * r + 16 * q;
    yp = L + L_Y + ch * Y_CH + YO + r * (4 * R + 16) + 16 * q;
  }
  DI void load(int s, int i) {
    if (i < 4) {
      const int n = 2 * s + (i >> 1);
      if (n < 32) { if (i & 1) hi[n] = *(LAS v2u*)(ah + 40 * (31 - n)); else lo[n] = *(LAS v2u*)(al + 40 * (31 - n)); }
    } else if (s < NS) bf[s] = *(LAS v4u*)(bb + 80 * (NT / 2 - 1 - s + 15));
  }
  DI void mma(int s, int t) {
    const int n = t - NT + 2 + 2 * s;
    if (s < NS && n >= 0 && n < 32) {
      if (n < 2) m_vv0(acc[t], v4u{lo[n].x, lo[n].y, hi[n].x, hi[n].y}, bf[s]); else m_vv(acc[t], v4u{lo[n].x, lo[n].y, hi[n].x, hi[n].y}, bf[s]);
    }
  }
  DI void flush(int s, int t) {
    const int n = t - NT + 2 + 2 * (s - 1);
    if (n == 30 || n == 31) *(LAS f4*)(yp + 64 * t) = acc[t];
  }
};
template <int B1, int B2, class F> DI void conv2(lptr L, int ch, int lane, F side) {
  Band<B1> u; Band<B2> v;
  u.init(L, ch, lane); v.init(L, ch, lane);
  constexpr int N1 = Band<B1>::NT, NS = Band<B1>::NS;
#pragma unroll
  for (int s = 0; s < 2; ++s)
#pragma unroll
    for (int i = 0; i < 5; ++i) { u.load(s, i); v.load(s, i); SB(); }
  sfor<0, 10 * (NS + 1)>([&](auto z) {
    constexpr int s = z.v / 10, i = z.v % 10;
    if (s >= 17 && i == 0) asm volatile("s_nop %0" ::"i"(s == NS ? 7 : s == NS - 1 ? 5 : 2));
    if (i < N1) { u.mma(s, i); u.flush(s, i); } else { v.mma(s, i - N1); v.flush(s, i - N1); }
    if (i < 5) u.load(s + 2, i); else v.load(s + 2, i - 5);
    side(IC<s>{}, IC<i>{});
    SB();
  });
}

__global__ void __launch_bounds__(256, 1)
fused_kernel(const u16* __restrict__ x, const u16* __restrict__ lar, const u16* __restrict__ aim, const u16* __restrict__ cre,
             const u16* __restrict__ cim, const u16* __restrict__ ldt, const u16* __restrict__ dsk, const u16* __restrict__ win,
             const u16* __restrict__ wsp, const u16* __restrict__ bsp, const u16* __restrict__ wout, const u16* __restrict__ cvec,
             const u16* __restrict__ wmod, u16* __restrict__ out, unsigned char* ws) {
  extern __shared__ __attribute__((aligned(16))) unsigned char smem[];
  lptr L = (lptr)smem;
  const u32 b = blockIdx.x, tid = threadIdx.x, lane = tid & 63;
  STAMP_INIT();
  STAMP(0);
  const u32 w = __builtin_amdgcn_readfirstlane(tid >> 6);
  const u32 xc = b & 7, rr = b >> 3, cp = xc * 32 + rr;
  const u32 c16 = lane & 15, q4 = lane >> 4;
  const u32 tag = *(const CAS u32*)(ws + WS_E) + 1u;
  const u32 tb = 512 * (b >> 6) + 32 * (b & 15) + 2 * ((b >> 4) & 3);
  const v4u sr0 = gld<v4u>(x, (tb + 8 * w) * 1024 + lane * 16), sr1 = gld<v4u>(x, (tb + 8 * w) * 1024 + 1024 + lane * 16);
  const u32 xgo = (tb + 8 * (lane & 3)) * 1024 + (16 * w + (lane >> 2)) * 16;
  const v4u xg0 = gld<v4u>(x, xgo), xg1 = gld<v4u>(x, xgo + 1024);
  const u32 mG = b % 48, mP = min(b / 48, 3u), mrow = mP * 128 + (lane >> 1);
  v4u wm[4];
  u32 cv[4];
#pragma unroll
  for (int n = 0; n < 4; ++n) wm[n] = gld<v4u>(wmod, (mrow + 32 * n) * 6144 + mG * 128 + w * 32 + (lane & 1) * 16);
#pragma unroll
  for (int n = 0; n < 4; ++n) cv[n] = gld<u16>(cvec, (mrow + 32 * n) * 2);
  const u32 pidx = (cp * 256 + tid) * 2;
  const u32 p_lar = gld<u16>(lar, pidx), p_aim = gld<u16>(aim, pidx), p_cr = gld<u16>(cre, pidx), p_ci = gld<u16>(cim, pidx);
  const u32 p_cr2 = gld<u16>(cre, pidx ^ 128), p_ci2 = gld<u16>(cim, pidx ^ 128);
  const u32 p_ldt = *(const CAS u32*)(ldt + 2 * cp), p_dsk = *(const CAS u32*)(dsk + 2 * cp);
  CFENCE();
  u32 pf0 = 0, pf1 = 0;
  {
    u64 pc;
    u32 len;
    asm volatile("s_getpc_b64 %0\nfk_text_a:\n\ts_mov_b32 %1, fk_text_b-fk_text_a" : "=s"(pc), "=s"(len));
    const u32 o = (rr * 16 + tid) * 128;
    if (tid < 16 && o < len) pf0 = gld<u32>((const void*)pc, o);
  }
  CFENCE();
  const u32 pi = (xc >> 1) * 8 + (rr & 7), pj = (xc & 1) * 4 + (rr >> 3);
  const u32 nb = pj < 3 ? 6 : 5, blk0 = 5 * pj + min(pj, 3u);
  const bool two = w + 4 < nb;
  const rsrc_t rs = __builtin_amdgcn_make_buffer_rsrc((void*)ws, 0, WS_END, 0x00020000);
  const v4u SENT = {~0u, ~0u, ~0u, ~0u};
  const int x1w = WS_X1 + ((tid >> 3) * 512 + 2 * cp) * 128 + (tid & 7) * 16;
  const u32 wio_src = ((32 * (pi >> 1) + (lane >> 1)) * 1376 + (pi & 1) * 688 + 16 * (blk0 + w)) * 2 + (lane & 1) * 16;
  const v4u wi0 = gld<v4u>(win, wio_src), wi1 = gld<v4u>(win, two ? wio_src + 128 : wio_src);
  const rsrc_t ro = __builtin_amdgcn_make_buffer_rsrc((void*)wout, 0, 688 * 1024, 0x00020000);
  const v4u wo0 = __builtin_amdgcn_raw_buffer_load_b128(ro, (lane >> 1) * 1024 + (lane & 1) * 16, b < 176 ? (b % 22) * 32768 + (b / 22) * 128 + w * 32 : OOB, 0);
  CFENCE();
  if (tid < 40) *(LAS v4u*)(L + L_TOE + (tid / 5) * TOE_C + 5120 + (tid % 5) * 16) = v4u{0, 0, 0, 0};
  if (tid < 150) *(LAS v4u*)(L + L_U + (tid / 75) * U_CH + (tid % 75) * 16) = v4u{0, 0, 0, 0};
  STAMP(23);
  {
    f2 a[8];
#pragma unroll
    for (int e = 0; e < 4; ++e) { a[2 * e] = f2{bflo(sr0[e]), bflo(sr1[e])}; a[2 * e + 1] = f2{bfhi(sr0[e]), bfhi(sr1[e])}; }
    f2 s = ((a[0] + a[1]) + (a[2] + a[3])) + ((a[4] + a[5]) + (a[6] + a[7]));
    float t = rowsum(sw32sum(s.x, s.y));
    t = sw16sum(t, t) * (1.f / 512);
    auto mm = __builtin_amdgcn_permlane32_swap(__float_as_uint(t), __float_as_uint(t), false, false);
    const f2 m = {__uint_as_float(mm[0]), __uint_as_float(mm[1])};
    s = f2{0.f, 0.f};
#pragma unroll
    for (int e = 0; e < 8; ++e) { const f2 u = a[e] - m; s = __builtin_elementwise_fma(u, u, s); }
    float v = rowsum(sw32sum(s.x, s.y));
    v = sw16sum(v, v) * (1.f / 512);
    st64(rs, (lane & 31) == 0 ? WS_STAT1 + b * 64 + w * 16 + (lane >> 5) * 8 : OOB, v2u{pkbf(t, ln_den(v)), tag});
  }
  {
    const int o = WS_XG + (4 * (16 * w + (lane >> 2))) * 16384 + b * 64 + (lane & 3) * 16;
    st128(rs, o, v4u{xg0.x, tag, xg1.x, tag});
    st128s(rs, o, 16384, v4u{xg0.y, tag, xg1.y, tag});
    st128s(rs, o, 32768, v4u{xg0.z, tag, xg1.z, tag});
    st128s(rs, o, 49152, v4u{xg0.w, tag, xg1.w, tag});
  }
  CFENCE();
  STAMP(22);
  {
    f2 a[4];
#pragma unroll
    for (int n = 0; n < 4; ++n) {
      const float c = bflo(cv[n]);
      const float s = rbf(c * __builtin_amdgcn_rcpf(1.f + __builtin_amdgcn_exp2f(-1.4426950408889634f * c)));
#pragma unroll
      for (int e = 0; e < 4; ++e) a[e] = n ? a[e] + bf2f(wm[n][e]) * s : bf2f(wm[n][e]) * s;
    }
    const float u0 = sw32sum(a[0].x, a[2].x), u1 = sw32sum(a[0].y, a[2].y), u2 = sw32sum(a[1].x, a[3].x), u3 = sw32sum(a[1].y, a[3].y);
    float t0 = sw16sum(u0, u2), t1 = sw16sum(u1, u3);
    t0 = dppadd<0x4E>(dppadd<0x124>(dppadd<0x128>(t0)));
    t1 = dppadd<0x4E>(dppadd<0x124>(dppadd<0x128>(t1)));
    st128(rs, (c16 < 2 && b < 192) ? WS_MOD + (mP * 1536 + mG * 32 + w * 8 + c16 * 4 + q4) * 16 : OOB, v4u{__float_as_uint(t0), tag, __float_as_uint(t1), tag});
  }
  CFENCE();
  STAMP(1);
  const u32 xw = 2 * (w & 1) + (w >> 1);
  const int wio = WS_WINP + (((pj * 2 + (pi & 1)) * 6 + w) * 16 + (pi >> 1)) * 1024 + lane * 16;
  const u32 pf2 = gld<u32>(wsp, (64 * pi + 8 * pj) * 4096 + tid * 128);
  st128(rs, x1w, SENT);
  st128(rs, x1w + 128, SENT);
  CFENCE();
  const rsrc_t rw = __builtin_amdgcn_make_buffer_rsrc((void*)wsp, 0, 2048 * 4096, 0x00020000);
  const u32 wvo = (lane >> 3) * 4096 + (lane & 7) * 16;
  const u32 wsb[4] = {(64 * pi + 16 * xw) * 4096 + w * 1024, (64 * pi + 16 * (xw ^ 1)) * 4096 + w * 1024,
                      (64 * pi + 16 * (xw ^ 2)) * 4096 + w * 1024, (64 * pi + 16 * (xw ^ 3)) * 4096 + w * 1024};
  v4u wraw[64], wsf[16][4];
  auto rq = [&](int g) { return (g + pj) & 3; };
  auto ldw = [&](int n) { wraw[n] = __builtin_amdgcn_raw_buffer_load_b128(rw, (wvo + rq(n >> 4) * 256) + ((n >> 3) & 1) * 128, wsb[(n & 7) >> 1] + (n & 1) * 32768, 0); };
  const u32 wwa = (u32)(size_t)L + L_SHF + w * 8192 + (lane >> 3) * 128 + ((lane & 7) ^ (lane >> 3)) * 16;
  auto shuf = [&](auto Z) {
    constexpr int c = Z.v >> 4, j = Z.v & 15, i = j & 7;
    if constexpr (j < 8) asm volatile("ds_write_b128 %0, %1 offset:%2" ::"v"(wwa), "a"(wraw[8 * c + i]), "i"(i * 1024) : "memory");
    else wsf[2 * c + (i & 1)][i >> 1] = *(LAS v4u*)(L + L_SHF + w * 8192 + (i >> 1) * 2048 + c16 * 128 + (((4 * (i & 1) + q4) ^ (c16 & 7)) * 16));
  };
  float kscale;
  {
    const float L2E = 1.4426950408889634f;
    const float dt = rbf(__builtin_amdgcn_exp2f(((w >> 1) ? bfhi(p_ldt) : bflo(p_ldt)) * L2E));
    const float al2 = (-rbf(__builtin_amdgcn_exp2f(bflo(p_lar) * L2E)) * dt) * L2E;
    const float rev = (bflo(p_aim) * dt) * 0.15915494309189535f;
    u32 mx = max(max(p_cr & 0x7fffu, p_ci & 0x7fffu), max(p_cr2 & 0x7fffu, p_ci2 & 0x7fffu));
    mx = max(mx, dppu<0xB1>(mx)); mx = max(mx, dppu<0x4E>(mx)); mx = max(mx, dppu<0x141>(mx)); mx = max(mx, dppu<0x140>(mx));
    { auto r = __builtin_amdgcn_permlane16_swap(mx, mx, false, false); mx = max(r[0], r[1]); }
    { auto r = __builtin_amdgcn_permlane32_swap(mx, mx, false, false); mx = max(r[0], r[1]); }
    u32 ex = mx >> 7;
    ex = min(max(ex, 1u), 253u);
    const float dn = __uint_as_float((254u - ex) << 23), up = __uint_as_float(ex << 23);
    kscale = 2.0f * dt * up;
    LAS u32* tp = (LAS u32*)(L + L_TAB + (tid >> 7) * TAB_CH + (tid & 127) * 4);
    f2 U[17], Y[16], V[8], X[31], wp[4], t[31];
    u32 pk[31];
    wp[0] = cexp_p(al2, rev, 1.f);
    Y[1] = cexp_p(al2, -rev, 16.f);
    V[1] = cexp_p(al2, rev, 256.f);
    Y[0] = f2{1.f, 0.f};
    U[0] = f2{bflo(p_cr) * dn, bflo(p_ci) * dn};
    SB();
    FORU(l, 4) {
      const int h = 1 << l;
      FORU(i, h) t[i] = cm1(U[i], wp[l]);
      if (l < 3) { t[8] = cm1(wp[l], wp[l]); t[9] = cm1(Y[h], Y[h]); }
      if (l < 2) t[10] = cm1(V[h], V[h]);
      FORU(i, h - 1) t[11 + i] = cm1(Y[1 + i], Y[h]);
      if (l < 3) { FORU(i, h - 1) t[18 + i] = cm1(V[1 + i], V[h]); }
      SB();
      FORU(i, h) U[h + i] = cm2(U[i], wp[l], t[i]);
      if (l < 3) { wp[l + 1] = cm2(wp[l], wp[l], t[8]); Y[2 * h] = cm2(Y[h], Y[h], t[9]); }
      if (l < 2) V[2 * h] = cm2(V[h], V[h], t[10]);
      FORU(i, h - 1) Y[h + 1 + i] = cm2(Y[1 + i], Y[h], t[11 + i]);
      if (l < 3) { FORU(i, h - 1) V[h + 1 + i] = cm2(V[1 + i], V[h], t[18 + i]); }
      SB();
    }
    auto xa = [](int i) { return i < 9 ? 1 : i < 19 ? 2 + (i - 9) / 5 : 4 + (i - 19) / 3; };
    auto xo = [](int i) { return i < 9 ? 2 * i : i < 19 ? 4 * ((i - 9) % 5) : 8 * ((i - 19) % 3); };
    u32 py[16], pu[16];
    t[0] = cm1(U[8], wp[3]);
    FORU(i, 16) { py[i] = pkh(Y[i]); if (i == 2) U[16] = cm2(U[8], wp[3], t[0]); if (i & 1) tp[(47 + (i >> 1)) * (TABP / 4)] = py[i >> 1]; SB(); }
    FORU(i, 16) { pu[i] = pkh(U[i]); if (i & 1) tp[(55 + (i >> 1)) * (TABP / 4)] = py[8 + (i >> 1)]; SB(); }
    FORU(i, 31) { t[i] = cm1(U[xo(i)], V[xa(i)]); if (!(i & 1)) tp[(i >> 1) * (TABP / 4)] = pu[i >> 1]; SB(); }
    FORU(i, 35) {
      if (i < 31) X[i] = cm2(U[xo(i)], V[xa(i)], t[i]);
      if (i >= 2 && i < 33) pk[i - 2] = pkh(X[i - 2]);
      if (i >= 4) tp[(12 + i) * (TABP / 4)] = pk[i - 4];
      SB();
    }
  }
  *(LAS v4u*)(L + L_PKI + w * 1024 + lane * 16) = wi0;
  if (w < 2) *(LAS v4u*)(L + L_PKI + 4096 + w * 1024 + lane * 16) = wi1;
  *(LAS v4u*)(L + L_PKW + w * 8192 + lane * 16) = wo0;
  v4u gx[4], gs[4], gm[3];
  const int mo = WS_MOD + ((lane & 3) * 1536 + 16 * (pi >> 1) + (lane >> 2)) * 16;
  const int mo2 = WS_MOD + ((lane & 3) * 1536 + 256 * min(lane >> 2, 2u) + cp) * 16;
  auto s1_ld = [&](int n) {
    if (n < 4) gx[n] = ld128s(rs, lane * 16 + n * 1024, WS_XG + cp * 16384 + w * 4096);
    else if (n < 8) gs[n - 4] = ld128s(rs, lane * 16 + (n - 4) * 1024, WS_STAT1 + w * 4096);
    else gm[n - 8] = n == 8 ? ld128(rs, mo2) : ld128s(rs, mo, (n == 9 ? 768 : 1024) * 16);
  };
  FORU(n, 3) s1_ld(n);
  __syncthreads();
  STAMP(2);
  f4 kacc[4];
  v2u pl[3], ph[3];
  {
    const u32 hi = w & 1;
    lptr tb_ = L + L_TAB + (w >> 1) * TAB_CH + 16 * q4;
    const u32 i2 = (c16 + ((c16 >> 1) & 1)) >> 1, i4 = (c16 + 1 + ((c16 >> 2) & 1)) >> 2, i8 = (c16 + 3 + ((c16 >> 3) & 1)) >> 3;
    const u32 row[4] = {hi ? 35 + i8 : c16, hi ? 38 + i8 : 16 + i2, hi ? 41 + i8 : 25 + i4, hi ? 44 + i8 : 30 + i4};
    v4u yf[8], xf[8][4];
    auto ldk = [&](int k, int i) {
      if (k < 8) { if (i < 4) xf[k][i] = *(LAS v4u*)(tb_ + row[i] * TABP + 64 * k); else yf[k] = *(LAS v4u*)(tb_ + (47 + c16) * TABP + 64 * k); }
    };
    FORU(k, 2) FORU(i, 5) ldk(k, 4 - i);
    SB();
    const u32 po = (8 * q4 + (c16 >> 2)) * 32 + 8 * (c16 & 3);
    FORU(k, 8) FORU(a, 4) {
      if (k) asm volatile("v_mfma_f32_16x16x32_f16 %0, %1, %2, %0" : "+v"(kacc[3 - a]) : "v"(xf[k][3 - a]), "v"(yf[k]));
      else asm volatile("v_mfma_f32_16x16x32_f16 %0, %1, %2, 0" : "=&v"(kacc[3 - a]) : "v"(xf[k][3 - a]), "v"(yf[k]));
      if (a == 0) ldk(k + 2, 4);
      ldk(k + 2, 3 - a);
      if (k < 4 && (a & 1)) s1_ld(3 + 2 * k + (a >> 1));
      if (k >= 6 && a < 3) {
        lptr p = L + (a == 0 ? L_PKI + w * 1024 : a == 1 ? L_PKI + 4096 + (w & 1) * 1024 : L_PKW + w * 8192) + po;
        if (k == 6) pl[a] = trread(p); else ph[a] = trread(p + 128);
      }
      SB();
    }
  }
  MM_TAIL();
  f2 pv[8];
  {
    const int i0 = 2044 - (1024 * (w & 1) + 768 + 16 * c16 + 4 * q4);
    lptr t0 = L + L_TOE + (w >> 1) * TOE_CH + toe_addr(i0), t1 = L + L_TOE + (w >> 1) * TOE_CH + toe_addr(i0 + 4);
    u32 P0[4], P1[4], M[4];
    FORU(a, 4) kacc[a] *= kscale;
    SB();
    FORU(a, 4) { P0[a] = pkbf(kacc[a].w, kacc[a].z); P1[a] = pkbf(kacc[a].y, kacc[a].x); M[a] = pkbf(kacc[a].z, kacc[a].y); }
    SB();
    FORU(a, 4) {
      lptr p0 = t0 + 640 * (3 - a), p1 = t1 + 640 * (3 - a);
      *(LAS v2u*)(p1) = v2u{P0[a], P1[a]};
      *(LAS u16*)(p0 + TOE_C + 6) = (u16)P0[a]; *(LAS u32*)(p1 + TOE_C) = M[a]; *(LAS u16*)(p1 + TOE_C + 4) = (u16)(P1[a] >> 16);
      *(LAS u32*)(p0 + 2 * TOE_C + 4) = P0[a]; *(LAS u32*)(p1 + 2 * TOE_C) = P1[a];
      *(LAS u16*)(p0 + 3 * TOE_C + 2) = (u16)P0[a]; *(LAS u32*)(p0 + 3 * TOE_C + 4) = M[a]; *(LAS u16*)(p1 + 3 * TOE_C) = (u16)(P1[a] >> 16);
      FORU(j, 2) { pv[2 * a + j] = bf2f((a & 1) ? ph[a >> 1][j] : pl[a >> 1][j]); pin(pv[2 * a + j]); }
      SB();
    }
    st128s(rs, lane * 16, b < 176 ? WS_WOP + (b * 4 + w) * 1024 : OOB, v4u{pl[2].x, pl[2].y, ph[2].x, ph[2].y});
  }
  asm volatile("" ::"v"(pf0), "v"(pf2));
  STAMP(3);
  auto s1_bad = [&]() {
    u32 mx = umax3(gm[0].y, gm[0].w, tag), mn = umin3(gm[0].y, gm[0].w, tag);
    FORU(k, 2) { mx = umax3(mx, gm[1 + k].y, gm[1 + k].w); mn = umin3(mn, gm[1 + k].y, gm[1 + k].w); }
    FORU(k, 4) {
      mx = umax3(umax3(mx, gx[k].y, gx[k].w), gs[k].y, gs[k].w);
      mn = umin3(umin3(mn, gx[k].y, gx[k].w), gs[k].y, gs[k].w);
    }
    return __any(mx != mn);
  };
  if (s1_bad()) {
#pragma nounroll
    for (int sp = 0; sp < SPIN_CAP; ++sp) {
      asm volatile("" ::: "memory");
      FORU(n, 11) s1_ld(n);
      CNT(29, 1);
      if (!s1_bad()) break;
    }
  }
  STAMP(4);
  u32 xv[8];
  u32 h0[4], h1[4];
  u32 gav;
  {
    float qs[6];
    f2 m[4], r[4], a[4], c[4];
    int wn = 0;
    auto fill = [&]() { SB(); ldw(wn); ++wn; SB(); };
    FORU(i, 6) qs[i] = dppadd<0xB1>(__uint_as_float((i & 1) ? gm[i >> 1].z : gm[i >> 1].x));
    FORU(k, 4) { xv[2 * k] = gx[k].x; xv[2 * k + 1] = gx[k].z; r[k] = f2{bfhi(gs[k].x), bfhi(gs[k].z)}; }
    fill();
    FORU(i, 6) qs[i] = dppadd<0x4E>(qs[i]);
    FORU(k, 4) m[k] = f2{bflo(gs[k].x), bflo(gs[k].z)};
    fill();
    const u32 mv = pkbf(qs[0], qs[1]), sbp = pkbf(qs[2], qs[3]), cbp = pkbf(qs[4], qs[5]);
    FORU(k, 4) r[k] = f2{__builtin_amdgcn_rcpf(r[k].x), __builtin_amdgcn_rcpf(r[k].y)};
    fill();
    const u32 sav = __builtin_amdgcn_readlane(mv, 0), cav = __builtin_amdgcn_readlane(mv, 4);
    gav = __builtin_amdgcn_readlane(mv, 8);
    const u32 scp = pkbf(bf2f(cbp) + 1.0f);
    FORU(k, 4) { a[k] = f2{bflo(gx[k].x), bflo(gx[k].z)}; c[k] = f2{bfhi(gx[k].x), bfhi(gx[k].z)}; }
    fill();
    const u32 s1v = pkbf(bf2f(cav) + 1.0f);
    FORU(k, 4) { a[k] -= m[k]; c[k] -= m[k]; }
    const u32 scb[4] = {dppu<0x150>(scp), dppu<0x154>(scp), dppu<0x158>(scp), dppu<0x15C>(scp)};
    fill();
    FORU(k, 4) { a[k] *= r[k]; c[k] *= r[k]; }
    f2 sc[4], sb[4], t[8], bw[2], sw[2];
    FORU(j, 4) sc[j] = bf2f(scb[j]);
    fill();
    FORU(k, 4) { a[k] = a[k] * bflo(s1v) + bflo(sav); c[k] = c[k] * bfhi(s1v) + bfhi(sav); }
    FORU(j, 8) t[j] = pv[j] * sc[j & 3];
    fill();
    v4u wpk[2];
    FORU(k, 4) { h0[k] = pkbf(a[k]); h1[k] = pkbf(c[k]); }
    FORU(j, 8) wpk[j >> 2][j & 3] = pkbf(t[j]);
    fill();
    lptr up = L + L_U + 80 * (15 + (tid >> 2)) + 16 * (tid & 3);
    *(LAS v4u*)up = v4u{h0[0], h0[1], h0[2], h0[3]};
    *(LAS v4u*)(up + U_CH) = v4u{h1[0], h1[1], h1[2], h1[3]};
    st128(rs, wio, wpk[0]);
    st128s(rs, wio, two ? 65536 : OOB, wpk[1]);
    const u32 sbb[4] = {dppu<0x150>(sbp), dppu<0x154>(sbp), dppu<0x158>(sbp), dppu<0x15C>(sbp)};
    FORU(j, 8) t[j] = bf2f(wpk[j >> 2][j & 3]);
    SB();
    FORU(j, 4) sb[j] = bf2f(sbb[j]);
    FORU(f, 2) sw[f] = (t[4 * f] + t[4 * f + 1]) + (t[4 * f + 2] + t[4 * f + 3]);
    SB();
    FORU(j, 8) pv[j] *= sb[j & 3];
    SB();
    FORU(f, 2) bw[f] = (pv[4 * f] + pv[4 * f + 1]) + (pv[4 * f + 2] + pv[4 * f + 3]);
    const float e0 = sw16sum(bw[0].x + bw[0].y, sw[0].x + sw[0].y), e1 = sw16sum(bw[1].x + bw[1].y, sw[1].x + sw[1].y);
    const int bwo = WS_BWP + (((pj * 2 + (pi & 1)) * 6 + w) * 16 + (pi >> 1)) * 128 + (lane & 31) * 4;
    __builtin_amdgcn_raw_buffer_store_b32(__float_as_uint(sw32sum(e0, e1)), rs, lane < 32 ? bwo : two ? bwo + 8192 : OOB, 0, SC1);
  }
  __syncthreads();
  STAMP(5);
  v4u cdv;
  const u32 fpo = lane < 48 ? WS_WINP + pj * 196608 + (((pi & 7) * 4 + w) * 48 + lane) * 128 : OOB;
  auto side = [&](auto S, auto I) {
    constexpr int s = S.v, i = I.v, z = 10 * s + i;
    if constexpr (z % 4 == 1 && z / 4 < 40) ldw(8 + z / 4);
    if (z == 43) {
      asm volatile("s_waitcnt vmcnt(11)" ::: "memory");
      __builtin_amdgcn_raw_buffer_store_b32(tag, rs, lane == 0 ? WS_CD + b * 64 + w * 4 : OOB, 0, SC1);
    }
    if (z == 123) pf1 = __builtin_amdgcn_raw_buffer_load_b32(rs, fpo, 0, SC1);
    if (z == 163) cdv = ld128(rs, WS_CD + tid * 64);
  };
  if (w & 1) conv2<1, 2>(L, w >> 1, lane, side);
  else conv2<0, 3>(L, w >> 1, lane, side);
  STAMP(21);
  {
    f2 y[2][4], t[2][4];
    const float ga0 = bflo(gav), ga1 = bfhi(gav);
    FORU(k, 4) { t[0][k] = bf2f(h0[k]); t[1][k] = bf2f(h1[k]); }
    FORU(k, 4) { y[0][k] = f2{bflo(xv[2 * k]), bflo(xv[2 * k + 1])}; y[1][k] = f2{bfhi(xv[2 * k]), bfhi(xv[2 * k + 1])}; }
    lptr yp[4];
    FORU(B, 4) {
      const u32 tp = 8 * tid - 512 * B;
      yp[B] = L + L_Y + (B == 0 ? 0 : B == 1 ? 8448 : B == 2 ? 14848 : 19200) + ((tp & 2047) / (128 - 32 * B)) * 16 + tp * 4;
      pin(yp[B]);
    }
    SB();
    FORU(k, 4) { t[0][k] *= bflo(p_dsk); t[1][k] *= bfhi(p_dsk); pin(y[0][k]); pin(y[1][k]); pin(t[0][k]); pin(t[1][k]); }
    __syncthreads();
    STAMP(6);
    f4 q[4][4];
    FORU(B, 4) {
      if (B <= (int)w) { FORU(e, 4) q[B][e] = *(LAS f4*)(yp[B] + (e >> 1) * Y_CH + (e & 1) * 16); }
      else { FORU(e, 4) q[B][e] = f4{0.f, 0.f, 0.f, 0.f}; }
    }
    FORU(B, 4) { FORU(e, 4) { t[e >> 1][2 * (e & 1)] += f2{q[B][e].x, q[B][e].y}; t[e >> 1][2 * (e & 1) + 1] += f2{q[B][e].z, q[B][e].w}; } SB(); }
    FORU(k, 4) { y[0][k] += t[0][k] * ga0; y[1][k] += t[1][k] * ga1; }
    SB();
    v4u o0, o1;
    FORU(k, 4) { o0[k] = pkbf(y[0][k]); o1[k] = pkbf(y[1][k]); }
    SB();
    st128(rs, x1w, san4(o0));
    st128(rs, x1w + 128, san4(o1));
  }
  CFENCE();
  asm volatile("" ::"v"(pf1));
  auto cd_bad = [&]() { return __any(umax3(umax3(cdv.x, cdv.y, cdv.z), cdv.w, tag) != umin3(umin3(cdv.x, cdv.y, cdv.z), cdv.w, tag)); };
  if (cd_bad()) {
#pragma nounroll
    for (int sp = 0; sp < SPIN_CAP; ++sp) {
      asm volatile("" ::: "memory");
      cdv = ld128(rs, WS_CD + tid * 64);
      if (!cd_bad()) break;
    }
  }
  __syncthreads();
  STAMP(7);
  const u32 blkS = 4 + (w >> 1);
  const bool hasS = blkS < nb;
  const u32 fo[4] = {((pj * 2 + 1) * 6 + w), ((pj * 2 + 1) * 6 + (hasS ? blkS : 4)), (pj * 12 + w), (pj * 12 + (hasS ? blkS : 4))};
  v4u ones = {0x3f803f80u, 0x3f803f80u, 0x3f803f80u, 0x3f803f80u};
  pin(ones);
  v4u wf[4][16];
  auto ldf = [&](int s, int k) { wf[s][k] = ld128s(rs, lane * 16 + (k & 3) * 1024, WS_WINP + fo[s] * 16384 + (k >> 2) * 4096); };
  f4 sW[4];
  f4 bW[4];
  auto ldb = [&](int s) {
    bW[s] = __builtin_bit_cast(f4, ld128s(rs, c16 * 128 + q4 * 16, WS_BWP + fo[s] * 2048));
    sW[s] = __builtin_bit_cast(f4, ld128s(rs, c16 * 128 + q4 * 16, WS_BWP + fo[s] * 2048 + 64));
  };
  auto redb = [&](int s) {
    FORU(e, 16) { f4& v = (e & 4) ? sW[s + (e >> 3)] : bW[s + (e >> 3)]; v[e & 3] = dppadd<0xB1>(v[e & 3]); }
    SB();
    FORU(e, 16) { f4& v = (e & 4) ? sW[s + (e >> 3)] : bW[s + (e >> 3)]; v[e & 3] = dppadd<0x4E>(v[e & 3]); }
    SB();
    FORU(e, 16) { f4& v = (e & 4) ? sW[s + (e >> 3)] : bW[s + (e >> 3)]; v[e & 3] = dppadd<0x141>(v[e & 3]); }
    SB();
    FORU(e, 16) { f4& v = (e & 4) ? sW[s + (e >> 3)] : bW[s + (e >> 3)]; v[e & 3] = dppadd<0x140>(v[e & 3]); }
    SB();
  };
  {
    ldb(0); ldb(1);
    v4u xr[16];
    const int xo = WS_X1 + pi * 65536 + w * 16384;
    sfor<0, 32>([&](auto z) {
      constexpr int n = z.v;
      if constexpr (n < 16) ldf(n & 1, n >> 1); else xr[n - 16] = ld128s(rs, lane * 16 + (n & 3) * 1024, xo + rq((n - 16) >> 2) * 4096);
      shuf(IC<2 * n>{}); shuf(IC<2 * n + 1>{});
      SB();
    });
    redb(0);
    pin(bW[0]); pin(bW[1]); pin(sW[0]); pin(sW[1]);
    STAMP(8);
    lptr xi = L + L_XIMG + w * 16384 + (lane >> 3) * 128 + (lane & 1) * 16;
    const u32 xs0 = (((lane & 7) >> 1) ^ ((lane >> 4) & 1)) * 32;
    u32 m = 0;
    FORU(k, 16) { *(LAS v4u*)(xi + rq(k >> 2) * 4096 + (k & 3) * 1024 + (xs0 ^ ((k & 1) * 64))) = xr[k]; m = vmax(m, xr[k]); ldf(k & 1, 8 + (k >> 1)); SB(); }
    if (__any(m == ~0u)) {
#pragma nounroll
      for (int sp = 0; sp < SPIN_CAP; ++sp) {
        asm volatile("" ::: "memory");
        FORU(k, 16) xr[k] = ld128s(rs, lane * 16, xo + rq(k >> 2) * 4096 + (k & 3) * 1024);
        SB();
        m = 0;
        FORU(k, 16) { *(LAS v4u*)(xi + rq(k >> 2) * 4096 + (k & 3) * 1024 + (xs0 ^ ((k & 1) * 64))) = xr[k]; m = vmax(m, xr[k]); SB(); }
        CNT(29, 100);
        if (!__any(m == ~0u)) break;
      }
    }
    STAMP(9);
  }
  __syncthreads();
  STAMP(10);
  const u32 xsw = ((c16 >> 3) & 1) | ((q4 & 1) << 1), xbo = (8 * q4 + (c16 >> 2)) * 128 + 8 * (c16 & 3);
  lptr xb[4];
#pragma unroll
  for (int t = 0; t < 4; ++t) xb[t] = L + xbo + ((t ^ xw ^ xsw) * 32);
  v2u bl[2][4], bh[2][4];
  auto ldx = [&](int k, int i) {
    if (k < 16) { if (i & 1) bh[k & 1][i >> 1] = trread(xb[i >> 1] + 4096 * k + 512); else bl[k & 1][i >> 1] = trread(xb[i >> 1] + 4096 * k); }
  };
  auto bx = [&](int k, int t) { return v4u{bl[k & 1][t].x, bl[k & 1][t].y, bh[k & 1][t].x, bh[k & 1][t].y}; };
  const int pco = WS_PB + ((pi * 4 + w) * 44 + blk0) * 512, pc2 = 128 + lane < 32 * nb ? lane * 16 : OOB;
  const int zco = WS_Z2N + ((pj * 6 + (w >> 1)) * 64 + 2 * pi + (w & 1)) * 1024, zc2 = w + 8 < 2 * nb ? zco + 262144 : OOB;
  f4 z2a[6];
  f2 rn[4];
  {
    f4 st0, st1;
    v2u sl[16], sh[16];
    FORU(k, 4) { sl[k] = trread(xb[0] + 4096 * k); sh[k] = trread(xb[0] + 4096 * k + 512); }
    MM_HEAD();
    sfor<0, 16>([&](auto z) {
      constexpr int k = z.v;
      const v4u v = {sl[k].x, sl[k].y, sh[k].x, sh[k].y};
      if (k) m_vv(st0, ones, v); else m_vv0(st0, ones, v);
      if constexpr (k + 4 < 16) sl[k + 4] = trread(xb[0] + 4096 * (k + 4)); else ldx(0, 2 * (k - 12));
      SB();
      if (k) m_vv(st1, v, v); else m_vv0(st1, v, v);
      if constexpr (k + 4 < 16) sh[k + 4] = trread(xb[0] + 4096 * (k + 4) + 512); else ldx(0, 2 * (k - 12) + 1);
      if (k == 1) st128s(rs, lane * 16, pco, SENT);
      if (k == 5) st128s(rs, lane * 16, pco + 1024, SENT);
      if (k == 9) st128s(rs, pc2, pco + 2048, SENT);
      if (k == 3) st128s(rs, lane * 16, zco, SENT);
      if (k == 7) st128s(rs, lane * 16, zco + 131072, SENT);
      if (k == 11) st128s(rs, lane * 16, zc2, SENT);
      SB();
    });
    float sv0 = 0.f, sv1 = 0.f;
    FORU(k, 16) FORU(i, 6) {
      const int u = i == 0 ? 0 : i == 1 ? 4 : i == 2 ? 1 : i == 3 ? 5 : i - 2;
      if (k) m_vv(z2a[u], wf[u >> 2][k], bx(k, u & 3)); else m_vv0(z2a[u], wf[u >> 2][k], bx(k, u & 3));
      if (i < 2) { ldx(k + 1, 2 * i); ldx(k + 1, 2 * i + 1); } else ldx(k + 1, i + 2);
      if (i == 2 && k) ldf(2 + ((k - 1) & 1), (k - 1) >> 1);
      if (i == 3 || i == 5) {
        const int z = 2 * k + (i == 5);
        if (z == 4) sv1 = (c16 & 1) ? st1.y : st1.x;
        if (z == 5) sv0 = (c16 & 1) ? st1.w : st1.z;
        if (z == 6) sv1 = (c16 & 2) ? sv0 : sv1;
        if (z == 7) { pin(st0); sv0 = st0.x * (1.f / 512); }
        if (z == 8) sv1 *= (1.f / 512);
        if (z == 9) sv1 = fmaxf(__builtin_fmaf(-sv0, sv0, sv1), 0.f);
        if (z == 10) sv1 = rbf(sv1);
        if (z == 11) sv0 = rbf(sv0);
        if (z == 12) sv1 = rbf(sv1 + 1e-5f);
        if (z == 13) sv1 = __builtin_amdgcn_sqrtf(sv1);
        if (z == 14) sv1 = rbf(sv1);
        if (z == 15) sv1 = __builtin_amdgcn_rcpf(sv1);
        if (z == 16) sv0 *= -sv1;
        if (z == 17) *(volatile LAS f2*)(L + (q4 == (c16 >> 2) ? L_XST + (16 * xw + c16) * 8 : L_XST + 512 + lane * 8)) = f2{sv1, sv0};
        if (z >= 4 && z <= 16) { pin(sv0); pin(sv1); }
        if (z == 22) __builtin_amdgcn_s_barrier();
        if (z >= 24 && z < 28) rn[z - 24] = *(volatile LAS f2*)(L + L_XST + (16 * ((z - 24) ^ xw) + c16) * 8);
      }
      SB();
    }
    MM_TAIL();
  }
  STAMP(24);
  auto shuf2 = [&](auto C, auto N) {
    constexpr int n = N.v % 12, z = 16 * (C.v + N.v / 12);
    if constexpr (N.v < 24) { if constexpr (n < 8) shuf(IC<z + n>{}); else { shuf(IC<z + 2 * n - 8>{}); shuf(IC<z + 2 * n - 7>{}); } }
  };
  auto epi = [&](f4 (&a)[6], int s0, auto fill) {
    f2 v[12];
    sfor<0, 2>([&](auto z) {
      FORU(j, 6) {
        const int i = 6 * z.v + j;
        const f4 &sw = sW[s0 + (i >> 3)], &bw = bW[s0 + (i >> 3)];
        v[i] = ((i & 1) ? f2{sw.z, sw.w} : f2{sw.x, sw.y}) * rn[(i >> 1) & 3].y + ((i & 1) ? f2{bw.z, bw.w} : f2{bw.x, bw.y});
      }
      fill(IC<z.v>{});
    });
    sfor<0, 2>([&](auto z) {
      FORU(j, 6) { const int i = 6 * z.v + j; v[i] = ((i & 1) ? f2{a[i >> 1].z, a[i >> 1].w} : f2{a[i >> 1].x, a[i >> 1].y}) * rn[(i >> 1) & 3].x + v[i]; }
      fill(IC<2 + z.v>{});
    });
    gelu_n<12>(v, fill);
    FORU(i, 6) { a[i] = f4{v[2 * i].x, v[2 * i].y, v[2 * i + 1].x, v[2 * i + 1].y}; pin(a[i]); }
  };
  {
    epi(z2a, 0, [&](auto N) {
      constexpr int n = N.v;
      SB();
      if constexpr ((n & 1) && n < 22) ldf(2 + ((15 + n / 2) & 1), (15 + n / 2) >> 1);
      if constexpr (n >= 22) ldw(26 + n);
      shuf2(IC<4>{}, N);
      SB();
    });
    if (!hasS) { z2a[4] = f4{0.f, 0.f, 0.f, 0.f}; z2a[5] = z2a[4]; }
    f2 zs[6], zq[6];
    FORU(i, 6) { zs[i] = f2{z2a[i].x, z2a[i].y} + f2{z2a[i].z, z2a[i].w}; zq[i] = f2{z2a[i].x, z2a[i].y} * f2{z2a[i].x, z2a[i].y}; }
    SB();
    FORU(i, 6) zq[i] = __builtin_elementwise_fma(f2{z2a[i].z, z2a[i].w}, f2{z2a[i].z, z2a[i].w}, zq[i]);
    FORU(i, 2) zs[i] += zs[4 + i];
    SB();
    float us[4], uq[4], u[4];
    FORU(i, 2) zq[i] += zq[4 + i];
    FORU(t, 4) us[t] = zs[t].x + zs[t].y;
    SB();
    FORU(t, 4) uq[3 - t] = zq[3 - t].x + zq[3 - t].y;
    SB();
    FORU(t, 4) u[3 - t] = sw32sum(us[3 - t], uq[3 - t]);
    LAS float* zp = (LAS float*)(L + L_ZST) + (w * 2 + (lane >> 5)) * 64 + c16;
    zp[16 * ((2 * (q4 & 1) + 1) ^ xw)] = sw16sum(u[1], u[3]);
    zp[16 * ((2 * (q4 & 1)) ^ xw)] = sw16sum(u[0], u[2]);
  }
  asm volatile("s_waitcnt vmcnt(20)" ::: "memory");
  __syncthreads();
  if (w == 0) {
    LAS float* zp = (LAS float*)(L + L_ZST) + lane;
    const float s = (zp[0] + zp[128]) + (zp[256] + zp[384]), ss = (zp[64] + zp[192]) + (zp[320] + zp[448]);
    st128(rs, WS_ST2 + ((pi * 8 + pj) * 64 + lane) * 16, v4u{__float_as_uint(s), tag, __float_as_uint(ss), tag});
  }
  __builtin_amdgcn_raw_buffer_store_b32(tag, rs, tid == 64 ? WS_F3 + (pj * 32 + pi) * 128 : OOB, 0, SC1);
  CFENCE();
  STAMP(11);
  f4 z1a[6];
  v2u xres[4];
  v4u g[8];
  u32 fv;
  auto f_ld = [&]() { fv = __builtin_amdgcn_raw_buffer_load_b32(rs, (lane & 7) * 128, WS_F3 + (pj * 32 + 8 * w) * 128, SC1); };
  auto s2b_ld = [&](int n) { g[n] = ld128s(rs, (2 * q4 * 64 + c16) * 16 + (n & 1) * 1024, WS_ST2 + pi * 8192 + ((n >> 1) ^ xw) * 256); };
  {
#pragma unroll
    for (int i = 0; i < 8; ++i) ldx(0, i);
    MM_HEAD();
    sfor<0, 96>([&](auto z) {
      constexpr int k = z.v / 6, i = z.v % 6, u = i == 0 ? 0 : i == 1 ? 4 : i == 2 ? 1 : i == 3 ? 5 : i - 2;
      if (k) m_vv(z1a[u], wf[2 + (u >> 2)][k], bx(k, u & 3)); else m_vv0(z1a[u], wf[2 + (u >> 2)][k], bx(k, u & 3));
      if (i < 2) { ldx(k + 1, 2 * i); ldx(k + 1, 2 * i + 1); } else ldx(k + 1, i + 2);
      if (k < 3 && (i == 2 || i == 4)) ldf(1 + (i >> 1), 13 + k);
      if (k == 3 && (i == 2 || i == 4)) ldb(1 + (i >> 1));
      if (k >= 4 && k < 10 && (i == 2 || i == 4)) ldw(52 + 2 * (k - 4) + (i >> 2));
      if (k >= 10 && k < 14 && (i == 3 || i == 5)) s2b_ld(2 * (k - 10) + (i == 5));
      if (k == 14 && i == 3) f_ld();
      SB();
    });
    MM_TAIL();
#pragma unroll
    for (int n = 0; n < 4; ++n) {
      const u32 row = 16 * n + 4 * q4 + (c16 >> 2);
      xres[n] = trread(L + L_XIMG + pj * 8192 + row * 128 + ((w ^ (((row >> 1) & 1) | (((row >> 3) & 1) << 1))) * 32) + 8 * (c16 & 3));
    }
  }
  STAMP(12);
  {
    auto bad = [&]() { u32 mx = tag, mn = tag; FORU(n, 8) { mx = umax3(mx, g[n].y, g[n].w); mn = umin3(mn, g[n].y, g[n].w); } return __any(mx != mn); };
    if (bad()) {
#pragma nounroll
      for (int sp = 0; sp < SPIN_CAP; ++sp) {
        asm volatile("" ::: "memory");
        FORU(n, 8) s2b_ld(n);
        CNT(29, 10000);
        if (!bad()) break;
      }
    }
    STAMP(13);
    lptr tw = L + L_TRS + w * TRS_W;
    float a[4], c[4];
    FORU(t, 4) { a[t] = __uint_as_float(g[2 * t].x) + __uint_as_float(g[2 * t + 1].x); c[t] = __uint_as_float(g[2 * t].z) + __uint_as_float(g[2 * t + 1].z); }
    SB();
    const float a0 = sw32sum(a[0], a[2]), a1 = sw32sum(a[1], a[3]), c0 = sw32sum(c[0], c[2]), c1 = sw32sum(c[1], c[3]);
    const float m = sw16sum(a0, a1) * (1.f / 688), var = fmaxf(sw16sum(c0, c1) * (1.f / 688) - m * m, 0.f);
    const float r = __builtin_amdgcn_rcpf(ln_den(var)), n0 = -rbf(m) * r;
    auto gather = [&](float v, float (&o)[4]) {
      auto e = __builtin_amdgcn_permlane16_swap(__float_as_uint(v), __float_as_uint(v), false, false);
      auto e0 = __builtin_amdgcn_permlane32_swap(e[0], e[0], false, false);
      auto e1 = __builtin_amdgcn_permlane32_swap(e[1], e[1], false, false);
      o[0] = __uint_as_float(e0[0]); o[2] = __uint_as_float(e0[1]); o[1] = __uint_as_float(e1[0]); o[3] = __uint_as_float(e1[1]);
    };
    gather(r, a); gather(n0, c);
    SB();
    FORU(i, 6) z2a[i] = z2a[i] * a[i & 3] + c[i & 3];
    SB();
    FORU(i, 6) *(LAS v2u*)(tw + (16 * ((i & 3) ^ xw) + c16) * 64 + 8 * (((i & 4) + q4) ^ (c16 >> 1))) = v2u{pkbf(z2a[i].x, z2a[i].y), pkbf(z2a[i].z, z2a[i].w)};
    const u32 r0 = 8 * q4 + (c16 >> 2);
    const int zo = WS_Z2N + (pj * 6 * 64 + 2 * pi) * 1024 + lane * 16;
#pragma unroll
    for (int n = 0; n < 3; ++n) {
      lptr pp = tw + (n < 2 ? n : (w & 1)) * 2048 + r0 * 64;
      const u32 un = (n < 2 ? 0 : 4) + (c16 & 3);
      const v2u lo = trread(pp + 8 * (un ^ ((r0 >> 1) & 7))), hi = trread(pp + 256 + 8 * (un ^ (((r0 + 4) >> 1) & 7)));
      st128s(rs, zo, n < 2 ? w * 65536 + n * 1024 : (hasS ? blkS * 65536 + (w & 1) * 1024 : OOB), san4(v4u{lo.x, lo.y, hi.x, hi.y}));
    }
  }
  CFENCE();
  STAMP(14);
  u32 bsr[4], gbp[2];
  v4u gg[2];
  FORU(t, 4) bsr[t] = gld<u16>(bsp, (64 * pi + 16 * (t ^ xw) + c16) * 2);
  FORU(m, 2) gg[m] = ld128(rs, WS_MOD + ((lane & 3) * 1536 + 1280 + 32 * pj + 16 * m + 8 * (c16 >> 3) + 2 * q4 + ((c16 >> 2) & 1)) * 16);
  if (__any(fv != tag)) {
#pragma nounroll
    for (int sp = 0; sp < SPIN_CAP; ++sp) {
      asm volatile("" ::: "memory");
      f_ld();
      if (!__any(fv != tag)) break;
    }
  }
  CFENCE();
  v2u z1[6];
  f4 ga[2][4];
  v4u wo[6][4];
  const u32 wob = WS_WOP + (pj * 22 + 6 * w) * 4096, wobh = ssel(w < 3, wob, OOB);
  auto ldo = [&](int n) { wo[n >> 2][n & 3] = ld128s(rs, lane * 16 + (n & 3) * 1024, (n < 16 ? wob : wobh) + (n >> 2) * 4096); };
  {
    const u32 zo = WS_Z2N + (pj * 6 * 64 + 16 * w) * 1024;
    const u32 rot = ((nb == 6 ? 0x1420u : 0x1320u) >> (4 * (pi & 3))) & 15;
    v4u zr[16];
    u32 zv[4];
    FORU(g, 4) zv[g] = lane * 16 + rq(g) * 4096;
    u32 so[6], rsl[6];
    FORU(it, 5) {
      const u32 blk = smod(w + 1 + smod((it ? it + nb - 6 : 0) + rot, nb - 1), nb);
      so[it] = zo + blk * 65536;
      rsl[it + 1] = ssel(blk < 4, blk * 3 + ((w - blk - 1) & 3), 4 * blk - 4 + w) * 4096;
    }
    so[5] = zo + w * 65536;
    auto ldz = [&](int k, u32 s0) { zr[k] = ld128s(rs, zv[k >> 2] + (k & 3) * 1024, s0); };
    redb(2);
    epi(z1a, 2, [&](auto N) {
      constexpr int n = N.v;
      SB();
      shuf2(IC<6>{}, N);
      if constexpr (n >= 6 && n < 22) ldz(n - 6, so[0]);
      SB();
    });
    FORU(i, 6) { z1[i] = v2u{pkbf(z1a[i].x, z1a[i].y), pkbf(z1a[i].z, z1a[i].w)}; pin(z1[i]); }
    FORU(m, 2) { gbp[m] = pkbf(quadsum(__uint_as_float(gg[m].x)), quadsum(__uint_as_float(gg[m].z))); pin(gbp[m]); }
    STAMP(25);
    __syncthreads();
    {
      auto bad = [&]() { u32 m = 0; FORU(k, 16) m = vmax(m, zr[k]); return __any(m == ~0u); };
      if (bad()) {
#pragma nounroll
        for (int sp = 0; sp < SPIN_CAP; ++sp) {
          asm volatile("" ::: "memory");
          FORU(k, 16) ldz(k, so[0]);
          CNT(28, 100);
          if (!bad()) break;
        }
      }
    }
    STAMP(15);
    u32 ra = 0, badm = 0;
    float ck = 0.f;
    MM_HEAD();
    sfor<0, 96 * 4>([&](auto z) {
      constexpr int it = z.v / 64, k = (z.v / 4) % 16, t = z.v % 4, h = it & 1;
      if (k) m_va(ga[h][t], zr[k], wsf[k][t]); else m_va0(ga[h][t], zr[k], wsf[k][t]);
      constexpr int pz = z.v / 4 - 1, pit = pz / 16, pk = pz % 16;
      if constexpr (t == 1 && pz >= 0) {
        if constexpr (pit < 5) ldz(pk, so[pit + 1]);
        else ldo(pk);
      }
      if constexpr (t == 3 && it == 5 && k >= 7) ldo(8 + k);
      if constexpr (it > 0) {
        if (k == 2 && t == 2) { ck = ga[h ^ 1][0].x + ga[h ^ 1][0].y; pin(ck); }
        if (k == 2 && t == 3) { ck += ga[h ^ 1][0].z; pin(ck); }
        if (k == 3 && t == 2) { ck += ga[h ^ 1][0].w; pin(ck); }
        if (k == 3 && t == 3) badm |= __any(ck != ck) ? 1u << (it - 1) : 0u;
        if (k >= 4 && k < 12 && !(k & 1) && t == 2) { ra = rsl[it] + lane * 16 + ((((k - 4) >> 1) ^ xw) * 1024); pin(ra); }
        if constexpr (k >= 4 && k < 12 && !(k & 1) && t == 3) *(LAS f4*)(L + L_RED + ra) = ga[h ^ 1][(k - 4) >> 1];
      }
      SB();
    });
    MM_TAIL();
    { const float t = (ga[1][0].x + ga[1][0].y) + (ga[1][0].z + ga[1][0].w); badm |= __any(t != t) ? 32u : 0u; }
    if (badm) {
#pragma nounroll
      for (u32 it = 0; it < 6; ++it) {
        if (!((badm >> it) & 1)) continue;
        CNT(28, 1);
        const u32 s0 = it == 0 ? so[0] : it == 1 ? so[1] : it == 2 ? so[2] : it == 3 ? so[3] : it == 4 ? so[4] : so[5];
        const u32 r0 = it == 0 ? rsl[1] : it == 1 ? rsl[2] : it == 2 ? rsl[3] : it == 3 ? rsl[4] : rsl[5];
#pragma nounroll
        for (int sp = 0; sp < SPIN_CAP; ++sp) {
          u32 m = 0;
          FORU(k, 16) ldz(k, s0);
          FORU(k, 16) m = vmax(m, zr[k]);
          asm volatile("" ::: "memory");
          if (!__any(m == ~0u)) break;
        }
        MM_HEAD();
        FORU(k, 16) FORU(t, 4) { if (k) m_va(ga[0][t], zr[k], wsf[k][t]); else m_va0(ga[0][t], zr[k], wsf[k][t]); }
        MM_TAIL();
        FORU(t, 4) {
          if (it < 5) *(LAS f4*)(L + L_RED + r0 + lane * 16 + ((t ^ xw) * 1024)) = ga[0][t];
          else ga[1][t] = ga[0][t];
        }
      }
    }
  }
  STAMP(26);
  {
    f2 zf[12], gs2[12];
    FORU(i, 12) { zf[i] = bf2f(z1[i >> 1][i & 1]); pin(zf[i]); }
    FORU(i, 12) {
      const float bs = bflo(bsr[(i >> 1) & 3]);
      gs2[i] = i < 8 ? ((i & 1) ? f2{ga[1][i >> 1].z, ga[1][i >> 1].w} : f2{ga[1][i >> 1].x, ga[1][i >> 1].y}) + bs : f2{bs, bs};
      pin(gs2[i]);
    }
    __syncthreads();
    STAMP(16);
    f4 q[4][6];
    FORU(s, 4) FORU(i, 6) {
      if (i < 4 && s < 3) q[s][i] = *(LAS f4*)(L + L_RED + ((w * 3 + s) * 4 + (i ^ xw)) * 1024 + lane * 16);
      if (i >= 4) q[s][i] = *(LAS f4*)(L + L_RED + ((blkS * 4 - 4 + s) * 4 + ((i & 3) ^ xw)) * 1024 + lane * 16);
    }
    SB();
    FORU(s, 4) { FORU(i, 6) if (i >= 4 || s < 3) { gs2[2 * i] += f2{q[s][i].x, q[s][i].y}; gs2[2 * i + 1] += f2{q[s][i].z, q[s][i].w}; } SB(); }
    FORU(i, 12) gs2[i] *= zf[i];
    SB();
    v2u P[6];
    FORU(i, 12) P[i >> 1][i & 1] = pkbf(gs2[i]);
    SB();
#pragma unroll
    for (int h = 0; h < 3; ++h) {
      auto rx = __builtin_amdgcn_permlane16_swap(P[2 * h].x, P[2 * h + 1].x, false, false);
      auto ry = __builtin_amdgcn_permlane16_swap(P[2 * h].y, P[2 * h + 1].y, false, false);
      const u32 tt = (2 * (h & 1) + (q4 & 1)) ^ xw;
      st128s(rs, WS_PB + ((pi * 4 + tt) * 44 + blk0) * 512 + (q4 >> 1) * 256 + c16 * 16, h < 2 ? w * 512 : (hasS ? blkS * 512 : OOB),
             san4(v4u{rx[0], ry[0], rx[1], ry[1]}));
    }
  }
  STAMP(17);
  {
    f4 oa[2][4];
    v4u pr[4][6];
    u32 pb[4], pbh[4];
    u32 rd[3];
    FORU(u, 4) {
      const u32 d = u < 3 ? 1 + smod(u + smod(pj & 3, 3), 3) : 4;
      pb[u] = WS_PB + ((pi * 4 + ((w + d) & 3)) * 44 + 12 * w) * 512; pbh[u] = ssel(w < 3, pb[u] + 4096, OOB);
      if (u < 3) rd[u] = L_RED2 + (((w + d) & 3) * 3 + 3 - d) * 4096 + lane * 16;
    }
    const u32 pv3 = (w == 3 && lane >= 32) ? OOB : lane * 16 + 3072;
    auto ldp = [&](int n) {
      const int u = n / 6, k = n % 6;
      pr[u][k] = ld128s(rs, k == 3 ? pv3 : lane * 16 + (k & 3) * 1024, k < 4 ? pb[u] : pbh[u]);
    };
    const u32 bk = 12 * w + lane % 12, po = (lane < 48 && bk < 43) ? WS_PB + ((pi * 4 + lane / 12) * 44 + bk) * 512 + 508 : OOB;
    DRAIN();
    STAMP(18);
#pragma nounroll
    for (int sp = 0; sp < SPIN_CAP; ++sp) {
      asm volatile("" ::: "memory");
      const u32 pv = __builtin_amdgcn_raw_buffer_load_b32(rs, po, 0, SC1);
      FORU(n, 12) { ldp(n); SB(); }
      if (__any(pv == ~0u)) { CNT(28, 10000); continue; }
      float ck = 0.f;
      MM_HEAD();
      sfor<0, 96>([&](auto z) {
        constexpr int u = z.v / 24, k = (z.v / 4) % 6, n = z.v % 4;
        if (k) m_va(oa[u & 1][n], wo[k][n], pr[u][k]); else m_va0(oa[u & 1][n], wo[k][n], pr[u][k]);
        if constexpr (n == 1 && z.v / 4 < 12) ldp(12 + z.v / 4);
        if constexpr (u > 0 && k == 2 && n == 0) { ck += oa[(u & 1) ^ 1][0].x; pin(ck); }
        if constexpr (u > 0 && k >= 2 && n == 2) *(LAS f4*)(L + rd[u - 1] + (k - 2) * 1024) = oa[(u & 1) ^ 1][k - 2];
        SB();
      });
      MM_TAIL();
      ck += oa[1][0].x;
      if (!__any(ck != ck)) break;
      u32 m = 0;
      FORU(n, 24) m = vmax(m, aget(pr[n / 6][n % 6]));
      if (!__any(m == ~0u)) break;
      CNT(28, 1000000);
    }
    __syncthreads();
    f4 o[4];
    FORU(n, 4) {
      lptr p = L + L_RED2 + (w * 12 + n) * 1024 + lane * 16;
      o[n] = (oa[1][n] + *(LAS f4*)p) + (*(LAS f4*)(p + 4096) + *(LAS f4*)(p + 8192));
    }
    STAMP(19);
    v2u O[4];
#pragma unroll
    for (int n = 0; n < 4; ++n) {
      const u32 g0 = (n & 1) ? dppu<0x158>(gbp[n >> 1]) : dppu<0x150>(gbp[n >> 1]), g1 = (n & 1) ? dppu<0x15C>(gbp[n >> 1]) : dppu<0x154>(gbp[n >> 1]);
      O[n] = v2u{pkbf(bf2f(xres[n].x) + bf2f(g0) * f2{o[n].x, o[n].y}), pkbf(bf2f(xres[n].y) + bf2f(g1) * f2{o[n].z, o[n].w})};
    }
#pragma unroll
    for (int h = 0; h < 2; ++h) {
      auto rx = __builtin_amdgcn_permlane16_swap(O[2 * h].x, O[2 * h + 1].x, false, false);
      auto ry = __builtin_amdgcn_permlane16_swap(O[2 * h].y, O[2 * h + 1].y, false, false);
      *(v4u*)(out + (64 * pi + 16 * w + c16) * 512 + 64 * pj + 16 * (2 * h + (q4 & 1)) + 8 * (q4 >> 1)) = v4u{rx[0], ry[0], rx[1], ry[1]};
    }
  }
  if (b == 0 && tid == 0) __builtin_amdgcn_raw_buffer_store_b32(tag, rs, WS_E, 0, SC1);
  asm volatile("fk_text_b:");
  STAMP(20);
  STAMP_FLUSH();
}

extern "C" void kernel_launch(void* const* d_in, const int* in_sizes, int n_in, void* d_out, int out_size, void* d_ws, size_t ws_size,
                              hipStream_t stream) {
  (void)in_sizes; (void)n_in; (void)out_size; (void)ws_size;
  (void)hipFuncSetAttribute((const void*)fused_kernel, hipFuncAttributeMaxDynamicSharedMemorySize, LDS_BYTES);
  fused_kernel<<<256, 256, LDS_BYTES, stream>>>((const u16*)d_in[0], (const u16*)d_in[1], (const u16*)d_in[2], (const u16*)d_in[3],
                                                (const u16*)d_in[4], (const u16*)d_in[5], (const u16*)d_in[6], (const u16*)d_in[8],
                                                (const u16*)d_in[9], (const u16*)d_in[10], (const u16*)d_in[11], (const u16*)d_in[12],
                                                (const u16*)d_in[13], (u16*)d_out, (unsigned char*)d_ws);
}
```

```cpp
#include <hip/hip_runtime.h>
#include <stdint.h>

typedef unsigned u32;
typedef unsigned short u16;
typedef unsigned long long u64;
typedef u32 v4u __attribute__((ext_vector_type(4)));
typedef u32 v2u __attribute__((ext_vector_type(2)));
typedef float f4 __attribute__((ext_vector_type(4)));
typedef float f2 __attribute__((ext_vector_type(2)));
typedef __bf16 bf8 __attribute__((ext_vector_type(8)));
typedef __bf16 bf2 __attribute__((ext_vector_type(2)));
typedef _Float16 h8 __attribute__((ext_vector_type(8)));
typedef _Float16 h2 __attribute__((ext_vector_type(2)));
typedef short s4 __attribute__((ext_vector_type(4)));
#define LAS __attribute__((address_space(3)))
#define GAS __attribute__((address_space(1)))
#define CAS __attribute__((address_space(4)))
#define DI __device__ __forceinline__
typedef __amdgpu_buffer_rsrc_t rsrc_t;
typedef LAS unsigned char* lptr;

#define XMAP 2

constexpr int WS_E = 0, WS_STAT1 = 4096, WS_CD = 32768, WS_ST2 = 65536, WS_F3 = 327680, WS_H4 = 360448, WS_MOD = 524288, WS_BWP = 655360;
constexpr int WS_XG = 1 << 20, WS_X1 = 5 << 20, WS_Z2N = 7 << 20, WS_PB = 10 << 20, WS_WINP = 13 << 20, WS_WOP = 29 << 19;
constexpr int WS_DBG = 24 << 20, WS_END = 25 << 20;
constexpr int OOB = 0x40000000;
constexpr int TABP = 544, TAB_CH = 63 * TABP;
constexpr int L_TAB = 0, L_Y = 0, Y_CH = 21504;
constexpr int L_PKW = 75776, L_SHF = 81920;
constexpr int L_PKI = 68544;
constexpr int L_TOE = 108544, TOE_C = 5312, TOE_CH = 4 * TOE_C;
constexpr int L_U = 151040, U_CH = 6320;
constexpr int L_XIMG = 0, L_TRS = 65536, TRS_W = 4096;
constexpr int L_RED = 0, L_RED2 = 81920;
constexpr int L_XST = 159744, L_ZST = L_XST + 1024;
constexpr int LDS_BYTES = 163840;
constexpr int SC1 = 16;
constexpr int SPIN_CAP = 1 << 15;
static_assert(LDS_BYTES <= 163840, "LDS");

DI float bflo(u32 v) { return __uint_as_float(v << 16); }
DI float bfhi(u32 v) { return __uint_as_float(v & 0xffff0000u); }
DI f2 bf2f(u32 v) { return f2{bflo(v), bfhi(v)}; }
DI u32 pkbf(float a, float b) { f2 v = {a, b}; return __builtin_bit_cast(u32, __builtin_convertvector(v, bf2)); }
DI u32 pkbf(f2 v) { return __builtin_bit_cast(u32, __builtin_convertvector(v, bf2)); }
DI u32 pkh(f2 v) { return __builtin_bit_cast(u32, __builtin_convertvector(v, h2)); }
DI float rbf(float a) { return bflo(pkbf(a, a)); }
DI u32 san(u32 v) { return v < 0xFFFFFFFEu ? v : 0xFFFFFFFEu; }
DI v4u san4(v4u v) { return v4u{san(v.x), san(v.y), san(v.z), san(v.w)}; }
DI u32 umax3(u32 a, u32 b, u32 c) { return max(max(a, b), c); }
DI u32 umin3(u32 a, u32 b, u32 c) { return min(min(a, b), c); }
DI u32 vmax(u32 m, v4u v) { return umax3(umax3(m, v.x, v.y), v.z, v.w); }
DI v4u ld128(rsrc_t rs, int off) { return __builtin_amdgcn_raw_buffer_load_b128(rs, off, 0, SC1); }
DI v4u ld128s(rsrc_t rs, int off, int soff) { return __builtin_amdgcn_raw_buffer_load_b128(rs, off, soff, SC1); }
DI void st128(rsrc_t rs, int off, v4u v) { __builtin_amdgcn_raw_buffer_store_b128(v, rs, off, 0, SC1); }
DI void st128s(rsrc_t rs, int off, int soff, v4u v) { __builtin_amdgcn_raw_buffer_store_b128(v, rs, off, soff, SC1); }
DI void st64(rsrc_t rs, int off, v2u v) { __builtin_amdgcn_raw_buffer_store_b64(v, rs, off, 0, SC1); }
#define DRAIN() asm volatile("s_waitcnt vmcnt(0)" ::: "memory")
#define SB() __builtin_amdgcn_sched_barrier(0)
#define CFENCE() do { asm volatile("" ::: "memory"); SB(); } while (0)
template <int C> DI u32 dppu(u32 v) { return (u32)__builtin_amdgcn_update_dpp(0, (int)v, C, 0xF, 0xF, true); }
template <int C> DI float dppadd(float v) { return v + __uint_as_float(dppu<C>(__float_as_uint(v))); }
DI float sw16sum(float a, float b) { auto r = __builtin_amdgcn_permlane16_swap(__float_as_uint(a), __float_as_uint(b), false, false); return __uint_as_float(r[0]) + __uint_as_float(r[1]); }
DI float sw32sum(float a, float b) { auto r = __builtin_amdgcn_permlane32_swap(__float_as_uint(a), __float_as_uint(b), false, false); return __uint_as_float(r[0]) + __uint_as_float(r[1]); }
DI float quadsum(float v) { v = dppadd<0xB1>(v); return dppadd<0x4E>(v); }
DI float rowsum(float v) { v = quadsum(v); v = dppadd<0x141>(v); return dppadd<0x140>(v); }
DI f4 mfma_bf(v4u a, v4u b, f4 c) { return __builtin_amdgcn_mfma_f32_16x16x32_bf16(__builtin_bit_cast(bf8, a), __builtin_bit_cast(bf8, b), c, 0, 0, 0); }
DI f4 mfma_h(v4u a, v4u b, f4 c) { return __builtin_amdgcn_mfma_f32_16x16x32_f16(__builtin_bit_cast(h8, a), __builtin_bit_cast(h8, b), c, 0, 0, 0); }
DI v2u trread(lptr p) { return __builtin_bit_cast(v2u, __builtin_amdgcn_ds_read_tr16_b64_v4i16((LAS s4*)p)); }
template <int N> struct IC { static constexpr int v = N; };
template <int I, int N, class F> DI void sfor(F f) { if constexpr (I < N) { f(IC<I>{}); sfor<I + 1, N>(f); } }
#define FORU(i, n) _Pragma("unroll") for (int i = 0; i < (n); ++i)
DI f2 cm1(f2 a, f2 b) { f2 t; asm("v_pk_mul_f32 %0, %1, %2 op_sel:[0,0] op_sel_hi:[0,1]" : "=v"(t) : "v"(a), "v"(b)); return t; }
DI f2 cm2(f2 a, f2 b, f2 t) { f2 r; asm("v_pk_fma_f32 %0, %1, %2, %3 op_sel:[1,1,0] op_sel_hi:[1,0,1] neg_lo:[0,1,0]" : "=v"(r) : "v"(a), "v"(b), "v"(t)); return r; }
DI f2 cexp_p(float al2, float rev, float p) {
  float m = __builtin_amdgcn_exp2f(al2 * p), f = __builtin_amdgcn_fractf(rev * p);
  return f2{m * __builtin_amdgcn_cosf(f), m * __builtin_amdgcn_sinf(f)};
}
template <int N, class F> DI void gelu_n(f2 (&v)[N], F fill) {
  const float c0 = -2.0f * 0.7978845608028654f * 1.4426950408889634f, c1 = c0 * 0.044715f;
  f2 a[N];
  sfor<0, 2>([&](auto z) { FORU(i, 6) a[6 * z.v + i] = v[6 * z.v + i] * v[6 * z.v + i]; fill(IC<4 + z.v>{}); });
  sfor<0, 2>([&](auto z) { FORU(i, 6) a[6 * z.v + i] = __builtin_elementwise_fma(a[6 * z.v + i], f2{c1, c1}, f2{c0, c0}); fill(IC<6 + z.v>{}); });
  sfor<0, 2>([&](auto z) { FORU(i, 6) a[6 * z.v + i] *= v[6 * z.v + i]; fill(IC<8 + z.v>{}); });
  sfor<0, 6>([&](auto z) { FORU(i, 2) a[2 * z.v + i] = f2{__builtin_amdgcn_exp2f(a[2 * z.v + i].x), __builtin_amdgcn_exp2f(a[2 * z.v + i].y)}; fill(IC<10 + z.v>{}); });
  sfor<0, 2>([&](auto z) { FORU(i, 6) a[6 * z.v + i] += 1.0f; fill(IC<16 + z.v>{}); });
  sfor<0, 6>([&](auto z) { FORU(i, 2) a[2 * z.v + i] = f2{__builtin_amdgcn_rcpf(a[2 * z.v + i].x), __builtin_amdgcn_rcpf(a[2 * z.v + i].y)}; fill(IC<18 + z.v>{}); });
  sfor<0, 2>([&](auto z) { FORU(i, 6) v[6 * z.v + i] *= a[6 * z.v + i]; fill(IC<24 + z.v>{}); });
}
DI float ln_den(float var) { return rbf(__builtin_amdgcn_sqrtf(rbf(rbf(var) + 1e-5f))); }
template <class T> DI T gld(const void* base, u32 off) { return *(const GAS T*)((const GAS char*)base + off); }
#define MF "v_mfma_f32_16x16x32_bf16 "
DI void m_av(f4& c, v4u a, v4u b) { asm volatile(MF "%0, %1, %2, %0" : "+v"(c) : "a"(a), "v"(b)); }
DI void m_va(f4& c, v4u a, v4u b) { asm volatile(MF "%0, %1, %2, %0" : "+v"(c) : "v"(a), "a"(b)); }
DI void m_vv(f4& c, v4u a, v4u b) { asm volatile(MF "%0, %1, %2, %0" : "+v"(c) : "v"(a), "v"(b)); }
DI void m_av0(f4& c, v4u a, v4u b) { asm volatile(MF "%0, %1, %2, 0" : "=&v"(c) : "a"(a), "v"(b)); }
DI void m_va0(f4& c, v4u a, v4u b) { asm volatile(MF "%0, %1, %2, 0" : "=&v"(c) : "v"(a), "a"(b)); }
DI void m_vv0(f4& c, v4u a, v4u b) { asm volatile(MF "%0, %1, %2, 0" : "=&v"(c) : "v"(a), "v"(b)); }
DI v4u aget(v4u a) {
  v4u r;
  _Pragma("unroll") for (int e = 0; e < 4; ++e) { u32 t; asm volatile("v_accvgpr_read_b32 %0, %1" : "=v"(t) : "a"(a[e])); r[e] = t; }
  return r;
}
DI u32 ssel(bool c, u32 a, u32 b) { asm("" : "+s"(a)); asm("" : "+s"(b)); return c ? a : b; }
DI u32 smod(u32 v, u32 m) { u32 t = v - m; asm("" : "+s"(t)); return (int)t >= 0 ? t : v; }
DI u32 fresh_lane() { u32 l; asm volatile("v_mbcnt_lo_u32_b32 %0, -1, 0\n\tv_mbcnt_hi_u32_b32 %0, -1, %0" : "=v"(l)); return l; }
template <class T> DI void pin(T& v) { asm volatile("" : "+v"(v)); }
#define MM_HEAD() do { SB(); asm volatile("s_nop 7"); SB(); } while (0)
#define MM_TAIL() do { SB(); asm volatile("s_nop 7"); SB(); } while (0)
DI int toe_addr(int pos) { return 2 * pos + 8 * (pos >> 4); }

#define STAMP(n)
#define STAMP_FLUSH()
#define STAMP_INIT()
#define CNT(n,v)

template <int BAND> struct Band {
  static constexpr int R = 128 - 32 * BAND, NT = R / 16, NS = 15 + NT / 2;
  static constexpr int YO = BAND == 0 ? 0 : BAND == 1 ? 8448 : BAND == 2 ? 14848 : 19200;
  lptr al, ah, bb, yp;
  f4 acc[NT];
  v2u lo[32], hi[32];
  v4u bf[NS];
  DI void init(lptr L, int ch, int lane) {
    const int r = lane & 15, q = lane >> 4, c = (3 - r) & 3;
    const int P = 2048 - 512 * BAND - 496 - 4 * (r >> 2) + 8 * q;
    al = L + L_TOE + ch * TOE_CH + c * TOE_C + toe_addr(P);
    ah = L + L_TOE + ch * TOE_CH + c * TOE_C + toe_addr(P + 4);
    bb = L + L_U + ch * U_CH + 80 * (R / 32) * r + 16 * q;
    yp = L + L_Y + ch * Y_CH + YO + r * (4 * R + 16) + 16 * q;
  }
  DI void load(int s, int i) {
    if (i < 4) {
      const int n = 2 * s + (i >> 1);
      if (n < 32) { if (i & 1) hi[n] = *(LAS v2u*)(ah + 40 * (31 - n)); else lo[n] = *(LAS v2u*)(al + 40 * (31 - n)); }
    } else if (s < NS) bf[s] = *(LAS v4u*)(bb + 80 * (NT / 2 - 1 - s + 15));
  }
  DI void mma(int s, int t) {
    const int n = t - NT + 2 + 2 * s;
    if (s < NS && n >= 0 && n < 32) {
      if (n < 2) m_vv0(acc[t], v4u{lo[n].x, lo[n].y, hi[n].x, hi[n].y}, bf[s]); else m_vv(acc[t], v4u{lo[n].x, lo[n].y, hi[n].x, hi[n].y}, bf[s]);
    }
  }
  DI void flush(int s, int t) {
    const int n = t - NT + 2 + 2 * (s - 1);
    if (n == 30 || n == 31) *(LAS f4*)(yp + 64 * t) = acc[t];
  }
};
template <int B1, int B2, class F> DI void conv2(lptr L, int ch, int lane, F side) {
  Band<B1> u; Band<B2> v;
  u.init(L, ch, lane); v.init(L, ch, lane);
  constexpr int N1 = Band<B1>::NT, NS = Band<B1>::NS;
#pragma unroll
  for (int s = 0; s < 2; ++s)
#pragma unroll
    for (int i = 0; i < 5; ++i) { u.load(s, i); v.load(s, i); SB(); }
  sfor<0, 10 * (NS + 1)>([&](auto z) {
    constexpr int s = z.v / 10, i = z.v % 10;
    if (s >= 17 && i == 0) asm volatile("s_nop %0" ::"i"(s == NS ? 7 : s == NS - 1 ? 5 : 2));
    if (i < N1) { u.mma(s, i); u.flush(s, i); } else { v.mma(s, i - N1); v.flush(s, i - N1); }
    if (i < 5) u.load(s + 2, i); else v.load(s + 2, i - 5);
    side(IC<s>{}, IC<i>{});
    SB();
  });
}

__global__ void __launch_bounds__(256, 1)
fused_kernel(const u16* __restrict__ x, const u16* __restrict__ lar, const u16* __restrict__ aim, const u16* __restrict__ cre,
             const u16* __restrict__ cim, const u16* __restrict__ ldt, const u16* __restrict__ dsk, const u16* __restrict__ win,
             const u16* __restrict__ wsp, const u16* __restrict__ bsp, const u16* __restrict__ wout, const u16* __restrict__ cvec,
             const u16* __restrict__ wmod, u16* __restrict__ out, unsigned char* ws) {
  extern __shared__ __attribute__((aligned(16))) unsigned char smem[];
  lptr L = (lptr)smem;
  const u32 b = blockIdx.x, tid = threadIdx.x, lane = tid & 63;
  STAMP_INIT();
  STAMP(0);
  const u32 w = __builtin_amdgcn_readfirstlane(tid >> 6);
  const u32 xc = b & 7, rr = b >> 3, cp = xc * 32 + rr;
  const u32 c16 = lane & 15, q4 = lane >> 4;
  const u32 tag = *(const CAS u32*)(ws + WS_E) + 1u;
  const u32 tb = 512 * (b >> 6) + 32 * (b & 15) + 2 * ((b >> 4) & 3);
  const v4u sr0 = gld<v4u>(x, (tb + 8 * w) * 1024 + lane * 16), sr1 = gld<v4u>(x, (tb + 8 * w) * 1024 + 1024 + lane * 16);
  const u32 xgo = (tb + 8 * (lane & 3)) * 1024 + (16 * w + (lane >> 2)) * 16;
  const v4u xg0 = gld<v4u>(x, xgo), xg1 = gld<v4u>(x, xgo + 1024);
  const u32 mG = b % 48, mP = min(b / 48, 3u), mrow = mP * 128 + (lane >> 1);
  v4u wm[4];
  u32 cv[4];
#pragma unroll
  for (int n = 0; n < 4; ++n) wm[n] = gld<v4u>(wmod, (mrow + 32 * n) * 6144 + mG * 128 + w * 32 + (lane & 1) * 16);
#pragma unroll
  for (int n = 0; n < 4; ++n) cv[n] = gld<u16>(cvec, (mrow + 32 * n) * 2);
  const u32 pidx = (cp * 256 + tid) * 2;
  const u32 p_lar = gld<u16>(lar, pidx), p_aim = gld<u16>(aim, pidx), p_cr = gld<u16>(cre, pidx), p_ci = gld<u16>(cim, pidx);
  const u32 p_cr2 = gld<u16>(cre, pidx ^ 128), p_ci2 = gld<u16>(cim, pidx ^ 128);
  const u32 p_ldt = *(const CAS u32*)(ldt + 2 * cp), p_dsk = *(const CAS u32*)(dsk + 2 * cp);
  CFENCE();
  u32 pf0 = 0, pf1 = 0;
  {
    u64 pc;
    u32 len;
    asm volatile("s_getpc_b64 %0\nfk_text_a:\n\ts_mov_b32 %1, fk_text_b-fk_text_a" : "=s"(pc), "=s"(len));
    const u32 o = (rr * 16 + tid) * 128;
    if (tid < 16 && o < len) pf0 = gld<u32>((const void*)pc, o);
  }
  CFENCE();
  const u32 pi = (xc >> 1) * 8 + (rr & 7), pj = (xc & 1) * 4 + (rr >> 3);
  const u32 nb = pj < 3 ? 6 : 5, blk0 = 5 * pj + min(pj, 3u);
  const bool two = w + 4 < nb;
  const rsrc_t rs = __builtin_amdgcn_make_buffer_rsrc((void*)ws, 0, WS_END, 0x00020000);
  const v4u SENT = {~0u, ~0u, ~0u, ~0u};
  const int x1w = WS_X1 + ((tid >> 3) * 512 + 2 * cp) * 128 + (tid & 7) * 16;
  const u32 wio_src = ((32 * (pi >> 1) + (lane >> 1)) * 1376 + (pi & 1) * 688 + 16 * (blk0 + w)) * 2 + (lane & 1) * 16;
  const v4u wi0 = gld<v4u>(win, wio_src), wi1 = gld<v4u>(win, two ? wio_src + 128 : wio_src);
  const rsrc_t ro = __builtin_amdgcn_make_buffer_rsrc((void*)wout, 0, 688 * 1024, 0x00020000);
  const v4u wo0 = __builtin_amdgcn_raw_buffer_load_b128(ro, (lane >> 1) * 1024 + (lane & 1) * 16, b < 176 ? (b % 22) * 32768 + (b / 22) * 128 + w * 32 : OOB, 0);
  CFENCE();
  if (tid < 40) *(LAS v4u*)(L + L_TOE + (tid / 5) * TOE_C + 5120 + (tid % 5) * 16) = v4u{0, 0, 0, 0};
  if (tid < 150) *(LAS v4u*)(L + L_U + (tid / 75) * U_CH + (tid % 75) * 16) = v4u{0, 0, 0, 0};
  STAMP(23);
  {
    f2 a[8];
#pragma unroll
    for (int e = 0; e < 4; ++e) { a[2 * e] = f2{bflo(sr0[e]), bflo(sr1[e])}; a[2 * e + 1] = f2{bfhi(sr0[e]), bfhi(sr1[e])}; }
    f2 s = ((a[0] + a[1]) + (a[2] + a[3])) + ((a[4] + a[5]) + (a[6] + a[7]));
    float t = rowsum(sw32sum(s.x, s.y));
    t = sw16sum(t, t) * (1.f / 512);
    auto mm = __builtin_amdgcn_permlane32_swap(__float_as_uint(t), __float_as_uint(t), false, false);
    const f2 m = {__uint_as_float(mm[0]), __uint_as_float(mm[1])};
    s = f2{0.f, 0.f};
#pragma unroll
    for (int e = 0; e < 8; ++e) { const f2 u = a[e] - m; s = __builtin_elementwise_fma(u, u, s); }
    float v = rowsum(sw32sum(s.x, s.y));
    v = sw16sum(v, v) * (1.f / 512);
    st64(rs, (lane & 31) == 0 ? WS_STAT1 + b * 64 + w * 16 + (lane >> 5) * 8 : OOB, v2u{pkbf(t, ln_den(v)), tag});
  }
  {
    const int o = WS_XG + (4 * (16 * w + (lane >> 2))) * 16384 + b * 64 + (lane & 3) * 16;
    st128(rs, o, v4u{xg0.x, tag, xg1.x, tag});
    st128s(rs, o, 16384, v4u{xg0.y, tag, xg1.y, tag});
    st128s(rs, o, 32768, v4u{xg0.z, tag, xg1.z, tag});
    st128s(rs, o, 49152, v4u{xg0.w, tag, xg1.w, tag});
  }
  CFENCE();
  STAMP(22);
  {
    f2 a[4];
#pragma unroll
    for (int n = 0; n < 4; ++n) {
      const float c = bflo(cv[n]);
      const float s = rbf(c * __builtin_amdgcn_rcpf(1.f + __builtin_amdgcn_exp2f(-1.4426950408889634f * c)));
#pragma unroll
      for (int e = 0; e < 4; ++e) a[e] = n ? a[e] + bf2f(wm[n][e]) * s : bf2f(wm[n][e]) * s;
    }
    const float u0 = sw32sum(a[0].x, a[2].x), u1 = sw32sum(a[0].y, a[2].y), u2 = sw32sum(a[1].x, a[3].x), u3 = sw32sum(a[1].y, a[3].y);
    float t0 = sw16sum(u0, u2), t1 = sw16sum(u1, u3);
    t0 = dppadd<0x4E>(dppadd<0x124>(dppadd<0x128>(t0)));
    t1 = dppadd<0x4E>(dppadd<0x124>(dppadd<0x128>(t1)));
    st128(rs, (c16 < 2 && b < 192) ? WS_MOD + (mP * 1536 + mG * 32 + w * 8 + c16 * 4 + q4) * 16 : OOB, v4u{__float_as_uint(t0), tag, __float_as_uint(t1), tag});
  }
  CFENCE();
  STAMP(1);
  const u32 xw = 2 * (w & 1) + (w >> 1);
  const int wio = WS_WINP + (((pj * 2 + (pi & 1)) * 6 + w) * 16 + (pi >> 1)) * 1024 + lane * 16;
  const u32 pf2 = gld<u32>(wsp, (64 * pi + 8 * pj) * 4096 + tid * 128);
  st128(rs, x1w, SENT);
  st128(rs, x1w + 128, SENT);
  CFENCE();
  const rsrc_t rw = __builtin_amdgcn_make_buffer_rsrc((void*)wsp, 0, 2048 * 4096, 0x00020000);
  const u32 wvo = (lane >> 3) * 4096 + (lane & 7) * 16;
  const u32 wsb[4] = {(64 * pi + 16 * xw) * 4096 + w * 1024, (64 * pi + 16 * (xw ^ 1)) * 4096 + w * 1024,
                      (64 * pi + 16 * (xw ^ 2)) * 4096 + w * 1024, (64 * pi + 16 * (xw ^ 3)) * 4096 + w * 1024};
  v4u wraw[64], wsf[16][4];
  auto rq = [&](int g) { return (g + pj) & 3; };
  auto ldw = [&](int n) { wraw[n] = __builtin_amdgcn_raw_buffer_load_b128(rw, (wvo + rq(n >> 4) * 256) + ((n >> 3) & 1) * 128, wsb[(n & 7) >> 1] + (n & 1) * 32768, 0); };
  const u32 wwa = (u32)(size_t)L + L_SHF + w * 8192 + (lane >> 3) * 128 + ((lane & 7) ^ (lane >> 3)) * 16;
  auto shuf = [&](auto Z) {
    constexpr int c = Z.v >> 4, j = Z.v & 15, i = j & 7;
    if constexpr (j < 8) asm volatile("ds_write_b128 %0, %1 offset:%2" ::"v"(wwa), "a"(wraw[8 * c + i]), "i"(i * 1024) : "memory");
    else wsf[2 * c + (i & 1)][i >> 1] = *(LAS v4u*)(L + L_SHF + w * 8192 + (i >> 1) * 2048 + c16 * 128 + (((4 * (i & 1) + q4) ^ (c16 & 7)) * 16));
  };
  float kscale;
  {
    const float L2E = 1.4426950408889634f;
    const float dt = rbf(__builtin_amdgcn_exp2f(((w >> 1) ? bfhi(p_ldt) : bflo(p_ldt)) * L2E));
    const float al2 = (-rbf(__builtin_amdgcn_exp2f(bflo(p_lar) * L2E)) * dt) * L2E;
    const float rev = (bflo(p_aim) * dt) * 0.15915494309189535f;
    u32 mx = max(max(p_cr & 0x7fffu, p_ci & 0x7fffu), max(p_cr2 & 0x7fffu, p_ci2 & 0x7fffu));
    mx = max(mx, dppu<0xB1>(mx)); mx = max(mx, dppu<0x4E>(mx)); mx = max(mx, dppu<0x141>(mx)); mx = max(mx, dppu<0x140>(mx));
    { auto r = __builtin_amdgcn_permlane16_swap(mx, mx, false, false); mx = max(r[0], r[1]); }
    { auto r = __builtin_amdgcn_permlane32_swap(mx, mx, false, false); mx = max(r[0], r[1]); }
    u32 ex = mx >> 7;
    ex = min(max(ex, 1u), 253u);
    const float dn = __uint_as_float((254u - ex) << 23), up = __uint_as_float(ex << 23);
    kscale = 2.0f * dt * up;
    LAS u32* tp = (LAS u32*)(L + L_TAB + (tid >> 7) * TAB_CH + (tid & 127) * 4);
    f2 U[17], Y[16], V[8], X[31], wp[4], t[31];
    u32 pk[31];
    wp[0] = cexp_p(al2, rev, 1.f);
    Y[1] = cexp_p(al2, -rev, 16.f);
    V[1] = cexp_p(al2, rev, 256.f);
    Y[0] = f2{1.f, 0.f};
    U[0] = f2{bflo(p_cr) * dn, bflo(p_ci) * dn};
    SB();
    FORU(l, 4) {
      const int h = 1 << l;
      FORU(i, h) t[i] = cm1(U[i], wp[l]);
      if (l < 3) { t[8] = cm1(wp[l], wp[l]); t[9] = cm1(Y[h], Y[h]); }
      if (l < 2) t[10] = cm1(V[h], V[h]);
      FORU(i, h - 1) t[11 + i] = cm1(Y[1 + i], Y[h]);
      if (l < 3) { FORU(i, h - 1) t[18 + i] = cm1(V[1 + i], V[h]); }
      SB();
      FORU(i, h) U[h + i] = cm2(U[i], wp[l], t[i]);
      if (l < 3) { wp[l + 1] = cm2(wp[l], wp[l], t[8]); Y[2 * h] = cm2(Y[h], Y[h], t[9]); }
      if (l < 2) V[2 * h] = cm2(V[h], V[h], t[10]);
      FORU(i, h - 1) Y[h + 1 + i] = cm2(Y[1 + i], Y[h], t[11 + i]);
      if (l < 3) { FORU(i, h - 1) V[h + 1 + i] = cm2(V[1 + i], V[h], t[18 + i]); }
      SB();
    }
    auto xa = [](int i) { return i < 9 ? 1 : i < 19 ? 2 + (i - 9) / 5 : 4 + (i - 19) / 3; };
    auto xo = [](int i) { return i < 9 ? 2 * i : i < 19 ? 4 * ((i - 9) % 5) : 8 * ((i - 19) % 3); };
    u32 py[16], pu[16];
    t[0] = cm1(U[8], wp[3]);
    FORU(i, 16) { py[i] = pkh(Y[i]); if (i == 2) U[16] = cm2(U[8], wp[3], t[0]); if (i & 1) tp[(47 + (i >> 1)) * (TABP / 4)] = py[i >> 1]; SB(); }
    FORU(i, 16) { pu[i] = pkh(U[i]); if (i & 1) tp[(55 + (i >> 1)) * (TABP / 4)] = py[8 + (i >> 1)]; SB(); }
    FORU(i, 31) { t[i] = cm1(U[xo(i)], V[xa(i)]); if (!(i & 1)) tp[(i >> 1) * (TABP / 4)] = pu[i >> 1]; SB(); }
    FORU(i, 35) {
      if (i < 31) X[i] = cm2(U[xo(i)], V[xa(i)], t[i]);
      if (i >= 2 && i < 33) pk[i - 2] = pkh(X[i - 2]);
      if (i >= 4) tp[(12 + i) * (TABP / 4)] = pk[i - 4];
      SB();
    }
  }
  *(LAS v4u*)(L + L_PKI + w * 1024 + lane * 16) = wi0;
  if (w < 2) *(LAS v4u*)(L + L_PKI + 4096 + w * 1024 + lane * 16) = wi1;
  *(LAS v4u*)(L + L_PKW + w * 8192 + lane * 16) = wo0;
  v4u gx[4], gs[4], gm[3];
  const int mo = WS_MOD + ((lane & 3) * 1536 + 16 * (pi >> 1) + (lane >> 2)) * 16;
  const int mo2 = WS_MOD + ((lane & 3) * 1536 + 256 * min(lane >> 2, 2u) + cp) * 16;
  auto s1_ld = [&](int n) {
    if (n < 4) gx[n] = ld128s(rs, lane * 16 + n * 1024, WS_XG + cp * 16384 + w * 4096);
    else if (n < 8) gs[n - 4] = ld128s(rs, lane * 16 + (n - 4) * 1024, WS_STAT1 + w * 4096);
    else gm[n - 8] = n == 8 ? ld128(rs, mo2) : ld128s(rs, mo, (n == 9 ? 768 : 1024) * 16);
  };
  FORU(n, 3) s1_ld(n);
  __syncthreads();
  STAMP(2);
  f4 kacc[4];
  v2u pl[3], ph[3];
  {
    const u32 hi = w & 1;
    lptr tb_ = L + L_TAB + (w >> 1) * TAB_CH + 16 * q4;
    const u32 i2 = (c16 + ((c16 >> 1) & 1)) >> 1, i4 = (c16 + 1 + ((c16 >> 2) & 1)) >> 2, i8 = (c16 + 3 + ((c16 >> 3) & 1)) >> 3;
    const u32 row[4] = {hi ? 35 + i8 : c16, hi ? 38 + i8 : 16 + i2, hi ? 41 + i8 : 25 + i4, hi ? 44 + i8 : 30 + i4};
    v4u yf[8], xf[8][4];
    auto ldk = [&](int k, int i) {
      if (k < 8) { if (i < 4) xf[k][i] = *(LAS v4u*)(tb_ + row[i] * TABP + 64 * k); else yf[k] = *(LAS v4u*)(tb_ + (47 + c16) * TABP + 64 * k); }
    };
    FORU(k, 2) FORU(i, 5) ldk(k, 4 - i);
    SB();
    const u32 po = (8 * q4 + (c16 >> 2)) * 32 + 8 * (c16 & 3);
    FORU(k, 8) FORU(a, 4) {
      if (k) asm volatile("v_mfma_f32_16x16x32_f16 %0, %1, %2, %0" : "+v"(kacc[3 - a]) : "v"(xf[k][3 - a]), "v"(yf[k]));
      else asm volatile("v_mfma_f32_16x16x32_f16 %0, %1, %2, 0" : "=&v"(kacc[3 - a]) : "v"(xf[k][3 - a]), "v"(yf[k]));
      if (a == 0) ldk(k + 2, 4);
      ldk(k + 2, 3 - a);
      if (k < 4 && (a & 1)) s1_ld(3 + 2 * k + (a >> 1));
      if (k >= 6 && a < 3) {
        lptr p = L + (a == 0 ? L_PKI + w * 1024 : a == 1 ? L_PKI + 4096 + (w & 1) * 1024 : L_PKW + w * 8192) + po;
        if (k == 6) pl[a] = trread(p); else ph[a] = trread(p + 128);
      }
      SB();
    }
  }
  MM_TAIL();
  f2 pv[8];
  {
    const int i0 = 2044 - (1024 * (w & 1) + 768 + 16 * c16 + 4 * q4);
    lptr t0 = L + L_TOE + (w >> 1) * TOE_CH + toe_addr(i0), t1 = L + L_TOE + (w >> 1) * TOE_CH + toe_addr(i0 + 4);
    u32 P0[4], P1[4], M[4];
    FORU(a, 4) kacc[a] *= kscale;
    SB();
    FORU(a, 4) { P0[a] = pkbf(kacc[a].w, kacc[a].z); P1[a] = pkbf(kacc[a].y, kacc[a].x); M[a] = pkbf(kacc[a].z, kacc[a].y); }
    SB();
    FORU(a, 4) {
      lptr p0 = t0 + 640 * (3 - a), p1 = t1 + 640 * (3 - a);
      *(LAS v2u*)(p1) = v2u{P0[a], P1[a]};
      *(LAS u16*)(p0 + TOE_C + 6) = (u16)P0[a]; *(LAS u32*)(p1 + TOE_C) = M[a]; *(LAS u16*)(p1 + TOE_C + 4) = (u16)(P1[a] >> 16);
      *(LAS u32*)(p0 + 2 * TOE_C + 4) = P0[a]; *(LAS u32*)(p1 + 2 * TOE_C) = P1[a];
      *(LAS u16*)(p0 + 3 * TOE_C + 2) = (u16)P0[a]; *(LAS u32*)(p0 + 3 * TOE_C + 4) = M[a]; *(LAS u16*)(p1 + 3 * TOE_C) = (u16)(P1[a] >> 16);
      FORU(j, 2) { pv[2 * a + j] = bf2f((a & 1) ? ph[a >> 1][j] : pl[a >> 1][j]); pin(pv[2 * a + j]); }
      SB();
    }
    st128s(rs, lane * 16, b < 176 ? WS_WOP + (b * 4 + w) * 1024 : OOB, v4u{pl[2].x, pl[2].y, ph[2].x, ph[2].y});
  }
  asm volatile("" ::"v"(pf0), "v"(pf2));
  STAMP(3);
  auto s1_bad = [&]() {
    u32 mx = umax3(gm[0].y, gm[0].w, tag), mn = umin3(gm[0].y, gm[0].w, tag);
    FORU(k, 2) { mx = umax3(mx, gm[1 + k].y, gm[1 + k].w); mn = umin3(mn, gm[1 + k].y, gm[1 + k].w); }
    FORU(k, 4) {
      mx = umax3(umax3(mx, gx[k].y, gx[k].w), gs[k].y, gs[k].w);
      mn = umin3(umin3(mn, gx[k].y, gx[k].w), gs[k].y, gs[k].w);
    }
    return __any(mx != mn);
  };
  if (s1_bad()) {
#pragma nounroll
    for (int sp = 0; sp < SPIN_CAP; ++sp) {
      asm volatile("" ::: "memory");
      FORU(n, 11) s1_ld(n);
      CNT(29, 1);
      if (!s1_bad()) break;
    }
  }
  STAMP(4);
  u32 xv[8];
  u32 h0[4], h1[4];
  u32 gav;
  {
    float qs[6];
    f2 m[4], r[4], a[4], c[4];
    int wn = 0;
    auto fill = [&]() { SB(); ldw(wn); ++wn; SB(); };
    FORU(i, 6) qs[i] = dppadd<0xB1>(__uint_as_float((i & 1) ? gm[i >> 1].z : gm[i >> 1].x));
    FORU(k, 4) { xv[2 * k] = gx[k].x; xv[2 * k + 1] = gx[k].z; r[k] = f2{bfhi(gs[k].x), bfhi(gs[k].z)}; }
    fill();
    FORU(i, 6) qs[i] = dppadd<0x4E>(qs[i]);
    FORU(k, 4) m[k] = f2{bflo(gs[k].x), bflo(gs[k].z)};
    fill();
    const u32 mv = pkbf(qs[0], qs[1]), sbp = pkbf(qs[2], qs[3]), cbp = pkbf(qs[4], qs[5]);
    FORU(k, 4) r[k] = f2{__builtin_amdgcn_rcpf(r[k].x), __builtin_amdgcn_rcpf(r[k].y)};
    fill();
    const u32 sav = __builtin_amdgcn_readlane(mv, 0), cav = __builtin_amdgcn_readlane(mv, 4);
    gav = __builtin_amdgcn_readlane(mv, 8);
    const u32 scp = pkbf(bf2f(cbp) + 1.0f);
    FORU(k, 4) { a[k] = f2{bflo(gx[k].x), bflo(gx[k].z)}; c[k] = f2{bfhi(gx[k].x), bfhi(gx[k].z)}; }
    fill();
    const u32 s1v = pkbf(bf2f(cav) + 1.0f);
    FORU(k, 4) { a[k] -= m[k]; c[k] -= m[k]; }
    const u32 scb[4] = {dppu<0x150>(scp), dppu<0x154>(scp), dppu<0x158>(scp), dppu<0x15C>(scp)};
    fill();
    FORU(k, 4) { a[k] *= r[k]; c[k] *= r[k]; }
    f2 sc[4], sb[4], t[8], bw[2], sw[2];
    FORU(j, 4) sc[j] = bf2f(scb[j]);
    fill();
    FORU(k, 4) { a[k] = a[k] * bflo(s1v) + bflo(sav); c[k] = c[k] * bfhi(s1v) + bfhi(sav); }
    FORU(j, 8) t[j] = pv[j] * sc[j & 3];
    fill();
    v4u wpk[2];
    FORU(k, 4) { h0[k] = pkbf(a[k]); h1[k] = pkbf(c[k]); }
    FORU(j, 8) wpk[j >> 2][j & 3] = pkbf(t[j]);
    fill();
    lptr up = L + L_U + 80 * (15 + (tid >> 2)) + 16 * (tid & 3);
    *(LAS v4u*)up = v4u{h0[0], h0[1], h0[2], h0[3]};
    *(LAS v4u*)(up + U_CH) = v4u{h1[0], h1[1], h1[2], h1[3]};
    st128(rs, wio, wpk[0]);
    st128s(rs, wio, two ? 65536 : OOB, wpk[1]);
    const u32 sbb[4] = {dppu<0x150>(sbp), dppu<0x154>(sbp), dppu<0x158>(sbp), dppu<0x15C>(sbp)};
    FORU(j, 8) t[j] = bf2f(wpk[j >> 2][j & 3]);
    SB();
    FORU(j, 4) sb[j] = bf2f(sbb[j]);
    FORU(f, 2) sw[f] = (t[4 * f] + t[4 * f + 1]) + (t[4 * f + 2] + t[4 * f + 3]);
    SB();
    FORU(j, 8) pv[j] *= sb[j & 3];
    SB();
    FORU(f, 2) bw[f] = (pv[4 * f] + pv[4 * f + 1]) + (pv[4 * f + 2] + pv[4 * f + 3]);
    const float e0 = sw16sum(bw[0].x + bw[0].y, sw[0].x + sw[0].y), e1 = sw16sum(bw[1].x + bw[1].y, sw[1].x + sw[1].y);
    const int bwo = WS_BWP + (((pj * 2 + (pi & 1)) * 6 + w) * 16 + (pi >> 1)) * 128 + (lane & 31) * 4;
    __builtin_amdgcn_raw_buffer_store_b32(__float_as_uint(sw32sum(e0, e1)), rs, lane < 32 ? bwo : two ? bwo + 8192 : OOB, 0, SC1);
  }
  __syncthreads();
  STAMP(5);
  v4u cdv;
  const u32 fpo = lane < 48 ? WS_WINP + pj * 196608 + (((pi & 7) * 4 + w) * 48 + lane) * 128 : OOB;
  auto side = [&](auto S, auto I) {
    constexpr int s = S.v, i = I.v, z = 10 * s + i;
    if constexpr (z % 4 == 1 && z / 4 < 40) ldw(8 + z / 4);
    if (z == 43) {
      asm volatile("s_waitcnt vmcnt(11)" ::: "memory");
      __builtin_amdgcn_raw_buffer_store_b32(tag, rs, lane == 0 ? WS_CD + b * 64 + w * 4 : OOB, 0, SC1);
    }
    if (z == 123) pf1 = __builtin_amdgcn_raw_buffer_load_b32(rs, fpo, 0, SC1);
    if (z == 163) cdv = ld128(rs, WS_CD + tid * 64);
  };
  if (w & 1) conv2<1, 2>(L, w >> 1, lane, side);
  else conv2<0, 3>(L, w >> 1, lane, side);
  STAMP(21);
  {
    f2 y[2][4], t[2][4];
    const float ga0 = bflo(gav), ga1 = bfhi(gav);
    FORU(k, 4) { t[0][k] = bf2f(h0[k]); t[1][k] = bf2f(h1[k]); }
    FORU(k, 4) { y[0][k] = f2{bflo(xv[2 * k]), bflo(xv[2 * k + 1])}; y[1][k] = f2{bfhi(xv[2 * k]), bfhi(xv[2 * k + 1])}; }
    lptr yp[4];
    FORU(B, 4) {
      const u32 tp = 8 * tid - 512 * B;
      yp[B] = L + L_Y + (B == 0 ? 0 : B == 1 ? 8448 : B == 2 ? 14848 : 19200) + ((tp & 2047) / (128 - 32 * B)) * 16 + tp * 4;
      pin(yp[B]);
    }
    SB();
    FORU(k, 4) { t[0][k] *= bflo(p_dsk); t[1][k] *= bfhi(p_dsk); pin(y[0][k]); pin(y[1][k]); pin(t[0][k]); pin(t[1][k]); }
    __syncthreads();
    STAMP(6);
    f4 q[4][4];
    FORU(B, 4) {
      if (B <= (int)w) { FORU(e, 4) q[B][e] = *(LAS f4*)(yp[B] + (e >> 1) * Y_CH + (e & 1) * 16); }
      else { FORU(e, 4) q[B][e] = f4{0.f, 0.f, 0.f, 0.f}; }
    }
    FORU(B, 4) { FORU(e, 4) { t[e >> 1][2 * (e & 1)] += f2{q[B][e].x, q[B][e].y}; t[e >> 1][2 * (e & 1) + 1] += f2{q[B][e].z, q[B][e].w}; } SB(); }
    FORU(k, 4) { y[0][k] += t[0][k] * ga0; y[1][k] += t[1][k] * ga1; }
    SB();
    v4u o0, o1;
    FORU(k, 4) { o0[k] = pkbf(y[0][k]); o1[k] = pkbf(y[1][k]); }
    SB();
    st128(rs, x1w, san4(o0));
    st128(rs, x1w + 128, san4(o1));
  }
  CFENCE();
  asm volatile("" ::"v"(pf1));
  auto cd_bad = [&]() { return __any(umax3(umax3(cdv.x, cdv.y, cdv.z), cdv.w, tag) != umin3(umin3(cdv.x, cdv.y, cdv.z), cdv.w, tag)); };
  if (cd_bad()) {
#pragma nounroll
    for (int sp = 0; sp < SPIN_CAP; ++sp) {
      asm volatile("" ::: "memory");
      cdv = ld128(rs, WS_CD + tid * 64);
      if (!cd_bad()) break;
    }
  }
  __syncthreads();
  STAMP(7);
  const u32 blkS = 4 + (w >> 1);
  const bool hasS = blkS < nb;
  const u32 fo[4] = {((pj * 2 + 1) * 6 + w), ((pj * 2 + 1) * 6 + (hasS ? blkS : 4)), (pj * 12 + w), (pj * 12 + (hasS ? blkS : 4))};
  v4u ones = {0x3f803f80u, 0x3f803f80u, 0x3f803f80u, 0x3f803f80u};
  pin(ones);
  v4u wf[4][16];
  auto ldf = [&](int s, int k) { wf[s][k] = ld128s(rs, lane * 16 + (k & 3) * 1024, WS_WINP + fo[s] * 16384 + (k >> 2) * 4096); };
  f4 sW[4];
  f4 bW[4];
  auto ldb = [&](int s) {
    bW[s] = __builtin_bit_cast(f4, ld128s(rs, c16 * 128 + q4 * 16, WS_BWP + fo[s] * 2048));
    sW[s] = __builtin_bit_cast(f4, ld128s(rs, c16 * 128 + q4 * 16, WS_BWP + fo[s] * 2048 + 64));
  };
  auto redb = [&](int s) {
    FORU(e, 16) { f4& v = (e & 4) ? sW[s + (e >> 3)] : bW[s + (e >> 3)]; v[e & 3] = dppadd<0xB1>(v[e & 3]); }
    SB();
    FORU(e, 16) { f4& v = (e & 4) ? sW[s + (e >> 3)] : bW[s + (e >> 3)]; v[e & 3] = dppadd<0x4E>(v[e & 3]); }
    SB();
    FORU(e, 16) { f4& v = (e & 4) ? sW[s + (e >> 3)] : bW[s + (e >> 3)]; v[e & 3] = dppadd<0x141>(v[e & 3]); }
    SB();
    FORU(e, 16) { f4& v = (e & 4) ? sW[s + (e >> 3)] : bW[s + (e >> 3)]; v[e & 3] = dppadd<0x140>(v[e & 3]); }
    SB();
  };
  {
    ldb(0); ldb(1);
    v4u xr[16];
    const int xo = WS_X1 + pi * 65536 + w * 16384;
    sfor<0, 32>([&](auto z) {
      constexpr int n = z.v;
      if constexpr (n < 16) ldf(n & 1, n >> 1); else xr[n - 16] = ld128s(rs, lane * 16 + (n & 3) * 1024, xo + rq((n - 16) >> 2) * 4096);
      shuf(IC<2 * n>{}); shuf(IC<2 * n + 1>{});
      SB();
    });
    redb(0);
    pin(bW[0]); pin(bW[1]); pin(sW[0]); pin(sW[1]);
    STAMP(8);
    lptr xi = L + L_XIMG + w * 16384 + (lane >> 3) * 128 + (lane & 1) * 16;
    const u32 xs0 = (((lane & 7) >> 1) ^ ((lane >> 4) & 1)) * 32;
    u32 m = 0;
    FORU(k, 16) { *(LAS v4u*)(xi + rq(k >> 2) * 4096 + (k & 3) * 1024 + (xs0 ^ ((k & 1) * 64))) = xr[k]; m = vmax(m, xr[k]); ldf(k & 1, 8 + (k >> 1)); SB(); }
    if (__any(m == ~0u)) {
#pragma nounroll
      for (int sp = 0; sp < SPIN_CAP; ++sp) {
        asm volatile("" ::: "memory");
        FORU(k, 16) xr[k] = ld128s(rs, lane * 16, xo + rq(k >> 2) * 4096 + (k & 3) * 1024);
        SB();
        m = 0;
        FORU(k, 16) { *(LAS v4u*)(xi + rq(k >> 2) * 4096 + (k & 3) * 1024 + (xs0 ^ ((k & 1) * 64))) = xr[k]; m = vmax(m, xr[k]); SB(); }
        CNT(29, 100);
        if (!__any(m == ~0u)) break;
      }
    }
    STAMP(9);
  }
  __syncthreads();
  STAMP(10);
  const u32 xsw = ((c16 >> 3) & 1) | ((q4 & 1) << 1), xbo = (8 * q4 + (c16 >> 2)) * 128 + 8 * (c16 & 3);
  lptr xb[4];
#pragma unroll
  for (int t = 0; t < 4; ++t) xb[t] = L + xbo + ((t ^ xw ^ xsw) * 32);
  v2u bl[3][4], bh[3][4];
  auto ldx = [&](int k, int i) {
    if (k < 16) { if (i & 1) bh[k % 3][i >> 1] = trread(xb[i >> 1] + 4096 * k + 512); else bl[k % 3][i >> 1] = trread(xb[i >> 1] + 4096 * k); }
  };
  auto bx = [&](int k, int t) { return v4u{bl[k % 3][t].x, bl[k % 3][t].y, bh[k % 3][t].x, bh[k % 3][t].y}; };
  auto xwait = [&](int k) { asm volatile("" ::"v"(bh[k % 3][3])); };
  const int pco = WS_PB + ((pi * 4 + w) * 44 + blk0) * 512, pc2 = 128 + lane < 32 * nb ? lane * 16 : OOB;
  const int zco = WS_Z2N + ((pj * 6 + (w >> 1)) * 64 + 2 * pi + (w & 1)) * 1024, zc2 = w + 8 < 2 * nb ? zco + 262144 : OOB;
  f4 z2a[6];
  f2 rn[4];
  {
    f4 st0, st1;
    v2u sl[16], sh[16];
    FORU(k, 4) { sl[k] = trread(xb[0] + 4096 * k); sh[k] = trread(xb[0] + 4096 * k + 512); }
    MM_HEAD();
    sfor<0, 16>([&](auto z) {
      constexpr int k = z.v;
      const v4u v = {sl[k].x, sl[k].y, sh[k].x, sh[k].y};
      if (k) m_vv(st0, ones, v); else m_vv0(st0, ones, v);
      if constexpr (k + 4 < 16) sl[k + 4] = trread(xb[0] + 4096 * (k + 4)); else { ldx((k - 12) >> 1, 4 * (k & 1)); ldx((k - 12) >> 1, 4 * (k & 1) + 1); }
      SB();
      if (k) m_vv(st1, v, v); else m_vv0(st1, v, v);
      if constexpr (k + 4 < 16) sh[k + 4] = trread(xb[0] + 4096 * (k + 4) + 512); else { ldx((k - 12) >> 1, 4 * (k & 1) + 2); ldx((k - 12) >> 1, 4 * (k & 1) + 3); }
      if (k == 1) st128s(rs, lane * 16, pco, SENT);
      if (k == 5) st128s(rs, lane * 16, pco + 1024, SENT);
      if (k == 9) st128s(rs, pc2, pco + 2048, SENT);
      if (k == 3) st128s(rs, lane * 16, zco, SENT);
      if (k == 7) st128s(rs, lane * 16, zco + 131072, SENT);
      if (k == 11) st128s(rs, lane * 16, zc2, SENT);
      SB();
    });
    float sv0 = 0.f, sv1 = 0.f;
    FORU(k, 16) FORU(i, 6) {
      const int u = i == 0 ? 0 : i == 1 ? 4 : i == 2 ? 1 : i == 3 ? 5 : i - 2;
      if (i == 0) xwait(k);
      if (k) m_vv(z2a[u], wf[u >> 2][k], bx(k, u & 3)); else m_vv0(z2a[u], wf[u >> 2][k], bx(k, u & 3));
      if (i < 2) { ldx(k + 2, 2 * i); ldx(k + 2, 2 * i + 1); } else ldx(k + 2, i + 2);
      if (i == 2 && k) ldf(2 + ((k - 1) & 1), (k - 1) >> 1);
      if (i == 3 || i == 5) {
        const int z = 2 * k + (i == 5);
        if (z == 4) sv1 = (c16 & 1) ? st1.y : st1.x;
        if (z == 5) sv0 = (c16 & 1) ? st1.w : st1.z;
        if (z == 6) sv1 = (c16 & 2) ? sv0 : sv1;
        if (z == 7) { pin(st0); sv0 = st0.x * (1.f / 512); }
        if (z == 8) sv1 *= (1.f / 512);
        if (z == 9) sv1 = fmaxf(__builtin_fmaf(-sv0, sv0, sv1), 0.f);
        if (z == 10) sv1 = rbf(sv1);
        if (z == 11) sv0 = rbf(sv0);
        if (z == 12) sv1 = rbf(sv1 + 1e-5f);
        if (z == 13) sv1 = __builtin_amdgcn_sqrtf(sv1);
        if (z == 14) sv1 = rbf(sv1);
        if (z == 15) sv1 = __builtin_amdgcn_rcpf(sv1);
        if (z == 16) sv0 *= -sv1;
        if (z == 17) *(volatile LAS f2*)(L + (q4 == (c16 >> 2) ? L_XST + (16 * xw + c16) * 8 : L_XST + 512 + lane * 8)) = f2{sv1, sv0};
        if (z >= 4 && z <= 16) { pin(sv0); pin(sv1); }
        if (z == 24) __builtin_amdgcn_s_barrier();
        if (z >= 26 && z < 30) rn[z - 26] = *(volatile LAS f2*)(L + L_XST + (16 * ((z - 26) ^ xw) + c16) * 8);
      }
      SB();
    }
    MM_TAIL();
  }
  STAMP(24);
  auto shuf2 = [&](auto C, auto N) {
    constexpr int n = N.v % 12, z = 16 * (C.v + N.v / 12);
    if constexpr (N.v < 24) { if constexpr (n < 8) shuf(IC<z + n>{}); else { shuf(IC<z + 2 * n - 8>{}); shuf(IC<z + 2 * n - 7>{}); } }
  };
  auto epi = [&](f4 (&a)[6], int s0, auto fill) {
    f2 v[12];
    sfor<0, 2>([&](auto z) {
      FORU(j, 6) {
        const int i = 6 * z.v + j;
        const f4 &sw = sW[s0 + (i >> 3)], &bw = bW[s0 + (i >> 3)];
        v[i] = ((i & 1) ? f2{sw.z, sw.w} : f2{sw.x, sw.y}) * rn[(i >> 1) & 3].y + ((i & 1) ? f2{bw.z, bw.w} : f2{bw.x, bw.y});
      }
      fill(IC<z.v>{});
    });
    sfor<0, 2>([&](auto z) {
      FORU(j, 6) { const int i = 6 * z.v + j; v[i] = ((i & 1) ? f2{a[i >> 1].z, a[i >> 1].w} : f2{a[i >> 1].x, a[i >> 1].y}) * rn[(i >> 1) & 3].x + v[i]; }
      fill(IC<2 + z.v>{});
    });
    gelu_n<12>(v, fill);
    FORU(i, 6) { a[i] = f4{v[2 * i].x, v[2 * i].y, v[2 * i + 1].x, v[2 * i + 1].y}; pin(a[i]); }
  };
  {
    epi(z2a, 0, [&](auto N) {
      constexpr int n = N.v;
      SB();
      if constexpr ((n & 1) && n < 22) ldf(2 + ((15 + n / 2) & 1), (15 + n / 2) >> 1);
      if constexpr (n >= 22) ldw(26 + n);
      shuf2(IC<4>{}, N);
      SB();
    });
    if (!hasS) { z2a[4] = f4{0.f, 0.f, 0.f, 0.f}; z2a[5] = z2a[4]; }
    f2 zs[6], zq[6];
    FORU(i, 6) { zs[i] = f2{z2a[i].x, z2a[i].y} + f2{z2a[i].z, z2a[i].w}; zq[i] = f2{z2a[i].x, z2a[i].y} * f2{z2a[i].x, z2a[i].y}; }
    SB();
    FORU(i, 6) zq[i] = __builtin_elementwise_fma(f2{z2a[i].z, z2a[i].w}, f2{z2a[i].z, z2a[i].w}, zq[i]);
    FORU(i, 2) zs[i] += zs[4 + i];
    SB();
    float us[4], uq[4], u[4];
    FORU(i, 2) zq[i] += zq[4 + i];
    FORU(t, 4) us[t] = zs[t].x + zs[t].y;
    SB();
    FORU(t, 4) uq[3 - t] = zq[3 - t].x + zq[3 - t].y;
    SB();
    FORU(t, 4) u[3 - t] = sw32sum(us[3 - t], uq[3 - t]);
    LAS float* zp = (LAS float*)(L + L_ZST) + (w * 2 + (lane >> 5)) * 64 + c16;
    zp[16 * ((2 * (q4 & 1) + 1) ^ xw)] = sw16sum(u[1], u[3]);
    zp[16 * ((2 * (q4 & 1)) ^ xw)] = sw16sum(u[0], u[2]);
  }
  asm volatile("s_waitcnt vmcnt(20)" ::: "memory");
  __syncthreads();
  if (w == 0) {
    LAS float* zp = (LAS float*)(L + L_ZST) + lane;
    const float s = (zp[0] + zp[128]) + (zp[256] + zp[384]), ss = (zp[64] + zp[192]) + (zp[320] + zp[448]);
    st128(rs, WS_ST2 + ((pi * 8 + pj) * 64 + lane) * 16, v4u{__float_as_uint(s), tag, __float_as_uint(ss), tag});
  }
  __builtin_amdgcn_raw_buffer_store_b32(tag, rs, tid == 64 ? WS_F3 + (pj * 32 + pi) * 128 : OOB, 0, SC1);
  CFENCE();
  STAMP(11);
  v4u wo[6][4];
  const u32 wob = WS_WOP + (pj * 22 + 6 * w) * 4096, wobh = ssel(w < 3, wob, OOB);
  auto ldo = [&](int n) { wo[n >> 2][n & 3] = ld128s(rs, lane * 16 + (n & 3) * 1024, (n < 16 ? wob : wobh) + (n >> 2) * 4096); };
  f4 z1a[6];
  v2u xres[4];
  v4u g[8];
  u32 fv;
  auto f_ld = [&]() { fv = __builtin_amdgcn_raw_buffer_load_b32(rs, (lane & 7) * 128, WS_F3 + (pj * 32 + 8 * w) * 128, SC1); };
  auto s2b_ld = [&](int n) { g[n] = ld128s(rs, (2 * q4 * 64 + c16) * 16 + (n & 1) * 1024, WS_ST2 + pi * 8192 + ((n >> 1) ^ xw) * 256); };
  {
    FORU(i, 16) ldx(i >> 3, i & 7);
    MM_HEAD();
    sfor<0, 96>([&](auto z) {
      constexpr int k = z.v / 6, i = z.v % 6, u = i == 0 ? 0 : i == 1 ? 4 : i == 2 ? 1 : i == 3 ? 5 : i - 2;
      if (i == 0) xwait(k);
      if (k) m_vv(z1a[u], wf[2 + (u >> 2)][k], bx(k, u & 3)); else m_vv0(z1a[u], wf[2 + (u >> 2)][k], bx(k, u & 3));
      if (i < 2) { ldx(k + 2, 2 * i); ldx(k + 2, 2 * i + 1); } else ldx(k + 2, i + 2);
      if (k < 3 && (i == 2 || i == 4)) ldf(1 + (i >> 1), 13 + k);
      if (k >= 3 && k < 9 && i == 3) ldw(49 + k);
      if ((k == 9 || k == 10) && i >= 2) s2b_ld(4 * (k - 9) + i - 2);
      if (k >= 11 && k < 14 && (i == 2 || i == 4)) ldw(58 + 2 * (k - 11) + (i >> 2));
      if (k == 14 && (i == 2 || i == 4)) ldb(1 + (i >> 1));
      if (k == 15 && i == 3) f_ld();
      SB();
    });
    MM_TAIL();
#pragma unroll
    for (int n = 0; n < 4; ++n) {
      const u32 row = 16 * n + 4 * q4 + (c16 >> 2);
      xres[n] = trread(L + L_XIMG + pj * 8192 + row * 128 + ((w ^ (((row >> 1) & 1) | (((row >> 3) & 1) << 1))) * 32) + 8 * (c16 & 3));
    }
  }
  STAMP(12);
  {
    auto bad = [&]() { u32 mx = tag, mn = tag; FORU(n, 8) { mx = umax3(mx, g[n].y, g[n].w); mn = umin3(mn, g[n].y, g[n].w); } return __any(mx != mn); };
    if (bad()) {
#pragma nounroll
      for (int sp = 0; sp < SPIN_CAP; ++sp) {
        asm volatile("" ::: "memory");
        FORU(n, 8) s2b_ld(n);
        CNT(29, 10000);
        if (!bad()) break;
      }
    }
    STAMP(13);
    lptr tw = L + L_TRS + w * TRS_W;
    float a[4], c[4];
    FORU(t, 4) { a[t] = __uint_as_float(g[2 * t].x) + __uint_as_float(g[2 * t + 1].x); c[t] = __uint_as_float(g[2 * t].z) + __uint_as_float(g[2 * t + 1].z); }
    SB();
    const float a0 = sw32sum(a[0], a[2]), a1 = sw32sum(a[1], a[3]), c0 = sw32sum(c[0], c[2]), c1 = sw32sum(c[1], c[3]);
    const float m = sw16sum(a0, a1) * (1.f / 688), var = fmaxf(sw16sum(c0, c1) * (1.f / 688) - m * m, 0.f);
    const float r = __builtin_amdgcn_rcpf(ln_den(var)), n0 = -rbf(m) * r;
    auto gather = [&](float v, float (&o)[4]) {
      auto e = __builtin_amdgcn_permlane16_swap(__float_as_uint(v), __float_as_uint(v), false, false);
      auto e0 = __builtin_amdgcn_permlane32_swap(e[0], e[0], false, false);
      auto e1 = __builtin_amdgcn_permlane32_swap(e[1], e[1], false, false);
      o[0] = __uint_as_float(e0[0]); o[2] = __uint_as_float(e0[1]); o[1] = __uint_as_float(e1[0]); o[3] = __uint_as_float(e1[1]);
    };
    gather(r, a); gather(n0, c);
    SB();
    FORU(i, 6) z2a[i] = z2a[i] * a[i & 3] + c[i & 3];
    SB();
    FORU(i, 6) *(LAS v2u*)(tw + (16 * ((i & 3) ^ xw) + c16) * 64 + 8 * (((i & 4) + q4) ^ (c16 >> 1))) = v2u{pkbf(z2a[i].x, z2a[i].y), pkbf(z2a[i].z, z2a[i].w)};
    const u32 r0 = 8 * q4 + (c16 >> 2);
    const int zo = WS_Z2N + (pj * 6 * 64 + 2 * pi) * 1024 + lane * 16;
#pragma unroll
    for (int n = 0; n < 3; ++n) {
      lptr pp = tw + (n < 2 ? n : (w & 1)) * 2048 + r0 * 64;
      const u32 un = (n < 2 ? 0 : 4) + (c16 & 3);
      const v2u lo = trread(pp + 8 * (un ^ ((r0 >> 1) & 7))), hi = trread(pp + 256 + 8 * (un ^ (((r0 + 4) >> 1) & 7)));
      st128s(rs, zo, n < 2 ? w * 65536 + n * 1024 : (hasS ? blkS * 65536 + (w & 1) * 1024 : OOB), san4(v4u{lo.x, lo.y, hi.x, hi.y}));
    }
  }
  CFENCE();
  STAMP(14);
  u32 bsr[4], gbp[2];
  v4u gg[2];
  FORU(t, 4) bsr[t] = gld<u16>(bsp, (64 * pi + 16 * (t ^ xw) + c16) * 2);
  FORU(m, 2) gg[m] = ld128(rs, WS_MOD + ((lane & 3) * 1536 + 1280 + 32 * pj + 16 * m + 8 * (c16 >> 3) + 2 * q4 + ((c16 >> 2) & 1)) * 16);
  if (__any(fv != tag)) {
#pragma nounroll
    for (int sp = 0; sp < SPIN_CAP; ++sp) {
      asm volatile("" ::: "memory");
      f_ld();
      if (!__any(fv != tag)) break;
    }
  }
  CFENCE();
  v2u z1[6];
  f4 ga[2][4], q[20];
  lptr qa[6];
  auto ldq = [&](int n) { q[n] = *(volatile LAS f4*)(n < 12 ? qa[n & 3] + (n >> 2) * 4096 : qa[4 + (n & 1)] + ((n - 12) >> 1) * 4096); };
  {
    const u32 zo = WS_Z2N + (pj * 6 * 64 + 16 * w) * 1024;
    const u32 rot = ((nb == 6 ? 0x1420u : 0x1320u) >> (4 * (pi & 3))) & 15;
    v4u zr[16];
    u32 zv[4];
    FORU(g, 4) zv[g] = lane * 16 + rq(g) * 4096;
    u32 so[6], rsl[6];
    FORU(it, 5) {
      const u32 blk = smod(w + 1 + smod((it ? it + nb - 6 : 0) + rot, nb - 1), nb);
      so[it] = zo + blk * 65536;
      rsl[it + 1] = ssel(blk < 4, blk * 3 + ((w - blk - 1) & 3), 4 * blk - 4 + w) * 4096;
    }
    so[5] = zo + w * 65536;
    auto ldz = [&](int k, u32 s0) { zr[k] = ld128s(rs, zv[k >> 2] + (k & 3) * 1024, s0); };
    FORU(n, 6) ldo(n);
    redb(2);
    epi(z1a, 2, [&](auto N) {
      constexpr int n = N.v;
      SB();
      shuf2(IC<6>{}, N);
      if constexpr (n < 6) ldo(6 + n);
      if constexpr (n >= 6 && n < 22) ldz(n - 6, so[0]);
      SB();
    });
    FORU(i, 6) { z1[i] = v2u{pkbf(z1a[i].x, z1a[i].y), pkbf(z1a[i].z, z1a[i].w)}; pin(z1[i]); }
    FORU(m, 2) { gbp[m] = pkbf(quadsum(__uint_as_float(gg[m].x)), quadsum(__uint_as_float(gg[m].z))); pin(gbp[m]); }
    STAMP(25);
    __syncthreads();
    {
      auto bad = [&]() { u32 m = 0; FORU(k, 16) m = vmax(m, zr[k]); return __any(m == ~0u); };
      if (bad()) {
#pragma nounroll
        for (int sp = 0; sp < SPIN_CAP; ++sp) {
          asm volatile("" ::: "memory");
          FORU(k, 16) ldz(k, so[0]);
          CNT(28, 100);
          if (!bad()) break;
        }
      }
    }
    STAMP(15);
    u32 ra = 0, badm = 0;
    float ck = 0.f;
    v4u oth;
    MM_HEAD();
    auto slot = [&](auto z) {
      constexpr int it = z.v / 64, k = (z.v / 4) % 16, t = z.v % 4, h = it & 1;
      if (k) m_va(ga[h][t], zr[k], wsf[k][t]); else m_va0(ga[h][t], zr[k], wsf[k][t]);
      constexpr int pz = z.v / 4 - 1, pit = pz / 16, pk = pz % 16;
      if constexpr (t == 1 && pz >= 0) {
        if constexpr (pit < 5) ldz(pk, so[pit + 1]);
        else if constexpr (pk < 6) ldo(12 + pk);
      }
      if constexpr (it > 0) {
        if (k == 2 && t == 0) { ck = ga[h ^ 1][0].x + ga[h ^ 1][0].y; pin(ck); }
        if (k == 3 && t == 0) { ck += ga[h ^ 1][0].z; pin(ck); }
        if (k == 4 && t == 0) { ck += ga[h ^ 1][0].w; pin(ck); }
        if (k == 5 && t == 0) badm |= __any(ck != ck) ? 1u << (it - 1) : 0u;
        if (k >= 2 && k < 6 && t == 2) { ra = rsl[it] + lane * 16 + (((k - 2) ^ xw) * 1024); pin(ra); }
        if constexpr (k >= 2 && k < 6 && t == 3) *(volatile LAS f4*)(L + L_RED + ra) = ga[h ^ 1][k - 2];
      }
      if constexpr (it == 5) {
        if constexpr (z.v >= 320 + 28 && z.v < 320 + 34) {
          constexpr int i = z.v - 348;
          u32 l16 = lane * 16;
          pin(l16);
          qa[i] = L + l16 + (L_RED + ((i < 4 ? w * 12 : (blkS * 4 - 4) * 4) + ((i & 3) ^ xw)) * 1024);
          pin(qa[i]);
        }
        if (k == 6 && t == 2) *(volatile LAS u32*)(L + L_XST + w * 4) = badm;
        if (k == 8 && t == 3) { asm volatile("s_waitcnt lgkmcnt(0)" ::: "memory"); __builtin_amdgcn_s_barrier(); }
        if (k == 9 && t == 0) oth = *(volatile LAS v4u*)(L + L_XST);
        if constexpr (z.v >= 320 + 37 && z.v < 320 + 57) ldq(z.v - 357);
      }
      SB();
    };
    if (nb == 6) sfor<0, 64>(slot);
    else { FORU(t, 4) ga[0][t] = f4{0.f, 0.f, 0.f, 0.f}; }
    sfor<64, 384>(slot);
    MM_TAIL();
    { const float t = (ga[1][0].x + ga[1][0].y) + (ga[1][0].z + ga[1][0].w); badm |= __any(t != t) ? 32u : 0u; }
    const u32 anyo = __builtin_amdgcn_readfirstlane(oth.x | oth.y | oth.z | oth.w);
    if (badm | anyo) {
#pragma nounroll
      for (u32 it = 0; it < 6; ++it) {
        if (!((badm >> it) & 1)) continue;
        CNT(28, 1);
        const u32 s0 = it == 0 ? so[0] : it == 1 ? so[1] : it == 2 ? so[2] : it == 3 ? so[3] : it == 4 ? so[4] : so[5];
        const u32 r0 = it == 0 ? rsl[1] : it == 1 ? rsl[2] : it == 2 ? rsl[3] : it == 3 ? rsl[4] : rsl[5];
#pragma nounroll
        for (int sp = 0; sp < SPIN_CAP; ++sp) {
          u32 m = 0;
          FORU(k, 16) ldz(k, s0);
          FORU(k, 16) m = vmax(m, zr[k]);
          asm volatile("" ::: "memory");
          if (!__any(m == ~0u)) break;
        }
        MM_HEAD();
        FORU(k, 16) FORU(t, 4) { if (k) m_va(ga[0][t], zr[k], wsf[k][t]); else m_va0(ga[0][t], zr[k], wsf[k][t]); }
        MM_TAIL();
        FORU(t, 4) {
          if (it < 5) *(LAS f4*)(L + L_RED + r0 + lane * 16 + ((t ^ xw) * 1024)) = ga[0][t];
          else ga[1][t] = ga[0][t];
        }
      }
      if (anyo) __syncthreads();
      FORU(n, 20) ldq(n);
    }
  }
  __builtin_amdgcn_raw_buffer_store_b32(tag, rs, lane == 0 ? 0 : OOB, w == 0 ? WS_H4 + (pi * 8 + pj) * 128 : OOB, SC1);
  STAMP(26);
  {
    const u32 lane = fresh_lane(), c16 = lane & 15, q4 = lane >> 4;
    f2 zf[12], gs2[12];
    auto half = [](const f4& v, int hh) { return hh ? f2{v.z, v.w} : f2{v.x, v.y}; };
    FORU(i, 12) gs2[i] = i < 8 ? half(q[i >> 1], i & 1) + half(q[4 + (i >> 1)], i & 1) : half(q[8 + (i >> 1)], i & 1) + half(q[10 + (i >> 1)], i & 1);
    FORU(i, 12) zf[i] = i < 8 ? half(ga[1][i >> 1], i & 1) + half(q[8 + (i >> 1)], i & 1) : half(q[12 + (i >> 1)], i & 1) + half(q[14 + (i >> 1)], i & 1);
    SB();
    FORU(i, 12) gs2[i] += bflo(bsr[(i >> 1) & 3]);
    SB();
    FORU(i, 12) gs2[i] += zf[i];
    SB();
    FORU(i, 12) zf[i] = bf2f(z1[i >> 1][i & 1]);
    SB();
    FORU(i, 12) gs2[i] *= zf[i];
    SB();
    v2u P[6];
    FORU(i, 12) P[i >> 1][i & 1] = pkbf(gs2[i]);
    SB();
#pragma unroll
    for (int h = 0; h < 3; ++h) {
      auto rx = __builtin_amdgcn_permlane16_swap(P[2 * h].x, P[2 * h + 1].x, false, false);
      auto ry = __builtin_amdgcn_permlane16_swap(P[2 * h].y, P[2 * h + 1].y, false, false);
      const u32 tt = (2 * (h & 1) + (q4 & 1)) ^ xw;
      st128s(rs, WS_PB + ((pi * 4 + tt) * 44 + blk0) * 512 + (q4 >> 1) * 256 + c16 * 16, h < 2 ? w * 512 : (hasS ? blkS * 512 : OOB),
             san4(v4u{rx[0], ry[0], rx[1], ry[1]}));
    }
    FORU(n, 6) ldo(18 + n);
  }
  STAMP(17);
  {
    const u32 lane = fresh_lane(), c16 = lane & 15, q4 = lane >> 4;
    f4 oa[2][4];
    v4u pr[4][6];
    u32 pb[4], pbh[4];
    u32 rd[3];
    FORU(u, 4) {
      const u32 d = u < 3 ? 1 + smod(u + smod(pj & 3, 3), 3) : 4;
      pb[u] = WS_PB + ((pi * 4 + ((w + d) & 3)) * 44 + 12 * w) * 512; pbh[u] = ssel(w < 3, pb[u] + 4096, OOB);
      if (u < 3) rd[u] = L_RED2 + (((w + d) & 3) * 3 + 3 - d) * 4096 + lane * 16;
    }
    const u32 pv3 = (w == 3 && lane >= 32) ? OOB : lane * 16 + 3072;
    auto ldp = [&](int n) {
      const int u = n / 6, k = n % 6;
      pr[u][k] = ld128s(rs, k == 3 ? pv3 : lane * 16 + (k & 3) * 1024, k < 4 ? pb[u] : pbh[u]);
    };
#pragma nounroll
    for (int sp = 0; sp < SPIN_CAP; ++sp) {
      asm volatile("" ::: "memory");
      if (!__any(__builtin_amdgcn_raw_buffer_load_b32(rs, (lane & 7) * 128, WS_H4 + pi * 1024, SC1) != tag)) break;
      CNT(28, 10000);
      __builtin_amdgcn_s_sleep(2);
    }
    STAMP(18);
    FORU(n, 12) { ldp(n); SB(); }
    u32 bad = 0;
    MM_HEAD();
    sfor<0, 96>([&](auto z) {
      constexpr int u = z.v / 24, k = (z.v / 4) % 6, n = z.v % 4;
      if (k) m_vv(oa[u & 1][n], wo[k][n], pr[u][k]); else m_vv0(oa[u & 1][n], wo[k][n], pr[u][k]);
      if constexpr (n == 1 && z.v / 4 < 12) ldp(12 + z.v / 4);
      if constexpr (u > 0 && k == 2 && n == 0) bad |= __any(oa[(u & 1) ^ 1][0].x != oa[(u & 1) ^ 1][0].x) ? 1u << (u - 1) : 0u;
      if constexpr (u > 0 && k >= 2 && n == 2) *(LAS f4*)(L + rd[u - 1] + (k - 2) * 1024) = oa[(u & 1) ^ 1][k - 2];
      SB();
    });
    MM_TAIL();
    bad |= __any(oa[1][0].x != oa[1][0].x) ? 8u : 0u;
    if (bad) {
#pragma nounroll
      for (u32 u = 0; u < 4; ++u) {
        if (!((bad >> u) & 1)) continue;
        CNT(28, 1000000);
        pb[0] = u == 0 ? pb[0] : u == 1 ? pb[1] : u == 2 ? pb[2] : pb[3];
        pbh[0] = u == 0 ? pbh[0] : u == 1 ? pbh[1] : u == 2 ? pbh[2] : pbh[3];
#pragma nounroll
        for (int sp = 0; sp < SPIN_CAP; ++sp) {
          u32 m = 0;
          asm volatile("" ::: "memory");
          FORU(k, 6) ldp(k);
          FORU(k, 6) m = vmax(m, pr[0][k]);
          if (!__any(m == ~0u)) break;
        }
        MM_HEAD();
        FORU(k, 6) FORU(n, 4) { if (k) m_vv(oa[0][n], wo[k][n], pr[0][k]); else m_vv0(oa[0][n], wo[k][n], pr[0][k]); }
        MM_TAIL();
        FORU(n, 4) {
          if (u < 3) *(LAS f4*)(L + (u == 0 ? rd[0] : u == 1 ? rd[1] : rd[2]) + n * 1024) = oa[0][n];
          else oa[1][n] = oa[0][n];
        }
      }
    }
    __syncthreads();
    f4 o[4];
    FORU(n, 4) {
      lptr p = L + L_RED2 + (w * 12 + n) * 1024 + lane * 16;
      o[n] = (oa[1][n] + *(LAS f4*)p) + (*(LAS f4*)(p + 4096) + *(LAS f4*)(p + 8192));
    }
    STAMP(19);
    v2u O[4];
#pragma unroll
    for (int n = 0; n < 4; ++n) {
      const u32 g0 = (n & 1) ? dppu<0x158>(gbp[n >> 1]) : dppu<0x150>(gbp[n >> 1]), g1 = (n & 1) ? dppu<0x15C>(gbp[n >> 1]) : dppu<0x154>(gbp[n >> 1]);
      O[n] = v2u{pkbf(bf2f(xres[n].x) + bf2f(g0) * f2{o[n].x, o[n].y}), pkbf(bf2f(xres[n].y) + bf2f(g1) * f2{o[n].z, o[n].w})};
    }
#pragma unroll
    for (int h = 0; h < 2; ++h) {
      auto rx = __builtin_amdgcn_permlane16_swap(O[2 * h].x, O[2 * h + 1].x, false, false);
      auto ry = __builtin_amdgcn_permlane16_swap(O[2 * h].y, O[2 * h + 1].y, false, false);
      *(v4u*)(out + (64 * pi + 16 * w + c16) * 512 + 64 * pj + 16 * (2 * h + (q4 & 1)) + 8 * (q4 >> 1)) = v4u{rx[0], ry[0], rx[1], ry[1]};
    }
  }
  if (b == 0 && w == 0) __builtin_amdgcn_raw_buffer_store_b32(tag, rs, WS_E, 0, SC1);
  asm volatile("fk_text_b:");
  STAMP(20);
  STAMP_FLUSH();
}

extern "C" void kernel_launch(void* const* d_in, const int* in_sizes, int n_in, void* d_out, int out_size, void* d_ws, size_t ws_size,
                              hipStream_t stream) {
  (void)in_sizes; (void)n_in; (void)out_size; (void)ws_size;
  (void)hipFuncSetAttribute((const void*)fused_kernel, hipFuncAttributeMaxDynamicSharedMemorySize, LDS_BYTES);
  fused_kernel<<<256, 256, LDS_BYTES, stream>>>((const u16*)d_in[0], (const u16*)d_in[1], (const u16*)d_in[2], (const u16*)d_in[3],
                                                (const u16*)d_in[4], (const u16*)d_in[5], (const u16*)d_in[6], (const u16*)d_in[8],
                                                (const u16*)d_in[9], (const u16*)d_in[10], (const u16*)d_in[11], (const u16*)d_in[12],
                                                (const u16*)d_in[13], (u16*)d_out, (unsigned char*)d_ws);
}
```

```cpp
#include <hip/hip_runtime.h>
#include <stdint.h>

typedef unsigned u32;
typedef unsigned short u16;
typedef unsigned long long u64;
typedef u32 v4u __attribute__((ext_vector_type(4)));
typedef u32 v2u __attribute__((ext_vector_type(2)));
typedef float f4 __attribute__((ext_vector_type(4)));
typedef float f2 __attribute__((ext_vector_type(2)));
typedef __bf16 bf8 __attribute__((ext_vector_type(8)));
typedef __bf16 bf2 __attribute__((ext_vector_type(2)));
typedef _Float16 h8 __attribute__((ext_vector_type(8)));
typedef _Float16 h2 __attribute__((ext_vector_type(2)));
typedef short s4 __attribute__((ext_vector_type(4)));
#define LAS __attribute__((address_space(3)))
#define GAS __attribute__((address_space(1)))
#define CAS __attribute__((address_space(4)))
#define DI __device__ __forceinline__
typedef __amdgpu_buffer_rsrc_t rsrc_t;
typedef LAS unsigned char* lptr;

#define XMAP 2

constexpr int WS_E = 0, WS_STAT1 = 4096, WS_CD = 32768, WS_ST2 = 65536, WS_F3 = 327680, WS_H4 = 360448, WS_MOD = 524288, WS_BWP = 655360;
constexpr int WS_XG = 1 << 20, WS_X1 = 5 << 20, WS_Z2N = 7 << 20, WS_PB = 10 << 20, WS_WINP = 13 << 20, WS_WOP = 29 << 19;
constexpr int WS_DBG = 24 << 20, WS_END = 25 << 20;
constexpr int OOB = 0x40000000;
constexpr int TABP = 544, TAB_CH = 63 * TABP;
constexpr int L_TAB = 0, L_Y = 0, Y_CH = 21504;
constexpr int L_PKW = 75776, L_SHF = 81920;
constexpr int L_PKI = 68544;
constexpr int L_TOE = 108544, TOE_C = 5312, TOE_CH = 4 * TOE_C;
constexpr int L_U = 151040, U_CH = 6320;
constexpr int L_XIMG = 0, L_TRS = 65536, TRS_W = 4096;
constexpr int L_RED = 0, L_RED2 = 81920;
constexpr int L_XST = 159744, L_ZST = L_XST + 1024;
constexpr int LDS_BYTES = 163840;
constexpr int SC1 = 16;
constexpr int SPIN_CAP = 1 << 15;
static_assert(LDS_BYTES <= 163840, "LDS");

DI float bflo(u32 v) { return __uint_as_float(v << 16); }
DI float bfhi(u32 v) { return __uint_as_float(v & 0xffff0000u); }
DI f2 bf2f(u32 v) { return f2{bflo(v), bfhi(v)}; }
DI u32 pkbf(float a, float b) { f2 v = {a, b}; return __builtin_bit_cast(u32, __builtin_convertvector(v, bf2)); }
DI u32 pkbf(f2 v) { return __builtin_bit_cast(u32, __builtin_convertvector(v, bf2)); }
DI u32 pkh(f2 v) { return __builtin_bit_cast(u32, __builtin_convertvector(v, h2)); }
DI float rbf(float a) { return bflo(pkbf(a, a)); }
DI u32 san(u32 v) { return v < 0xFFFFFFFEu ? v : 0xFFFFFFFEu; }
DI v4u san4(v4u v) { return v4u{san(v.x), san(v.y), san(v.z), san(v.w)}; }
DI u32 umax3(u32 a, u32 b, u32 c) { return max(max(a, b), c); }
DI u32 umin3(u32 a, u32 b, u32 c) { return min(min(a, b), c); }
DI u32 vmax(u32 m, v4u v) { return umax3(umax3(m, v.x, v.y), v.z, v.w); }
DI v4u ld128(rsrc_t rs, int off) { return __builtin_amdgcn_raw_buffer_load_b128(rs, off, 0, SC1); }
DI v4u ld128s(rsrc_t rs, int off, int soff) { return __builtin_amdgcn_raw_buffer_load_b128(rs, off, soff, SC1); }
DI void st128(rsrc_t rs, int off, v4u v) { __builtin_amdgcn_raw_buffer_store_b128(v, rs, off, 0, SC1); }
DI void st128s(rsrc_t rs, int off, int soff, v4u v) { __builtin_amdgcn_raw_buffer_store_b128(v, rs, off, soff, SC1); }
DI void st64(rsrc_t rs, int off, v2u v) { __builtin_amdgcn_raw_buffer_store_b64(v, rs, off, 0, SC1); }
#define RARE(c) __builtin_expect(!!(c), 0)
#define DRAIN() asm volatile("s_waitcnt vmcnt(0)" ::: "memory")
#define SB() __builtin_amdgcn_sched_barrier(0)
#define CFENCE() do { asm volatile("" ::: "memory"); SB(); } while (0)
template <int C> DI u32 dppu(u32 v) { return (u32)__builtin_amdgcn_update_dpp(0, (int)v, C, 0xF, 0xF, true); }
template <int C> DI float dppadd(float v) { return v + __uint_as_float(dppu<C>(__float_as_uint(v))); }
DI float sw16sum(float a, float b) { auto r = __builtin_amdgcn_permlane16_swap(__float_as_uint(a), __float_as_uint(b), false, false); return __uint_as_float(r[0]) + __uint_as_float(r[1]); }
DI float sw32sum(float a, float b) { auto r = __builtin_amdgcn_permlane32_swap(__float_as_uint(a), __float_as_uint(b), false, false); return __uint_as_float(r[0]) + __uint_as_float(r[1]); }
DI float quadsum(float v) { v = dppadd<0xB1>(v); return dppadd<0x4E>(v); }
DI float rowsum(float v) { v = quadsum(v); v = dppadd<0x141>(v); return dppadd<0x140>(v); }
DI f4 mfma_bf(v4u a, v4u b, f4 c) { return __builtin_amdgcn_mfma_f32_16x16x32_bf16(__builtin_bit_cast(bf8, a), __builtin_bit_cast(bf8, b), c, 0, 0, 0); }
DI f4 mfma_h(v4u a, v4u b, f4 c) { return __builtin_amdgcn_mfma_f32_16x16x32_f16(__builtin_bit_cast(h8, a), __builtin_bit_cast(h8, b), c, 0, 0, 0); }
DI v2u trread(lptr p) { return __builtin_bit_cast(v2u, __builtin_amdgcn_ds_read_tr16_b64_v4i16((LAS s4*)p)); }
template <int N> struct IC { static constexpr int v = N; };
template <int I, int N, class F> DI void sfor(F f) { if constexpr (I < N) { f(IC<I>{}); sfor<I + 1, N>(f); } }
#define FORU(i, n) _Pragma("unroll") for (int i = 0; i < (n); ++i)
DI f2 cm1(f2 a, f2 b) { f2 t; asm("v_pk_mul_f32 %0, %1, %2 op_sel:[0,0] op_sel_hi:[0,1]" : "=v"(t) : "v"(a), "v"(b)); return t; }
DI f2 cm2(f2 a, f2 b, f2 t) { f2 r; asm("v_pk_fma_f32 %0, %1, %2, %3 op_sel:[1,1,0] op_sel_hi:[1,0,1] neg_lo:[0,1,0]" : "=v"(r) : "v"(a), "v"(b), "v"(t)); return r; }
DI f2 cexp_p(float al2, float rev, float p) {
  float m = __builtin_amdgcn_exp2f(al2 * p), f = __builtin_amdgcn_fractf(rev * p);
  return f2{m * __builtin_amdgcn_cosf(f), m * __builtin_amdgcn_sinf(f)};
}
template <int N, class F> DI void gelu_n(f2 (&v)[N], F fill) {
  const float c0 = -2.0f * 0.7978845608028654f * 1.4426950408889634f, c1 = c0 * 0.044715f;
  f2 a[N];
  sfor<0, 2>([&](auto z) { FORU(i, 6) a[6 * z.v + i] = v[6 * z.v + i] * v[6 * z.v + i]; fill(IC<4 + z.v>{}); });
  sfor<0, 2>([&](auto z) { FORU(i, 6) a[6 * z.v + i] = __builtin_elementwise_fma(a[6 * z.v + i], f2{c1, c1}, f2{c0, c0}); fill(IC<6 + z.v>{}); });
  sfor<0, 2>([&](auto z) { FORU(i, 6) a[6 * z.v + i] *= v[6 * z.v + i]; fill(IC<8 + z.v>{}); });
  sfor<0, 6>([&](auto z) { FORU(i, 2) a[2 * z.v + i] = f2{__builtin_amdgcn_exp2f(a[2 * z.v + i].x), __builtin_amdgcn_exp2f(a[2 * z.v + i].y)}; fill(IC<10 + z.v>{}); });
  sfor<0, 2>([&](auto z) { FORU(i, 6) a[6 * z.v + i] += 1.0f; fill(IC<16 + z.v>{}); });
  sfor<0, 6>([&](auto z) { FORU(i, 2) a[2 * z.v + i] = f2{__builtin_amdgcn_rcpf(a[2 * z.v + i].x), __builtin_amdgcn_rcpf(a[2 * z.v + i].y)}; fill(IC<18 + z.v>{}); });
  sfor<0, 2>([&](auto z) { FORU(i, 6) v[6 * z.v + i] *= a[6 * z.v + i]; fill(IC<24 + z.v>{}); });
}
DI float ln_den(float var) { return rbf(__builtin_amdgcn_sqrtf(rbf(rbf(var) + 1e-5f))); }
template <class T> DI T gld(const void* base, u32 off) { return *(const GAS T*)((const GAS char*)base + off); }
#define MF "v_mfma_f32_16x16x32_bf16 "
DI void m_av(f4& c, v4u a, v4u b) { asm volatile(MF "%0, %1, %2, %0" : "+v"(c) : "a"(a), "v"(b)); }
DI void m_va(f4& c, v4u a, v4u b) { asm volatile(MF "%0, %1, %2, %0" : "+v"(c) : "v"(a), "a"(b)); }
DI void m_vv(f4& c, v4u a, v4u b) { asm volatile(MF "%0, %1, %2, %0" : "+v"(c) : "v"(a), "v"(b)); }
DI void m_av0(f4& c, v4u a, v4u b) { asm volatile(MF "%0, %1, %2, 0" : "=&v"(c) : "a"(a), "v"(b)); }
DI void m_va0(f4& c, v4u a, v4u b) { asm volatile(MF "%0, %1, %2, 0" : "=&v"(c) : "v"(a), "a"(b)); }
DI void m_vv0(f4& c, v4u a, v4u b) { asm volatile(MF "%0, %1, %2, 0" : "=&v"(c) : "v"(a), "v"(b)); }
DI v4u aget(v4u a) {
  v4u r;
  _Pragma("unroll") for (int e = 0; e < 4; ++e) { u32 t; asm volatile("v_accvgpr_read_b32 %0, %1" : "=v"(t) : "a"(a[e])); r[e] = t; }
  return r;
}
DI u32 ssel(bool c, u32 a, u32 b) { asm("" : "+s"(a)); asm("" : "+s"(b)); return c ? a : b; }
DI u32 smod(u32 v, u32 m) { u32 t = v - m; asm("" : "+s"(t)); return (int)t >= 0 ? t : v; }
DI u32 fresh_lane() { u32 l; asm volatile("v_mbcnt_lo_u32_b32 %0, -1, 0\n\tv_mbcnt_hi_u32_b32 %0, -1, %0" : "=v"(l)); return l; }
template <class T> DI void pin(T& v) { asm volatile("" : "+v"(v)); }
#define MM_HEAD() do { SB(); asm volatile("s_nop 7"); SB(); } while (0)
#define MM_TAIL() do { SB(); asm volatile("s_nop 7"); SB(); } while (0)
DI int toe_addr(int pos) { return 2 * pos + 8 * (pos >> 4); }

#define STAMP(n)
#define STAMP_FLUSH()
#define STAMP_INIT()
#define CNT(n,v)

template <int BAND> struct Band {
  static constexpr int R = 128 - 32 * BAND, NT = R / 16, NS = 15 + NT / 2;
  static constexpr int YO = BAND == 0 ? 0 : BAND == 1 ? 8448 : BAND == 2 ? 14848 : 19200;
  lptr al, ah, bb, yp;
  f4 acc[NT];
  v2u lo[32], hi[32];
  v4u bf[NS];
  DI void init(lptr L, int ch, int lane) {
    const int r = lane & 15, q = lane >> 4, c = (3 - r) & 3;
    const int P = 2048 - 512 * BAND - 496 - 4 * (r >> 2) + 8 * q;
    al = L + L_TOE + ch * TOE_CH + c * TOE_C + toe_addr(P);
    ah = L + L_TOE + ch * TOE_CH + c * TOE_C + toe_addr(P + 4);
    bb = L + L_U + ch * U_CH + 80 * (R / 32) * r + 16 * q;
    yp = L + L_Y + ch * Y_CH + YO + r * (4 * R + 16) + 16 * q;
  }
  DI void load(int s, int i) {
    if (i < 4) {
      const int n = 2 * s + (i >> 1);
      if (n < 32) { if (i & 1) hi[n] = *(LAS v2u*)(ah + 40 * (31 - n)); else lo[n] = *(LAS v2u*)(al + 40 * (31 - n)); }
    } else if (s < NS) bf[s] = *(LAS v4u*)(bb + 80 * (NT / 2 - 1 - s + 15));
  }
  DI void mma(int s, int t) {
    const int n = t - NT + 2 + 2 * s;
    if (s < NS && n >= 0 && n < 32) {
      if (n < 2) m_vv0(acc[t], v4u{lo[n].x, lo[n].y, hi[n].x, hi[n].y}, bf[s]); else m_vv(acc[t], v4u{lo[n].x, lo[n].y, hi[n].x, hi[n].y}, bf[s]);
    }
  }
  DI void flush(int s, int t) {
    const int n = t - NT + 2 + 2 * (s - 1);
    if (n == 30 || n == 31) *(LAS f4*)(yp + 64 * t) = acc[t];
  }
};
template <int B1, int B2, class F> DI void conv2(lptr L, int ch, int lane, F side) {
  Band<B1> u; Band<B2> v;
  u.init(L, ch, lane); v.init(L, ch, lane);
  constexpr int N1 = Band<B1>::NT, NS = Band<B1>::NS;
#pragma unroll
  for (int s = 0; s < 2; ++s)
#pragma unroll
    for (int i = 0; i < 5; ++i) { u.load(s, i); v.load(s, i); SB(); }
  sfor<0, 10 * (NS + 1)>([&](auto z) {
    constexpr int s = z.v / 10, i = z.v % 10;
    if (s >= 17 && i == 0) asm volatile("s_nop %0" ::"i"(s == NS ? 7 : s == NS - 1 ? 5 : 2));
    if (i < N1) { u.mma(s, i); u.flush(s, i); } else { v.mma(s, i - N1); v.flush(s, i - N1); }
    if (i < 5) u.load(s + 2, i); else v.load(s + 2, i - 5);
    side(IC<s>{}, IC<i>{});
    SB();
  });
}

__global__ void __launch_bounds__(256, 1)
fused_kernel(const u16* __restrict__ x, const u16* __restrict__ lar, const u16* __restrict__ aim, const u16* __restrict__ cre,
             const u16* __restrict__ cim, const u16* __restrict__ ldt, const u16* __restrict__ dsk, const u16* __restrict__ win,
             const u16* __restrict__ wsp, const u16* __restrict__ bsp, const u16* __restrict__ wout, const u16* __restrict__ cvec,
             const u16* __restrict__ wmod, u16* __restrict__ out, unsigned char* ws) {
  extern __shared__ __attribute__((aligned(16))) unsigned char smem[];
  lptr L = (lptr)smem;
  const u32 b = blockIdx.x, tid = threadIdx.x, lane = tid & 63;
  STAMP_INIT();
  STAMP(0);
  const u32 w = __builtin_amdgcn_readfirstlane(tid >> 6);
  const u32 xc = b & 7, rr = b >> 3, cp = xc * 32 + rr;
  const u32 c16 = lane & 15, q4 = lane >> 4;
  u32 pf0 = 0, pf1 = 0;
  {
    u64 pc;
    u32 len;
    asm volatile("s_getpc_b64 %0\nfk_text_a:\n\ts_mov_b32 %1, fk_text_b-fk_text_a" : "=s"(pc), "=s"(len));
    const u32 o = (rr * 16 + tid) * 128;
    if (tid < 16 && o < len) pf0 = gld<u32>((const void*)pc, o);
  }
  CFENCE();
  const u32 tag = *(const CAS u32*)(ws + WS_E) + 1u;
  const u32 tb = 512 * (b >> 6) + 32 * (b & 15) + 2 * ((b >> 4) & 3);
  const v4u sr0 = gld<v4u>(x, (tb + 8 * w) * 1024 + lane * 16), sr1 = gld<v4u>(x, (tb + 8 * w) * 1024 + 1024 + lane * 16);
  const u32 xgo = (tb + 8 * (lane & 3)) * 1024 + (16 * w + (lane >> 2)) * 16;
  const v4u xg0 = gld<v4u>(x, xgo), xg1 = gld<v4u>(x, xgo + 1024);
  const u32 mG = b % 48, mP = min(b / 48, 3u), mrow = mP * 128 + (lane >> 1);
  v4u wm[4];
  u32 cv[4];
#pragma unroll
  for (int n = 0; n < 4; ++n) wm[n] = gld<v4u>(wmod, (mrow + 32 * n) * 6144 + mG * 128 + w * 32 + (lane & 1) * 16);
#pragma unroll
  for (int n = 0; n < 4; ++n) cv[n] = gld<u16>(cvec, (mrow + 32 * n) * 2);
  const u32 pidx = (cp * 256 + tid) * 2;
  const u32 p_lar = gld<u16>(lar, pidx), p_aim = gld<u16>(aim, pidx), p_cr = gld<u16>(cre, pidx), p_ci = gld<u16>(cim, pidx);
  const u32 p_cr2 = gld<u16>(cre, pidx ^ 128), p_ci2 = gld<u16>(cim, pidx ^ 128);
  const u32 p_ldt = *(const CAS u32*)(ldt + 2 * cp), p_dsk = *(const CAS u32*)(dsk + 2 * cp);
  CFENCE();
  const u32 pi = (xc >> 1) * 8 + (rr & 7), pj = (xc & 1) * 4 + (rr >> 3);
  const u32 nb = pj < 3 ? 6 : 5, blk0 = 5 * pj + min(pj, 3u);
  const bool two = w + 4 < nb;
  const rsrc_t rs = __builtin_amdgcn_make_buffer_rsrc((void*)ws, 0, WS_END, 0x00020000);
  const v4u SENT = {~0u, ~0u, ~0u, ~0u};
  const int x1w = WS_X1 + ((tid >> 3) * 512 + 2 * cp) * 128 + (tid & 7) * 16;
  const u32 wio_src = ((32 * (pi >> 1) + (lane >> 1)) * 1376 + (pi & 1) * 688 + 16 * (blk0 + w)) * 2 + (lane & 1) * 16;
  const v4u wi0 = gld<v4u>(win, wio_src), wi1 = gld<v4u>(win, two ? wio_src + 128 : wio_src);
  const rsrc_t ro = __builtin_amdgcn_make_buffer_rsrc((void*)wout, 0, 688 * 1024, 0x00020000);
  const v4u wo0 = __builtin_amdgcn_raw_buffer_load_b128(ro, (lane >> 1) * 1024 + (lane & 1) * 16, b < 176 ? (b % 22) * 32768 + (b / 22) * 128 + w * 32 : OOB, 0);
  CFENCE();
  if (tid < 40) *(LAS v4u*)(L + L_TOE + (tid / 5) * TOE_C + 5120 + (tid % 5) * 16) = v4u{0, 0, 0, 0};
  if (tid < 150) *(LAS v4u*)(L + L_U + (tid / 75) * U_CH + (tid % 75) * 16) = v4u{0, 0, 0, 0};
  STAMP(23);
  {
    f2 a[8];
#pragma unroll
    for (int e = 0; e < 4; ++e) { a[2 * e] = f2{bflo(sr0[e]), bflo(sr1[e])}; a[2 * e + 1] = f2{bfhi(sr0[e]), bfhi(sr1[e])}; }
    f2 s = ((a[0] + a[1]) + (a[2] + a[3])) + ((a[4] + a[5]) + (a[6] + a[7]));
    float t = rowsum(sw32sum(s.x, s.y));
    t = sw16sum(t, t) * (1.f / 512);
    auto mm = __builtin_amdgcn_permlane32_swap(__float_as_uint(t), __float_as_uint(t), false, false);
    const f2 m = {__uint_as_float(mm[0]), __uint_as_float(mm[1])};
    s = f2{0.f, 0.f};
#pragma unroll
    for (int e = 0; e < 8; ++e) { const f2 u = a[e] - m; s = __builtin_elementwise_fma(u, u, s); }
    float v = rowsum(sw32sum(s.x, s.y));
    v = sw16sum(v, v) * (1.f / 512);
    st64(rs, (lane & 31) == 0 ? WS_STAT1 + b * 64 + w * 16 + (lane >> 5) * 8 : OOB, v2u{pkbf(t, ln_den(v)), tag});
  }
  {
    const int o = WS_XG + (4 * (16 * w + (lane >> 2))) * 16384 + b * 64 + (lane & 3) * 16;
    st128(rs, o, v4u{xg0.x, tag, xg1.x, tag});
    st128s(rs, o, 16384, v4u{xg0.y, tag, xg1.y, tag});
    st128s(rs, o, 32768, v4u{xg0.z, tag, xg1.z, tag});
    st128s(rs, o, 49152, v4u{xg0.w, tag, xg1.w, tag});
  }
  CFENCE();
  STAMP(22);
  {
    f2 a[4];
#pragma unroll
    for (int n = 0; n < 4; ++n) {
      const float c = bflo(cv[n]);
      const float s = rbf(c * __builtin_amdgcn_rcpf(1.f + __builtin_amdgcn_exp2f(-1.4426950408889634f * c)));
#pragma unroll
      for (int e = 0; e < 4; ++e) a[e] = n ? a[e] + bf2f(wm[n][e]) * s : bf2f(wm[n][e]) * s;
    }
    const float u0 = sw32sum(a[0].x, a[2].x), u1 = sw32sum(a[0].y, a[2].y), u2 = sw32sum(a[1].x, a[3].x), u3 = sw32sum(a[1].y, a[3].y);
    float t0 = sw16sum(u0, u2), t1 = sw16sum(u1, u3);
    t0 = dppadd<0x4E>(dppadd<0x124>(dppadd<0x128>(t0)));
    t1 = dppadd<0x4E>(dppadd<0x124>(dppadd<0x128>(t1)));
    st128(rs, (c16 < 2 && b < 192) ? WS_MOD + (mP * 1536 + mG * 32 + w * 8 + c16 * 4 + q4) * 16 : OOB, v4u{__float_as_uint(t0), tag, __float_as_uint(t1), tag});
  }
  CFENCE();
  STAMP(1);
  const u32 xw = 2 * (w & 1) + (w >> 1);
  const int wio = WS_WINP + (((pj * 2 + (pi & 1)) * 6 + w) * 16 + (pi >> 1)) * 1024 + lane * 16;
  const u32 pfo = (64 * pi + 8 * pj + 2 * w + ((lane >> 4) & 1)) * 4096 + ((lane >> 2) & 3) * 1024 + (lane & 3) * 128;
  u32 pf2 = 0, pf3;
  if (lane < 32) pf2 = gld<u32>(wsp, pfo);
  st128(rs, x1w, SENT);
  st128(rs, x1w + 128, SENT);
  CFENCE();
  const rsrc_t rw = __builtin_amdgcn_make_buffer_rsrc((void*)wsp, 0, 2048 * 4096, 0x00020000);
  const u32 wvo = (lane >> 3) * 4096 + (lane & 7) * 16;
  const u32 wsb[4] = {(64 * pi + 16 * xw) * 4096 + w * 1024, (64 * pi + 16 * (xw ^ 1)) * 4096 + w * 1024,
                      (64 * pi + 16 * (xw ^ 2)) * 4096 + w * 1024, (64 * pi + 16 * (xw ^ 3)) * 4096 + w * 1024};
  v4u wraw[64], wsf[16][4];
  auto rq = [&](int g) { return (g + pj) & 3; };
  auto rk = [&](int g) { return g ^ (pj & 1); };
  auto ldw = [&](int n) { wraw[n] = __builtin_amdgcn_raw_buffer_load_b128(rw, (wvo + rk(n >> 4) * 256) + ((n >> 3) & 1) * 128, wsb[(n & 7) >> 1] + (n & 1) * 32768, 0); };
  const u32 wwa = (u32)(size_t)L + L_SHF + w * 8192 + (lane >> 3) * 128 + ((lane & 7) ^ (lane >> 3)) * 16;
  auto shuf = [&](auto Z) {
    constexpr int c = Z.v >> 4, j = Z.v & 15, i = j & 7;
    if constexpr (j < 8) asm volatile("ds_write_b128 %0, %1 offset:%2" ::"v"(wwa), "a"(wraw[8 * c + i]), "i"(i * 1024) : "memory");
    else wsf[2 * c + (i & 1)][i >> 1] = *(LAS v4u*)(L + L_SHF + w * 8192 + (i >> 1) * 2048 + c16 * 128 + (((4 * (i & 1) + q4) ^ (c16 & 7)) * 16));
  };
  float kscale;
  {
    const float L2E = 1.4426950408889634f;
    const float dt = rbf(__builtin_amdgcn_exp2f(((w >> 1) ? bfhi(p_ldt) : bflo(p_ldt)) * L2E));
    const float al2 = (-rbf(__builtin_amdgcn_exp2f(bflo(p_lar) * L2E)) * dt) * L2E;
    const float rev = (bflo(p_aim) * dt) * 0.15915494309189535f;
    u32 mx = max(max(p_cr & 0x7fffu, p_ci & 0x7fffu), max(p_cr2 & 0x7fffu, p_ci2 & 0x7fffu));
    mx = max(mx, dppu<0xB1>(mx)); mx = max(mx, dppu<0x4E>(mx)); mx = max(mx, dppu<0x141>(mx)); mx = max(mx, dppu<0x140>(mx));
    { auto r = __builtin_amdgcn_permlane16_swap(mx, mx, false, false); mx = max(r[0], r[1]); }
    { auto r = __builtin_amdgcn_permlane32_swap(mx, mx, false, false); mx = max(r[0], r[1]); }
    u32 ex = mx >> 7;
    ex = min(max(ex, 1u), 253u);
    const float dn = __uint_as_float((254u - ex) << 23), up = __uint_as_float(ex << 23);
    kscale = 2.0f * dt * up;
    LAS u32* tp = (LAS u32*)(L + L_TAB + (tid >> 7) * TAB_CH + (tid & 127) * 4);
    f2 U[17], Y[16], V[8], X[31], wp[4], t[31];
    u32 pk[31];
    wp[0] = cexp_p(al2, rev, 1.f);
    Y[1] = cexp_p(al2, -rev, 16.f);
    V[1] = cexp_p(al2, rev, 256.f);
    Y[0] = f2{1.f, 0.f};
    U[0] = f2{bflo(p_cr) * dn, bflo(p_ci) * dn};
    SB();
    FORU(l, 4) {
      const int h = 1 << l;
      FORU(i, h) t[i] = cm1(U[i], wp[l]);
      if (l < 3) { t[8] = cm1(wp[l], wp[l]); t[9] = cm1(Y[h], Y[h]); }
      if (l < 2) t[10] = cm1(V[h], V[h]);
      FORU(i, h - 1) t[11 + i] = cm1(Y[1 + i], Y[h]);
      if (l < 3) { FORU(i, h - 1) t[18 + i] = cm1(V[1 + i], V[h]); }
      SB();
      FORU(i, h) U[h + i] = cm2(U[i], wp[l], t[i]);
      if (l < 3) { wp[l + 1] = cm2(wp[l], wp[l], t[8]); Y[2 * h] = cm2(Y[h], Y[h], t[9]); }
      if (l < 2) V[2 * h] = cm2(V[h], V[h], t[10]);
      FORU(i, h - 1) Y[h + 1 + i] = cm2(Y[1 + i], Y[h], t[11 + i]);
      if (l < 3) { FORU(i, h - 1) V[h + 1 + i] = cm2(V[1 + i], V[h], t[18 + i]); }
      SB();
    }
    auto xa = [](int i) { return i < 9 ? 1 : i < 19 ? 2 + (i - 9) / 5 : 4 + (i - 19) / 3; };
    auto xo = [](int i) { return i < 9 ? 2 * i : i < 19 ? 4 * ((i - 9) % 5) : 8 * ((i - 19) % 3); };
    u32 py[16], pu[16];
    t[0] = cm1(U[8], wp[3]);
    FORU(i, 16) { py[i] = pkh(Y[i]); if (i == 2) U[16] = cm2(U[8], wp[3], t[0]); if (i & 1) tp[(47 + (i >> 1)) * (TABP / 4)] = py[i >> 1]; SB(); }
    FORU(i, 16) { pu[i] = pkh(U[i]); if (i & 1) tp[(55 + (i >> 1)) * (TABP / 4)] = py[8 + (i >> 1)]; SB(); }
    FORU(i, 31) { t[i] = cm1(U[xo(i)], V[xa(i)]); if (!(i & 1)) tp[(i >> 1) * (TABP / 4)] = pu[i >> 1]; SB(); }
    FORU(i, 35) {
      if (i < 31) X[i] = cm2(U[xo(i)], V[xa(i)], t[i]);
      if (i >= 2 && i < 33) pk[i - 2] = pkh(X[i - 2]);
      if (i >= 4) tp[(12 + i) * (TABP / 4)] = pk[i - 4];
      SB();
    }
  }
  *(LAS v4u*)(L + L_PKI + w * 1024 + lane * 16) = wi0;
  if (w < 2) *(LAS v4u*)(L + L_PKI + 4096 + w * 1024 + lane * 16) = wi1;
  *(LAS v4u*)(L + L_PKW + w * 8192 + lane * 16) = wo0;
  v4u gx[4], gs[4], gm[3];
  const int mo = WS_MOD + ((lane & 3) * 1536 + 16 * (pi >> 1) + (lane >> 2)) * 16;
  const int mo2 = WS_MOD + ((lane & 3) * 1536 + 256 * min(lane >> 2, 2u) + cp) * 16;
  auto s1_ld = [&](int n) {
    if (n < 4) gx[n] = ld128s(rs, lane * 16 + n * 1024, WS_XG + cp * 16384 + w * 4096);
    else if (n < 8) gs[n - 4] = ld128s(rs, lane * 16 + (n - 4) * 1024, WS_STAT1 + w * 4096);
    else gm[n - 8] = n == 8 ? ld128(rs, mo2) : ld128s(rs, mo, (n == 9 ? 768 : 1024) * 16);
  };
  FORU(n, 3) s1_ld(n);
  __syncthreads();
  STAMP(2);
  f4 kacc[4];
  v2u pl[3], ph[3];
  {
    const u32 hi = w & 1;
    lptr tb_ = L + L_TAB + (w >> 1) * TAB_CH + 16 * q4;
    const u32 i2 = (c16 + ((c16 >> 1) & 1)) >> 1, i4 = (c16 + 1 + ((c16 >> 2) & 1)) >> 2, i8 = (c16 + 3 + ((c16 >> 3) & 1)) >> 3;
    const u32 row[4] = {hi ? 35 + i8 : c16, hi ? 38 + i8 : 16 + i2, hi ? 41 + i8 : 25 + i4, hi ? 44 + i8 : 30 + i4};
    v4u yf[8], xf[8][4];
    auto ldk = [&](int k, int i) {
      if (k < 8) { if (i < 4) xf[k][i] = *(LAS v4u*)(tb_ + row[i] * TABP + 64 * k); else yf[k] = *(LAS v4u*)(tb_ + (47 + c16) * TABP + 64 * k); }
    };
    FORU(k, 2) FORU(i, 5) ldk(k, 4 - i);
    SB();
    const u32 po = (8 * q4 + (c16 >> 2)) * 32 + 8 * (c16 & 3);
    FORU(k, 8) FORU(a, 4) {
      if (k) asm volatile("v_mfma_f32_16x16x32_f16 %0, %1, %2, %0" : "+v"(kacc[3 - a]) : "v"(xf[k][3 - a]), "v"(yf[k]));
      else asm volatile("v_mfma_f32_16x16x32_f16 %0, %1, %2, 0" : "=&v"(kacc[3 - a]) : "v"(xf[k][3 - a]), "v"(yf[k]));
      if (a == 0) ldk(k + 2, 4);
      ldk(k + 2, 3 - a);
      if (k < 4 && (a & 1)) s1_ld(3 + 2 * k + (a >> 1));
      if (k >= 6 && a < 3) {
        lptr p = L + (a == 0 ? L_PKI + w * 1024 : a == 1 ? L_PKI + 4096 + (w & 1) * 1024 : L_PKW + w * 8192) + po;
        if (k == 6) pl[a] = trread(p); else ph[a] = trread(p + 128);
      }
      SB();
    }
  }
  MM_TAIL();
  f2 pv[8];
  {
    const int i0 = 2044 - (1024 * (w & 1) + 768 + 16 * c16 + 4 * q4);
    lptr t0 = L + L_TOE + (w >> 1) * TOE_CH + toe_addr(i0), t1 = L + L_TOE + (w >> 1) * TOE_CH + toe_addr(i0 + 4);
    u32 P0[4], P1[4], M[4];
    FORU(a, 4) kacc[a] *= kscale;
    SB();
    FORU(a, 4) { P0[a] = pkbf(kacc[a].w, kacc[a].z); P1[a] = pkbf(kacc[a].y, kacc[a].x); M[a] = pkbf(kacc[a].z, kacc[a].y); }
    SB();
    FORU(a, 4) {
      lptr p0 = t0 + 640 * (3 - a), p1 = t1 + 640 * (3 - a);
      *(LAS v2u*)(p1) = v2u{P0[a], P1[a]};
      *(LAS u16*)(p0 + TOE_C + 6) = (u16)P0[a]; *(LAS u32*)(p1 + TOE_C) = M[a]; *(LAS u16*)(p1 + TOE_C + 4) = (u16)(P1[a] >> 16);
      *(LAS u32*)(p0 + 2 * TOE_C + 4) = P0[a]; *(LAS u32*)(p1 + 2 * TOE_C) = P1[a];
      *(LAS u16*)(p0 + 3 * TOE_C + 2) = (u16)P0[a]; *(LAS u32*)(p0 + 3 * TOE_C + 4) = M[a]; *(LAS u16*)(p1 + 3 * TOE_C) = (u16)(P1[a] >> 16);
      FORU(j, 2) { pv[2 * a + j] = bf2f((a & 1) ? ph[a >> 1][j] : pl[a >> 1][j]); pin(pv[2 * a + j]); }
      SB();
    }
    st128s(rs, lane * 16, b < 176 ? WS_WOP + (b * 4 + w) * 1024 : OOB, v4u{pl[2].x, pl[2].y, ph[2].x, ph[2].y});
  }
  asm volatile("" ::"v"(pf0), "v"(pf2));
  STAMP(3);
  auto s1_bad = [&]() {
    u32 mx = umax3(gm[0].y, gm[0].w, tag), mn = umin3(gm[0].y, gm[0].w, tag);
    FORU(k, 2) { mx = umax3(mx, gm[1 + k].y, gm[1 + k].w); mn = umin3(mn, gm[1 + k].y, gm[1 + k].w); }
    FORU(k, 4) {
      mx = umax3(umax3(mx, gx[k].y, gx[k].w), gs[k].y, gs[k].w);
      mn = umin3(umin3(mn, gx[k].y, gx[k].w), gs[k].y, gs[k].w);
    }
    return __any(mx != mn);
  };
  if (RARE(s1_bad())) {
#pragma nounroll
    for (int sp = 0; sp < SPIN_CAP; ++sp) {
      asm volatile("" ::: "memory");
      FORU(n, 11) s1_ld(n);
      CNT(29, 1);
      if (!s1_bad()) break;
    }
  }
  STAMP(4);
  u32 xv[8];
  u32 h0[4], h1[4];
  u32 gav;
  {
    float qs[6];
    f2 m[4], r[4], a[4], c[4];
    int wn = 0;
    auto fill = [&]() { SB(); ldw(wn); ++wn; SB(); };
    FORU(i, 6) qs[i] = dppadd<0xB1>(__uint_as_float((i & 1) ? gm[i >> 1].z : gm[i >> 1].x));
    FORU(k, 4) { xv[2 * k] = gx[k].x; xv[2 * k + 1] = gx[k].z; r[k] = f2{bfhi(gs[k].x), bfhi(gs[k].z)}; }
    fill();
    FORU(i, 6) qs[i] = dppadd<0x4E>(qs[i]);
    FORU(k, 4) m[k] = f2{bflo(gs[k].x), bflo(gs[k].z)};
    fill();
    const u32 mv = pkbf(qs[0], qs[1]), sbp = pkbf(qs[2], qs[3]), cbp = pkbf(qs[4], qs[5]);
    FORU(k, 4) r[k] = f2{__builtin_amdgcn_rcpf(r[k].x), __builtin_amdgcn_rcpf(r[k].y)};
    fill();
    const u32 sav = __builtin_amdgcn_readlane(mv, 0), cav = __builtin_amdgcn_readlane(mv, 4);
    gav = __builtin_amdgcn_readlane(mv, 8);
    const u32 scp = pkbf(bf2f(cbp) + 1.0f);
    FORU(k, 4) { a[k] = f2{bflo(gx[k].x), bflo(gx[k].z)}; c[k] = f2{bfhi(gx[k].x), bfhi(gx[k].z)}; }
    fill();
    const u32 s1v = pkbf(bf2f(cav) + 1.0f);
    FORU(k, 4) { a[k] -= m[k]; c[k] -= m[k]; }
    const u32 scb[4] = {dppu<0x150>(scp), dppu<0x154>(scp), dppu<0x158>(scp), dppu<0x15C>(scp)};
    fill();
    FORU(k, 4) { a[k] *= r[k]; c[k] *= r[k]; }
    f2 sc[4], sb[4], t[8], bw[2], sw[2];
    FORU(j, 4) sc[j] = bf2f(scb[j]);
    fill();
    FORU(k, 4) { a[k] = a[k] * bflo(s1v) + bflo(sav); c[k] = c[k] * bfhi(s1v) + bfhi(sav); }
    FORU(j, 8) t[j] = pv[j] * sc[j & 3];
    fill();
    v4u wpk[2];
    FORU(k, 4) { h0[k] = pkbf(a[k]); h1[k] = pkbf(c[k]); }
    FORU(j, 8) wpk[j >> 2][j & 3] = pkbf(t[j]);
    fill();
    lptr up = L + L_U + 80 * (15 + (tid >> 2)) + 16 * (tid & 3);
    *(LAS v4u*)up = v4u{h0[0], h0[1], h0[2], h0[3]};
    *(LAS v4u*)(up + U_CH) = v4u{h1[0], h1[1], h1[2], h1[3]};
    st128(rs, wio, wpk[0]);
    st128s(rs, wio, two ? 65536 : OOB, wpk[1]);
    const u32 sbb[4] = {dppu<0x150>(sbp), dppu<0x154>(sbp), dppu<0x158>(sbp), dppu<0x15C>(sbp)};
    FORU(j, 8) t[j] = bf2f(wpk[j >> 2][j & 3]);
    SB();
    FORU(j, 4) sb[j] = bf2f(sbb[j]);
    FORU(f, 2) sw[f] = (t[4 * f] + t[4 * f + 1]) + (t[4 * f + 2] + t[4 * f + 3]);
    SB();
    FORU(j, 8) pv[j] *= sb[j & 3];
    SB();
    FORU(f, 2) bw[f] = (pv[4 * f] + pv[4 * f + 1]) + (pv[4 * f + 2] + pv[4 * f + 3]);
    const float e0 = sw16sum(bw[0].x + bw[0].y, sw[0].x + sw[0].y), e1 = sw16sum(bw[1].x + bw[1].y, sw[1].x + sw[1].y);
    const int bwo = WS_BWP + (((pj * 2 + (pi & 1)) * 6 + w) * 16 + (pi >> 1)) * 128 + (lane & 31) * 4;
    __builtin_amdgcn_raw_buffer_store_b32(__float_as_uint(sw32sum(e0, e1)), rs, lane < 32 ? bwo : two ? bwo + 8192 : OOB, 0, SC1);
  }
  __syncthreads();
  STAMP(5);
  v4u cdv;
  const u32 fpo = lane < 48 ? WS_WINP + pj * 196608 + (((pi & 7) * 4 + w) * 48 + lane) * 128 : OOB;
  auto side = [&](auto S, auto I) {
    constexpr int s = S.v, i = I.v, z = 10 * s + i;
    if constexpr (z % 4 == 1 && z / 4 < 24) ldw(8 + z / 4);
    if (z == 99) pf3 = __builtin_amdgcn_raw_buffer_load_b32(rw, lane < 32 ? pfo : OOB, 512, 0);
    if (z == 43) {
      asm volatile("s_waitcnt vmcnt(11)" ::: "memory");
      __builtin_amdgcn_raw_buffer_store_b32(tag, rs, lane == 0 ? WS_CD + b * 64 + w * 4 : OOB, 0, SC1);
    }
    if (z == 123) pf1 = __builtin_amdgcn_raw_buffer_load_b32(rs, fpo, 0, SC1);
    if (z == 163) cdv = ld128(rs, WS_CD + tid * 64);
  };
  if (w & 1) conv2<1, 2>(L, w >> 1, lane, side);
  else conv2<0, 3>(L, w >> 1, lane, side);
  STAMP(21);
  {
    f2 y[2][4], t[2][4];
    const float ga0 = bflo(gav), ga1 = bfhi(gav);
    FORU(k, 4) { t[0][k] = bf2f(h0[k]); t[1][k] = bf2f(h1[k]); }
    FORU(k, 4) { y[0][k] = f2{bflo(xv[2 * k]), bflo(xv[2 * k + 1])}; y[1][k] = f2{bfhi(xv[2 * k]), bfhi(xv[2 * k + 1])}; }
    lptr yp[4];
    FORU(B, 4) {
      const u32 tp = 8 * tid - 512 * B;
      yp[B] = L + L_Y + (B == 0 ? 0 : B == 1 ? 8448 : B == 2 ? 14848 : 19200) + ((tp & 2047) / (128 - 32 * B)) * 16 + tp * 4;
      pin(yp[B]);
    }
    SB();
    FORU(k, 4) { t[0][k] *= bflo(p_dsk); t[1][k] *= bfhi(p_dsk); pin(y[0][k]); pin(y[1][k]); pin(t[0][k]); pin(t[1][k]); }
    __syncthreads();
    STAMP(6);
    f4 q[4][4];
    FORU(B, 4) {
      if (B <= (int)w) { FORU(e, 4) q[B][e] = *(LAS f4*)(yp[B] + (e >> 1) * Y_CH + (e & 1) * 16); }
      else { FORU(e, 4) q[B][e] = f4{0.f, 0.f, 0.f, 0.f}; }
    }
    FORU(B, 4) { FORU(e, 4) { t[e >> 1][2 * (e & 1)] += f2{q[B][e].x, q[B][e].y}; t[e >> 1][2 * (e & 1) + 1] += f2{q[B][e].z, q[B][e].w}; } SB(); }
    FORU(k, 4) { y[0][k] += t[0][k] * ga0; y[1][k] += t[1][k] * ga1; }
    SB();
    v4u o0, o1;
    FORU(k, 4) { o0[k] = pkbf(y[0][k]); o1[k] = pkbf(y[1][k]); }
    SB();
    st128(rs, x1w, san4(o0));
    st128(rs, x1w + 128, san4(o1));
  }
  CFENCE();
  asm volatile("" ::"v"(pf1), "v"(pf3));
  auto cd_bad = [&]() { return __any(umax3(umax3(cdv.x, cdv.y, cdv.z), cdv.w, tag) != umin3(umin3(cdv.x, cdv.y, cdv.z), cdv.w, tag)); };
  if (RARE(cd_bad())) {
#pragma nounroll
    for (int sp = 0; sp < SPIN_CAP; ++sp) {
      asm volatile("" ::: "memory");
      cdv = ld128(rs, WS_CD + tid * 64);
      if (!cd_bad()) break;
    }
  }
  __syncthreads();
  STAMP(7);
  const u32 blkS = 4 + (w >> 1);
  const bool hasS = blkS < nb;
  const u32 fo[4] = {((pj * 2 + 1) * 6 + w), ((pj * 2 + 1) * 6 + (hasS ? blkS : 4)), (pj * 12 + w), (pj * 12 + (hasS ? blkS : 4))};
  v4u ones = {0x3f803f80u, 0x3f803f80u, 0x3f803f80u, 0x3f803f80u};
  pin(ones);
  v4u wf[4][16];
  auto ldf = [&](int s, int k) { wf[s][k] = ld128s(rs, lane * 16 + (k & 3) * 1024, WS_WINP + fo[s] * 16384 + (k >> 2) * 4096); };
  f4 sW[4];
  f4 bW[4];
  auto ldb = [&](int s) {
    bW[s] = __builtin_bit_cast(f4, ld128s(rs, c16 * 128 + q4 * 16, WS_BWP + fo[s] * 2048));
    sW[s] = __builtin_bit_cast(f4, ld128s(rs, c16 * 128 + q4 * 16, WS_BWP + fo[s] * 2048 + 64));
  };
  auto redb = [&](int s) {
    FORU(e, 16) { f4& v = (e & 4) ? sW[s + (e >> 3)] : bW[s + (e >> 3)]; v[e & 3] = dppadd<0xB1>(v[e & 3]); }
    SB();
    FORU(e, 16) { f4& v = (e & 4) ? sW[s + (e >> 3)] : bW[s + (e >> 3)]; v[e & 3] = dppadd<0x4E>(v[e & 3]); }
    SB();
    FORU(e, 16) { f4& v = (e & 4) ? sW[s + (e >> 3)] : bW[s + (e >> 3)]; v[e & 3] = dppadd<0x141>(v[e & 3]); }
    SB();
    FORU(e, 16) { f4& v = (e & 4) ? sW[s + (e >> 3)] : bW[s + (e >> 3)]; v[e & 3] = dppadd<0x140>(v[e & 3]); }
    SB();
  };
  {
    ldb(0); ldb(1);
    v4u xr[16];
    const int xo = WS_X1 + pi * 65536 + w * 16384;
    sfor<0, 32>([&](auto z) {
      constexpr int n = z.v;
      if constexpr (n < 16) ldf(n & 1, n >> 1); else xr[n - 16] = ld128s(rs, lane * 16 + (n & 3) * 1024, xo + rq((n - 16) >> 2) * 4096);
      shuf(IC<2 * n>{}); shuf(IC<2 * n + 1>{});
      SB();
    });
    redb(0);
    pin(bW[0]); pin(bW[1]); pin(sW[0]); pin(sW[1]);
    STAMP(8);
    lptr xi = L + L_XIMG + w * 16384 + (lane >> 3) * 128 + (lane & 1) * 16;
    const u32 xs0 = (((lane & 7) >> 1) ^ ((lane >> 4) & 1)) * 32;
    u32 m = 0;
    FORU(k, 16) { *(LAS v4u*)(xi + rq(k >> 2) * 4096 + (k & 3) * 1024 + (xs0 ^ ((k & 1) * 64))) = xr[k]; m = vmax(m, xr[k]); ldf(k & 1, 8 + (k >> 1)); SB(); }
    if (RARE(__any(m == ~0u))) {
#pragma nounroll
      for (int sp = 0; sp < SPIN_CAP; ++sp) {
        asm volatile("" ::: "memory");
        FORU(k, 16) xr[k] = ld128s(rs, lane * 16, xo + rq(k >> 2) * 4096 + (k & 3) * 1024);
        SB();
        m = 0;
        FORU(k, 16) { *(LAS v4u*)(xi + rq(k >> 2) * 4096 + (k & 3) * 1024 + (xs0 ^ ((k & 1) * 64))) = xr[k]; m = vmax(m, xr[k]); SB(); }
        CNT(29, 100);
        if (!__any(m == ~0u)) break;
      }
    }
    STAMP(9);
  }
  __syncthreads();
  STAMP(10);
  const u32 xsw = ((c16 >> 3) & 1) | ((q4 & 1) << 1), xbo = (8 * q4 + (c16 >> 2)) * 128 + 8 * (c16 & 3);
  lptr xb[4];
#pragma unroll
  for (int t = 0; t < 4; ++t) xb[t] = L + xbo + ((t ^ xw ^ xsw) * 32);
  v2u bl[3][4], bh[3][4];
  auto ldx = [&](int k, int i) {
    if (k < 16) { if (i & 1) bh[k % 3][i >> 1] = trread(xb[i >> 1] + 4096 * k + 512); else bl[k % 3][i >> 1] = trread(xb[i >> 1] + 4096 * k); }
  };
  auto bx = [&](int k, int t) { return v4u{bl[k % 3][t].x, bl[k % 3][t].y, bh[k % 3][t].x, bh[k % 3][t].y}; };
  auto xwait = [&](int k) { asm volatile("" ::"v"(bh[k % 3][3])); };
  const int pco = WS_PB + ((pi * 4 + w) * 44 + blk0) * 512, pc2 = 128 + lane < 32 * nb ? lane * 16 : OOB;
  const int zco = WS_Z2N + ((pj * 6 + (w >> 1)) * 64 + 2 * pi + (w & 1)) * 1024, zc2 = w + 8 < 2 * nb ? zco + 262144 : OOB;
  f4 z2a[6];
  f2 rn[4];
  {
    f4 st0, st1;
    v2u sl[16], sh[16];
    FORU(k, 4) { sl[k] = trread(xb[0] + 4096 * k); sh[k] = trread(xb[0] + 4096 * k + 512); }
    MM_HEAD();
    sfor<0, 16>([&](auto z) {
      constexpr int k = z.v;
      const v4u v = {sl[k].x, sl[k].y, sh[k].x, sh[k].y};
      if (k) m_vv(st0, ones, v); else m_vv0(st0, ones, v);
      if constexpr (k + 4 < 16) sl[k + 4] = trread(xb[0] + 4096 * (k + 4)); else { ldx((k - 12) >> 1, 4 * (k & 1)); ldx((k - 12) >> 1, 4 * (k & 1) + 1); }
      SB();
      if (k) m_vv(st1, v, v); else m_vv0(st1, v, v);
      if constexpr (k + 4 < 16) sh[k + 4] = trread(xb[0] + 4096 * (k + 4) + 512); else { ldx((k - 12) >> 1, 4 * (k & 1) + 2); ldx((k - 12) >> 1, 4 * (k & 1) + 3); }
      if (k == 1) st128s(rs, lane * 16, pco, SENT);
      if (k == 5) st128s(rs, lane * 16, pco + 1024, SENT);
      if (k == 9) st128s(rs, pc2, pco + 2048, SENT);
      if (k == 3) st128s(rs, lane * 16, zco, SENT);
      if (k == 7) st128s(rs, lane * 16, zco + 131072, SENT);
      if (k == 11) st128s(rs, lane * 16, zc2, SENT);
      SB();
    });
    float sv0 = 0.f, sv1 = 0.f;
    FORU(k, 16) FORU(i, 6) {
      const int u = i == 0 ? 0 : i == 1 ? 4 : i == 2 ? 1 : i == 3 ? 5 : i - 2;
      if (i == 0) xwait(k);
      if (k) m_vv(z2a[u], wf[u >> 2][k], bx(k, u & 3)); else m_vv0(z2a[u], wf[u >> 2][k], bx(k, u & 3));
      if (i < 2) { ldx(k + 2, 2 * i); ldx(k + 2, 2 * i + 1); } else ldx(k + 2, i + 2);
      if (i == 2 && k) ldf(2 + ((k - 1) & 1), (k - 1) >> 1);
      if (i == 4 && (k & 1)) ldw(32 + (k >> 1));
      if (i == 3 || i == 5) {
        const int z = 2 * k + (i == 5);
        if (z == 4) sv1 = (c16 & 1) ? st1.y : st1.x;
        if (z == 5) sv0 = (c16 & 1) ? st1.w : st1.z;
        if (z == 6) sv1 = (c16 & 2) ? sv0 : sv1;
        if (z == 7) { pin(st0); sv0 = st0.x * (1.f / 512); }
        if (z == 8) sv1 *= (1.f / 512);
        if (z == 9) sv1 = fmaxf(__builtin_fmaf(-sv0, sv0, sv1), 0.f);
        if (z == 10) sv1 = rbf(sv1);
        if (z == 11) sv0 = rbf(sv0);
        if (z == 12) sv1 = rbf(sv1 + 1e-5f);
        if (z == 13) sv1 = __builtin_amdgcn_sqrtf(sv1);
        if (z == 14) sv1 = rbf(sv1);
        if (z == 15) sv1 = __builtin_amdgcn_rcpf(sv1);
        if (z == 16) sv0 *= -sv1;
        if (z == 17) *(volatile LAS f2*)(L + (q4 == (c16 >> 2) ? L_XST + (16 * xw + c16) * 8 : L_XST + 512 + lane * 8)) = f2{sv1, sv0};
        if (z >= 4 && z <= 16) { pin(sv0); pin(sv1); }
        if (z == 24) __builtin_amdgcn_s_barrier();
        if (z >= 26 && z < 30) rn[z - 26] = *(volatile LAS f2*)(L + L_XST + (16 * ((z - 26) ^ xw) + c16) * 8);
      }
      SB();
    }
    MM_TAIL();
  }
  STAMP(24);
  auto shuf2 = [&](auto C, auto N) {
    constexpr int n = N.v % 12, z = 16 * (C.v + N.v / 12);
    if constexpr (N.v < 24) { if constexpr (n < 8) shuf(IC<z + n>{}); else { shuf(IC<z + 2 * n - 8>{}); shuf(IC<z + 2 * n - 7>{}); } }
  };
  auto epi = [&](f4 (&a)[6], int s0, auto fill) {
    f2 v[12];
    sfor<0, 2>([&](auto z) {
      FORU(j, 6) {
        const int i = 6 * z.v + j;
        const f4 &sw = sW[s0 + (i >> 3)], &bw = bW[s0 + (i >> 3)];
        v[i] = ((i & 1) ? f2{sw.z, sw.w} : f2{sw.x, sw.y}) * rn[(i >> 1) & 3].y + ((i & 1) ? f2{bw.z, bw.w} : f2{bw.x, bw.y});
      }
      fill(IC<z.v>{});
    });
    sfor<0, 2>([&](auto z) {
      FORU(j, 6) { const int i = 6 * z.v + j; v[i] = ((i & 1) ? f2{a[i >> 1].z, a[i >> 1].w} : f2{a[i >> 1].x, a[i >> 1].y}) * rn[(i >> 1) & 3].x + v[i]; }
      fill(IC<2 + z.v>{});
    });
    gelu_n<12>(v, fill);
    FORU(i, 6) { a[i] = f4{v[2 * i].x, v[2 * i].y, v[2 * i + 1].x, v[2 * i + 1].y}; pin(a[i]); }
  };
  {
    epi(z2a, 0, [&](auto N) {
      constexpr int n = N.v;
      SB();
      if constexpr ((n & 1) && n < 22) ldf(2 + ((15 + n / 2) & 1), (15 + n / 2) >> 1);
      if constexpr (!(n & 1) && n < 24) ldw(40 + n / 2);
      SB();
    });
    if (!hasS) { z2a[4] = f4{0.f, 0.f, 0.f, 0.f}; z2a[5] = z2a[4]; }
    f2 zs[6], zq[6];
    FORU(i, 6) { zs[i] = f2{z2a[i].x, z2a[i].y} + f2{z2a[i].z, z2a[i].w}; zq[i] = f2{z2a[i].x, z2a[i].y} * f2{z2a[i].x, z2a[i].y}; }
    SB();
    FORU(i, 6) zq[i] = __builtin_elementwise_fma(f2{z2a[i].z, z2a[i].w}, f2{z2a[i].z, z2a[i].w}, zq[i]);
    FORU(i, 2) zs[i] += zs[4 + i];
    SB();
    float us[4], uq[4], u[4];
    FORU(i, 2) zq[i] += zq[4 + i];
    FORU(t, 4) us[t] = zs[t].x + zs[t].y;
    SB();
    FORU(t, 4) uq[3 - t] = zq[3 - t].x + zq[3 - t].y;
    SB();
    FORU(t, 4) u[3 - t] = sw32sum(us[3 - t], uq[3 - t]);
    LAS float* zp = (LAS float*)(L + L_ZST) + (w * 2 + (lane >> 5)) * 64 + c16;
    zp[16 * ((2 * (q4 & 1) + 1) ^ xw)] = sw16sum(u[1], u[3]);
    zp[16 * ((2 * (q4 & 1)) ^ xw)] = sw16sum(u[0], u[2]);
  }
  asm volatile("s_waitcnt vmcnt(20)" ::: "memory");
  __syncthreads();
  if (w == 0) {
    LAS float* zp = (LAS float*)(L + L_ZST) + lane;
    const float s = (zp[0] + zp[128]) + (zp[256] + zp[384]), ss = (zp[64] + zp[192]) + (zp[320] + zp[448]);
    st128(rs, WS_ST2 + ((pi * 8 + pj) * 64 + lane) * 16, v4u{__float_as_uint(s), tag, __float_as_uint(ss), tag});
  }
  __builtin_amdgcn_raw_buffer_store_b32(tag, rs, tid == 64 ? WS_F3 + (pj * 32 + pi) * 128 : OOB, 0, SC1);
  CFENCE();
  STAMP(11);
  v4u wo[6][4];
  const u32 wob = WS_WOP + (pj * 22 + 6 * w) * 4096, wobh = ssel(w < 3, wob, OOB);
  auto ldo = [&](int n) { wo[n >> 2][n & 3] = ld128s(rs, lane * 16 + (n & 3) * 1024, (n < 16 ? wob : wobh) + (n >> 2) * 4096); };
  f4 z1a[6];
  v2u xres[4];
  v4u g[8];
  u32 fv;
  auto f_ld = [&]() { fv = __builtin_amdgcn_raw_buffer_load_b32(rs, (lane & 7) * 128, WS_F3 + (pj * 32 + 8 * w) * 128, SC1); };
  auto s2b_ld = [&](int n) { g[n] = ld128s(rs, (2 * q4 * 64 + c16) * 16 + (n & 1) * 1024, WS_ST2 + pi * 8192 + ((n >> 1) ^ xw) * 256); };
  {
    FORU(i, 16) ldx(i >> 3, i & 7);
    MM_HEAD();
    sfor<0, 96>([&](auto z) {
      constexpr int k = z.v / 6, i = z.v % 6, u = i == 0 ? 0 : i == 1 ? 4 : i == 2 ? 1 : i == 3 ? 5 : i - 2;
      if (i == 0) xwait(k);
      if (k) m_vv(z1a[u], wf[2 + (u >> 2)][k], bx(k, u & 3)); else m_vv0(z1a[u], wf[2 + (u >> 2)][k], bx(k, u & 3));
      if (i < 2) { ldx(k + 2, 2 * i); ldx(k + 2, 2 * i + 1); } else ldx(k + 2, i + 2);
      if (k < 3 && (i == 2 || i == 4)) ldf(1 + (i >> 1), 13 + k);
      if (k >= 3 && k < 9 && i == 3) ldw(49 + k);
      if ((k == 9 || k == 10) && i >= 2) s2b_ld(4 * (k - 9) + i - 2);
      if (k >= 11 && k < 14 && (i == 2 || i == 4)) ldw(58 + 2 * (k - 11) + (i >> 2));
      if (k == 14 && (i == 2 || i == 4)) ldb(1 + (i >> 1));
      if (k == 15 && i == 3) f_ld();
      SB();
    });
    MM_TAIL();
#pragma unroll
    for (int n = 0; n < 4; ++n) {
      const u32 row = 16 * n + 4 * q4 + (c16 >> 2);
      xres[n] = trread(L + L_XIMG + pj * 8192 + row * 128 + ((w ^ (((row >> 1) & 1) | (((row >> 3) & 1) << 1))) * 32) + 8 * (c16 & 3));
    }
  }
  STAMP(12);
  {
    auto bad = [&]() { u32 mx = tag, mn = tag; FORU(n, 8) { mx = umax3(mx, g[n].y, g[n].w); mn = umin3(mn, g[n].y, g[n].w); } return __any(mx != mn); };
    if (RARE(bad())) {
#pragma nounroll
      for (int sp = 0; sp < SPIN_CAP; ++sp) {
        asm volatile("" ::: "memory");
        FORU(n, 8) s2b_ld(n);
        CNT(29, 10000);
        if (!bad()) break;
      }
    }
    STAMP(13);
    lptr tw = L + L_TRS + w * TRS_W;
    float a[4], c[4];
    FORU(t, 4) { a[t] = __uint_as_float(g[2 * t].x) + __uint_as_float(g[2 * t + 1].x); c[t] = __uint_as_float(g[2 * t].z) + __uint_as_float(g[2 * t + 1].z); }
    SB();
    const float a0 = sw32sum(a[0], a[2]), a1 = sw32sum(a[1], a[3]), c0 = sw32sum(c[0], c[2]), c1 = sw32sum(c[1], c[3]);
    const float m = sw16sum(a0, a1) * (1.f / 688), var = fmaxf(sw16sum(c0, c1) * (1.f / 688) - m * m, 0.f);
    const float r = __builtin_amdgcn_rcpf(ln_den(var)), n0 = -rbf(m) * r;
    auto gather = [&](float v, float (&o)[4]) {
      auto e = __builtin_amdgcn_permlane16_swap(__float_as_uint(v), __float_as_uint(v), false, false);
      auto e0 = __builtin_amdgcn_permlane32_swap(e[0], e[0], false, false);
      auto e1 = __builtin_amdgcn_permlane32_swap(e[1], e[1], false, false);
      o[0] = __uint_as_float(e0[0]); o[2] = __uint_as_float(e0[1]); o[1] = __uint_as_float(e1[0]); o[3] = __uint_as_float(e1[1]);
    };
    gather(r, a); gather(n0, c);
    SB();
    FORU(i, 6) z2a[i] = z2a[i] * a[i & 3] + c[i & 3];
    SB();
    FORU(i, 6) *(LAS v2u*)(tw + (16 * ((i & 3) ^ xw) + c16) * 64 + 8 * (((i & 4) + q4) ^ (c16 >> 1))) = v2u{pkbf(z2a[i].x, z2a[i].y), pkbf(z2a[i].z, z2a[i].w)};
    const u32 r0 = 8 * q4 + (c16 >> 2);
    const int zo = WS_Z2N + (pj * 6 * 64 + 2 * pi) * 1024 + lane * 16;
#pragma unroll
    for (int n = 0; n < 3; ++n) {
      lptr pp = tw + (n < 2 ? n : (w & 1)) * 2048 + r0 * 64;
      const u32 un = (n < 2 ? 0 : 4) + (c16 & 3);
      const v2u lo = trread(pp + 8 * (un ^ ((r0 >> 1) & 7))), hi = trread(pp + 256 + 8 * (un ^ (((r0 + 4) >> 1) & 7)));
      st128s(rs, zo, n < 2 ? w * 65536 + n * 1024 : (hasS ? blkS * 65536 + (w & 1) * 1024 : OOB), san4(v4u{lo.x, lo.y, hi.x, hi.y}));
    }
  }
  CFENCE();
  STAMP(14);
  u32 bsr[4], gbp[2];
  v4u gg[2];
  FORU(t, 4) bsr[t] = gld<u16>(bsp, (64 * pi + 16 * (t ^ xw) + c16) * 2);
  FORU(m, 2) gg[m] = ld128(rs, WS_MOD + ((lane & 3) * 1536 + 1280 + 32 * pj + 16 * m + 8 * (c16 >> 3) + 2 * q4 + ((c16 >> 2) & 1)) * 16);
  if (RARE(__any(fv != tag))) {
#pragma nounroll
    for (int sp = 0; sp < SPIN_CAP; ++sp) {
      asm volatile("" ::: "memory");
      f_ld();
      if (!__any(fv != tag)) break;
    }
  }
  CFENCE();
  v2u z1[6];
  f4 ga[2][4], q[20];
  lptr qa[6];
  auto ldq = [&](int n) { q[n] = *(volatile LAS f4*)(n < 12 ? qa[n & 3] + (n >> 2) * 4096 : qa[4 + (n & 1)] + ((n - 12) >> 1) * 4096); };
  {
    const u32 zo = WS_Z2N + (pj * 6 * 64 + 16 * w) * 1024;
    const u32 rot = ((nb == 6 ? 0x1420u : 0x1320u) >> (4 * (pi & 3))) & 15;
    v4u zr[16];
    u32 zv[4];
    FORU(g, 4) zv[g] = lane * 16 + rk(g) * 4096;
    u32 so[6], rsl[6];
    FORU(it, 5) {
      const u32 blk = smod(w + 1 + smod((it > 1 ? it + nb - 6 : it) + rot, nb - 1), nb);
      so[it] = zo + blk * 65536;
      rsl[it + 1] = ssel(blk < 4, blk * 3 + ((w - blk - 1) & 3), 4 * blk - 4 + w) * 4096;
    }
    so[5] = zo + w * 65536;
    auto ldz = [&](int k, u32 s0) { zr[k] = ld128s(rs, zv[k >> 2] + (k & 3) * 1024, s0); };
    FORU(n, 7) ldo(n);
    redb(2);
    epi(z1a, 2, [&](auto N) {
      constexpr int n = N.v;
      SB();
      shuf2(IC<4>{}, N);
      if constexpr (n < 5) ldo(7 + n);
      if constexpr (n >= 6 && n < 22) ldz(n - 6, so[0]);
      SB();
    });
    FORU(i, 6) { z1[i] = v2u{pkbf(z1a[i].x, z1a[i].y), pkbf(z1a[i].z, z1a[i].w)}; pin(z1[i]); }
    FORU(m, 2) { gbp[m] = pkbf(quadsum(__uint_as_float(gg[m].x)), quadsum(__uint_as_float(gg[m].z))); pin(gbp[m]); }
    STAMP(25);
    __syncthreads();
    {
      auto bad = [&]() { u32 m = 0; FORU(k, 16) m = vmax(m, zr[k]); return __any(m == ~0u); };
      if (RARE(bad())) {
#pragma nounroll
        for (int sp = 0; sp < SPIN_CAP; ++sp) {
          asm volatile("" ::: "memory");
          FORU(k, 16) ldz(k, so[0]);
          CNT(28, 100);
          if (!bad()) break;
        }
      }
    }
    STAMP(15);
    u32 ra = 0, badm = 0;
    float ck = 0.f;
    v4u oth;
    MM_HEAD();
    auto slot = [&](auto z) {
      constexpr int it = z.v / 64, k = (z.v / 4) % 16, t = z.v % 4, h = it & 1;
      if (k) m_va(ga[h][t], zr[k], wsf[k][t]); else m_va0(ga[h][t], zr[k], wsf[k][t]);
      constexpr int pz = z.v / 4 - 1, pit = pz / 16, pk = pz % 16;
      if constexpr (t == 1 && pz >= 0) {
        if constexpr (pit < 5) ldz(pk, so[pit + 1]);
        else if constexpr (pk < 6) ldo(12 + pk);
      }
      if constexpr (it == 0 && t != 1 && 3 * k + t - (t > 0) < 32) shuf(IC<96 + 3 * k + t - (t > 0)>{});
      if constexpr (it > 0) {
        if (k == 2 && t == 0) { ck = ga[h ^ 1][0].x + ga[h ^ 1][0].y; pin(ck); }
        if (k == 3 && t == 0) { ck += ga[h ^ 1][0].z; pin(ck); }
        if (k == 4 && t == 0) { ck += ga[h ^ 1][0].w; pin(ck); }
        if (k == 5 && t == 0) badm |= __any(ck != ck) ? 1u << (it - 1) : 0u;
        if (k >= 2 && k < 6 && t == 2) { ra = rsl[it] + lane * 16 + (((k - 2) ^ xw) * 1024); pin(ra); }
        if constexpr (k >= 2 && k < 6 && t == 3) *(volatile LAS f4*)(L + L_RED + ra) = ga[h ^ 1][k - 2];
      }
      if constexpr (it == 5) {
        if constexpr (z.v >= 320 + 28 && z.v < 320 + 34) {
          constexpr int i = z.v - 348;
          u32 l16 = lane * 16;
          pin(l16);
          qa[i] = L + l16 + (L_RED + ((i < 4 ? w * 12 : (blkS * 4 - 4) * 4) + ((i & 3) ^ xw)) * 1024);
          pin(qa[i]);
        }
        if (k == 6 && t == 2) *(volatile LAS u32*)(L + L_XST + w * 4) = badm;
        if (k == 8 && t == 3) { asm volatile("s_waitcnt lgkmcnt(0)" ::: "memory"); __builtin_amdgcn_s_barrier(); }
        if (k == 9 && t == 0) oth = *(volatile LAS v4u*)(L + L_XST);
        if constexpr (z.v >= 320 + 37 && z.v < 320 + 57) ldq(z.v - 357);
      }
      SB();
    };
    sfor<0, 64>(slot);
    if (nb == 6) sfor<64, 128>(slot);
    else {
      MM_TAIL();
      const float t = (ga[0][0].x + ga[0][0].y) + (ga[0][0].z + ga[0][0].w);
      badm = __any(t != t) ? 1u : 0u;
      FORU(t, 4) { *(volatile LAS f4*)(L + L_RED + rsl[1] + lane * 16 + ((t ^ xw) * 1024)) = ga[0][t]; ga[1][t] = f4{0.f, 0.f, 0.f, 0.f}; }
      MM_HEAD();
    }
    sfor<128, 384>(slot);
    MM_TAIL();
    { const float t = (ga[1][0].x + ga[1][0].y) + (ga[1][0].z + ga[1][0].w); badm |= __any(t != t) ? 32u : 0u; }
    const u32 anyo = __builtin_amdgcn_readfirstlane(oth.x | oth.y | oth.z | oth.w);
    if (RARE(badm | anyo)) {
#pragma nounroll
      for (u32 it = 0; it < 6; ++it) {
        if (!((badm >> it) & 1)) continue;
        CNT(28, 1);
        const u32 s0 = it == 0 ? so[0] : it == 1 ? so[1] : it == 2 ? so[2] : it == 3 ? so[3] : it == 4 ? so[4] : so[5];
        const u32 r0 = it == 0 ? rsl[1] : it == 1 ? rsl[2] : it == 2 ? rsl[3] : it == 3 ? rsl[4] : rsl[5];
#pragma nounroll
        for (int sp = 0; sp < SPIN_CAP; ++sp) {
          u32 m = 0;
          FORU(k, 16) ldz(k, s0);
          FORU(k, 16) m = vmax(m, zr[k]);
          asm volatile("" ::: "memory");
          if (!__any(m == ~0u)) break;
        }
        MM_HEAD();
        FORU(k, 16) FORU(t, 4) { if (k) m_va(ga[0][t], zr[k], wsf[k][t]); else m_va0(ga[0][t], zr[k], wsf[k][t]); }
        MM_TAIL();
        FORU(t, 4) {
          if (it < 5) *(LAS f4*)(L + L_RED + r0 + lane * 16 + ((t ^ xw) * 1024)) = ga[0][t];
          else ga[1][t] = ga[0][t];
        }
      }
      if (anyo) __syncthreads();
      FORU(n, 20) ldq(n);
    }
  }
  __builtin_amdgcn_raw_buffer_store_b32(tag, rs, lane == 0 ? 0 : OOB, w == 0 ? WS_H4 + (pi * 8 + pj) * 128 : OOB, SC1);
  STAMP(26);
  {
    const u32 lane = fresh_lane(), c16 = lane & 15, q4 = lane >> 4;
    f2 zf[12], gs2[12];
    auto half = [](const f4& v, int hh) { return hh ? f2{v.z, v.w} : f2{v.x, v.y}; };
    FORU(i, 12) gs2[i] = i < 8 ? half(q[i >> 1], i & 1) + half(q[4 + (i >> 1)], i & 1) : half(q[8 + (i >> 1)], i & 1) + half(q[10 + (i >> 1)], i & 1);
    FORU(i, 12) zf[i] = i < 8 ? half(ga[1][i >> 1], i & 1) + half(q[8 + (i >> 1)], i & 1) : half(q[12 + (i >> 1)], i & 1) + half(q[14 + (i >> 1)], i & 1);
    SB();
    FORU(i, 12) gs2[i] += bflo(bsr[(i >> 1) & 3]);
    SB();
    FORU(i, 12) gs2[i] += zf[i];
    SB();
    FORU(i, 12) zf[i] = bf2f(z1[i >> 1][i & 1]);
    SB();
    FORU(i, 12) gs2[i] *= zf[i];
    SB();
    v2u P[6];
    FORU(i, 12) P[i >> 1][i & 1] = pkbf(gs2[i]);
    SB();
#pragma unroll
    for (int h = 0; h < 3; ++h) {
      auto rx = __builtin_amdgcn_permlane16_swap(P[2 * h].x, P[2 * h + 1].x, false, false);
      auto ry = __builtin_amdgcn_permlane16_swap(P[2 * h].y, P[2 * h + 1].y, false, false);
      const u32 tt = (2 * (h & 1) + (q4 & 1)) ^ xw;
      st128s(rs, WS_PB + ((pi * 4 + tt) * 44 + blk0) * 512 + (q4 >> 1) * 256 + c16 * 16, h < 2 ? w * 512 : (hasS ? blkS * 512 : OOB),
             san4(v4u{rx[0], ry[0], rx[1], ry[1]}));
    }
    FORU(n, 6) ldo(18 + n);
  }
  STAMP(17);
  {
    const u32 lane = fresh_lane(), c16 = lane & 15, q4 = lane >> 4;
    f4 oa[2][4];
    v4u pr[4][6];
    u32 pb[4], pbh[4];
    u32 rd[3];
    FORU(u, 4) {
      const u32 d = u < 3 ? 1 + smod(u + smod(pj & 3, 3), 3) : 4;
      pb[u] = WS_PB + ((pi * 4 + ((w + d) & 3)) * 44 + 12 * w) * 512; pbh[u] = ssel(w < 3, pb[u] + 4096, OOB);
      if (u < 3) rd[u] = L_RED2 + (((w + d) & 3) * 3 + 3 - d) * 4096 + lane * 16;
    }
    const u32 pv3 = (w == 3 && lane >= 32) ? OOB : lane * 16 + 3072;
    auto ldp = [&](int n) {
      const int u = n / 6, k = n % 6;
      pr[u][k] = ld128s(rs, k == 3 ? pv3 : lane * 16 + (k & 3) * 1024, k < 4 ? pb[u] : pbh[u]);
    };
#pragma nounroll
    for (int sp = 0; sp < SPIN_CAP; ++sp) {
      asm volatile("" ::: "memory");
      if (!__any(__builtin_amdgcn_raw_buffer_load_b32(rs, (lane & 7) * 128, WS_H4 + pi * 1024, SC1) != tag)) break;
      CNT(28, 10000);
      __builtin_amdgcn_s_sleep(2);
    }
    STAMP(18);
    FORU(n, 12) { ldp(n); SB(); }
    u32 bad = 0;
    MM_HEAD();
    sfor<0, 96>([&](auto z) {
      constexpr int u = z.v / 24, k = (z.v / 4) % 6, n = z.v % 4;
      if (k) m_vv(oa[u & 1][n], wo[k][n], pr[u][k]); else m_vv0(oa[u & 1][n], wo[k][n], pr[u][k]);
      if constexpr (n == 1 && z.v / 4 < 12) ldp(12 + z.v / 4);
      if constexpr (u > 0 && k == 2 && n == 0) bad |= __any(oa[(u & 1) ^ 1][0].x != oa[(u & 1) ^ 1][0].x) ? 1u << (u - 1) : 0u;
      if constexpr (u > 0 && k >= 2 && n == 2) *(LAS f4*)(L + rd[u - 1] + (k - 2) * 1024) = oa[(u & 1) ^ 1][k - 2];
      SB();
    });
    MM_TAIL();
    bad |= __any(oa[1][0].x != oa[1][0].x) ? 8u : 0u;
    if (RARE(bad)) {
#pragma nounroll
      for (u32 u = 0; u < 4; ++u) {
        if (!((bad >> u) & 1)) continue;
        CNT(28, 1000000);
        pb[0] = u == 0 ? pb[0] : u == 1 ? pb[1] : u == 2 ? pb[2] : pb[3];
        pbh[0] = u == 0 ? pbh[0] : u == 1 ? pbh[1] : u == 2 ? pbh[2] : pbh[3];
#pragma nounroll
        for (int sp = 0; sp < SPIN_CAP; ++sp) {
          u32 m = 0;
          asm volatile("" ::: "memory");
          FORU(k, 6) ldp(k);
          FORU(k, 6) m = vmax(m, pr[0][k]);
          if (!__any(m == ~0u)) break;
        }
        asm volatile("fk_text_b:");
        MM_HEAD();
        FORU(k, 6) FORU(n, 4) { if (k) m_vv(oa[0][n], wo[k][n], pr[0][k]); else m_vv0(oa[0][n], wo[k][n], pr[0][k]); }
        MM_TAIL();
        FORU(n, 4) {
          if (u < 3) *(LAS f4*)(L + (u == 0 ? rd[0] : u == 1 ? rd[1] : rd[2]) + n * 1024) = oa[0][n];
          else oa[1][n] = oa[0][n];
        }
      }
    }
    __syncthreads();
    f4 o[4];
    FORU(n, 4) {
      lptr p = L + L_RED2 + (w * 12 + n) * 1024 + lane * 16;
      o[n] = (oa[1][n] + *(LAS f4*)p) + (*(LAS f4*)(p + 4096) + *(LAS f4*)(p + 8192));
    }
    STAMP(19);
    v2u O[4];
#pragma unroll
    for (int n = 0; n < 4; ++n) {
      const u32 g0 = (n & 1) ? dppu<0x158>(gbp[n >> 1]) : dppu<0x150>(gbp[n >> 1]), g1 = (n & 1) ? dppu<0x15C>(gbp[n >> 1]) : dppu<0x154>(gbp[n >> 1]);
      O[n] = v2u{pkbf(bf2f(xres[n].x) + bf2f(g0) * f2{o[n].x, o[n].y}), pkbf(bf2f(xres[n].y) + bf2f(g1) * f2{o[n].z, o[n].w})};
    }
#pragma unroll
    for (int h = 0; h < 2; ++h) {
      auto rx = __builtin_amdgcn_permlane16_swap(O[2 * h].x, O[2 * h + 1].x, false, false);
      auto ry = __builtin_amdgcn_permlane16_swap(O[2 * h].y, O[2 * h + 1].y, false, false);
      *(v4u*)(out + (64 * pi + 16 * w + c16) * 512 + 64 * pj + 16 * (2 * h + (q4 & 1)) + 8 * (q4 >> 1)) = v4u{rx[0], ry[0], rx[1], ry[1]};
    }
  }
  if (b == 0 && w == 0) __builtin_amdgcn_raw_buffer_store_b32(tag, rs, WS_E, 0, SC1);
  STAMP(20);
  STAMP_FLUSH();
}

__global__ void pre_kernel(unsigned char* ws) {
  if (blockIdx.x == 0 && threadIdx.x == 0) *(volatile u32*)(ws + WS_DBG) = (u32)__builtin_amdgcn_s_memrealtime();
}

extern "C" void kernel_launch(void* const* d_in, const int* in_sizes, int n_in, void* d_out, int out_size, void* d_ws, size_t ws_size,
                              hipStream_t stream) {
  (void)in_sizes; (void)n_in; (void)out_size; (void)ws_size;
  (void)hipFuncSetAttribute((const void*)fused_kernel, hipFuncAttributeMaxDynamicSharedMemorySize, LDS_BYTES);
  pre_kernel<<<256, 64, 0, stream>>>((unsigned char*)d_ws);
  fused_kernel<<<256, 256, LDS_BYTES, stream>>>((const u16*)d_in[0], (const u16*)d_in[1], (const u16*)d_in[2], (const u16*)d_in[3],
                                                (const u16*)d_in[4], (const u16*)d_in[5], (const u16*)d_in[6], (const u16*)d_in[8],
                                                (const u16*)d_in[9], (const u16*)d_in[10], (const u16*)d_in[11], (const u16*)d_in[12],
                                                (const u16*)d_in[13], (u16*)d_out, (unsigned char*)d_ws);
}
```

```cpp
#include <hip/hip_runtime.h>
#include <stdint.h>

typedef unsigned u32;
typedef unsigned short u16;
typedef unsigned long long u64;
typedef u32 v4u __attribute__((ext_vector_type(4)));
typedef u32 v2u __attribute__((ext_vector_type(2)));
typedef float f4 __attribute__((ext_vector_type(4)));
typedef float f2 __attribute__((ext_vector_type(2)));
typedef __bf16 bf8 __attribute__((ext_vector_type(8)));
typedef __bf16 bf2 __attribute__((ext_vector_type(2)));
typedef _Float16 h8 __attribute__((ext_vector_type(8)));
typedef _Float16 h2 __attribute__((ext_vector_type(2)));
typedef short s4 __attribute__((ext_vector_type(4)));
#define LAS __attribute__((address_space(3)))
#define GAS __attribute__((address_space(1)))
#define CAS __attribute__((address_space(4)))
#define DI __device__ __forceinline__
typedef __amdgpu_buffer_rsrc_t rsrc_t;
typedef LAS unsigned char* lptr;

#define XMAP 2

constexpr int WS_E = 0, WS_STAT1 = 4096, WS_CD = 32768, WS_ST2 = 65536, WS_F3 = 327680, WS_H4 = 360448, WS_MOD = 524288, WS_BWP = 655360;
constexpr int WS_XG = 1 << 20, WS_X1 = 5 << 20, WS_Z2N = 7 << 20, WS_PB = 10 << 20, WS_WINP = 13 << 20, WS_WOP = 29 << 19;
constexpr int WS_DBG = 24 << 20, WS_END = 25 << 20;
constexpr int OOB = 0x40000000;
constexpr int TABP = 544, TAB_CH = 63 * TABP;
constexpr int L_TAB = 0, L_Y = 0, Y_CH = 21504;
constexpr int L_PKW = 75776, L_SHF = 81920;
constexpr int L_PKI = 68544;
constexpr int L_TOE = 108544, TOE_C = 5312, TOE_CH = 4 * TOE_C;
constexpr int L_U = 151040, U_CH = 6320;
constexpr int L_XIMG = 0, L_TRS = 65536, TRS_W = 4096;
constexpr int L_RED = 0, L_RED2 = 81920;
constexpr int L_XST = 159744, L_ZST = L_XST + 1024;
constexpr int LDS_BYTES = 163840;
constexpr int SC1 = 16;
constexpr int SPIN_CAP = 1 << 15;
static_assert(LDS_BYTES <= 163840, "LDS");

DI float bflo(u32 v) { return __uint_as_float(v << 16); }
DI float bfhi(u32 v) { return __uint_as_float(v & 0xffff0000u); }
DI f2 bf2f(u32 v) { return f2{bflo(v), bfhi(v)}; }
DI u32 pkbf(float a, float b) { f2 v = {a, b}; return __builtin_bit_cast(u32, __builtin_convertvector(v, bf2)); }
DI u32 pkbf(f2 v) { return __builtin_bit_cast(u32, __builtin_convertvector(v, bf2)); }
DI u32 pkbf_a(float a, float b) { u32 r; asm("v_cvt_pk_bf16_f32 %0, %1, %2" : "=v"(r) : "v"(a), "v"(b)); return r; }
DI u32 pkh(f2 v) { return __builtin_bit_cast(u32, __builtin_convertvector(v, h2)); }
DI float rbf(float a) { return bflo(pkbf(a, a)); }
DI u32 san(u32 v) { return v < 0xFFFFFFFEu ? v : 0xFFFFFFFEu; }
DI v4u san4(v4u v) { return v4u{san(v.x), san(v.y), san(v.z), san(v.w)}; }
DI u32 umax3(u32 a, u32 b, u32 c) { return max(max(a, b), c); }
DI u32 umin3(u32 a, u32 b, u32 c) { return min(min(a, b), c); }
DI u32 vmax(u32 m, v4u v) { return umax3(umax3(m, v.x, v.y), v.z, v.w); }
DI v4u ld128(rsrc_t rs, int off) { return __builtin_amdgcn_raw_buffer_load_b128(rs, off, 0, SC1); }
DI v4u ld128s(rsrc_t rs, int off, int soff) { return __builtin_amdgcn_raw_buffer_load_b128(rs, off, soff, SC1); }
DI void st128(rsrc_t rs, int off, v4u v) { __builtin_amdgcn_raw_buffer_store_b128(v, rs, off, 0, SC1); }
DI void st128s(rsrc_t rs, int off, int soff, v4u v) { __builtin_amdgcn_raw_buffer_store_b128(v, rs, off, soff, SC1); }
DI void st64(rsrc_t rs, int off, v2u v) { __builtin_amdgcn_raw_buffer_store_b64(v, rs, off, 0, SC1); }
DI void reld(v4u& v, rsrc_t rs, u32 voff, u32 soff) {
  asm volatile("s_nop 4\n\tbuffer_load_dwordx4 %0, %1, %2, %3 offen sc1" : "+v"(v) : "v"(voff), "s"(rs), "s"(soff) : "memory");
}
#define RARE(c) __builtin_expect(!!(c), 0)
#define DRAIN() asm volatile("s_waitcnt vmcnt(0)" ::: "memory")
#define SB() __builtin_amdgcn_sched_barrier(0)
#define CFENCE() do { asm volatile("" ::: "memory"); SB(); } while (0)
template <int C> DI u32 dppu(u32 v) { return (u32)__builtin_amdgcn_update_dpp(0, (int)v, C, 0xF, 0xF, true); }
template <int C> DI float dppadd(float v) { return v + __uint_as_float(dppu<C>(__float_as_uint(v))); }
DI float sw16sum(float a, float b) { auto r = __builtin_amdgcn_permlane16_swap(__float_as_uint(a), __float_as_uint(b), false, false); return __uint_as_float(r[0]) + __uint_as_float(r[1]); }
DI float sw32sum(float a, float b) { auto r = __builtin_amdgcn_permlane32_swap(__float_as_uint(a), __float_as_uint(b), false, false); return __uint_as_float(r[0]) + __uint_as_float(r[1]); }
DI float quadsum(float v) { v = dppadd<0xB1>(v); return dppadd<0x4E>(v); }
DI float rowsum(float v) { v = quadsum(v); v = dppadd<0x141>(v); return dppadd<0x140>(v); }
DI f4 mfma_bf(v4u a, v4u b, f4 c) { return __builtin_amdgcn_mfma_f32_16x16x32_bf16(__builtin_bit_cast(bf8, a), __builtin_bit_cast(bf8, b), c, 0, 0, 0); }
DI f4 mfma_h(v4u a, v4u b, f4 c) { return __builtin_amdgcn_mfma_f32_16x16x32_f16(__builtin_bit_cast(h8, a), __builtin_bit_cast(h8, b), c, 0, 0, 0); }
DI v2u trread(lptr p) { return __builtin_bit_cast(v2u, __builtin_amdgcn_ds_read_tr16_b64_v4i16((LAS s4*)p)); }
template <int N> struct IC { static constexpr int v = N; };
template <int I, int N, class F> DI void sfor(F f) { if constexpr (I < N) { f(IC<I>{}); sfor<I + 1, N>(f); } }
#define FORU(i, n) _Pragma("unroll") for (int i = 0; i < (n); ++i)
DI f2 cm1(f2 a, f2 b) { f2 t; asm("v_pk_mul_f32 %0, %1, %2 op_sel:[0,0] op_sel_hi:[0,1]" : "=v"(t) : "v"(a), "v"(b)); return t; }
DI f2 cm2(f2 a, f2 b, f2 t) { f2 r; asm("v_pk_fma_f32 %0, %1, %2, %3 op_sel:[1,1,0] op_sel_hi:[1,0,1] neg_lo:[0,1,0]" : "=v"(r) : "v"(a), "v"(b), "v"(t)); return r; }
DI f2 cexp_p(float al2, float rev, float p) {
  float m = __builtin_amdgcn_exp2f(al2 * p), f = __builtin_amdgcn_fractf(rev * p);
  return f2{m * __builtin_amdgcn_cosf(f), m * __builtin_amdgcn_sinf(f)};
}
template <int N, class F> DI void gelu_n(f2 (&v)[N], F fill) {
  const float c0 = -2.0f * 0.7978845608028654f * 1.4426950408889634f, c1 = c0 * 0.044715f;
  f2 a[N];
  sfor<0, 2>([&](auto z) { FORU(i, 6) a[6 * z.v + i] = v[6 * z.v + i] * v[6 * z.v + i]; fill(IC<4 + z.v>{}); });
  sfor<0, 2>([&](auto z) { FORU(i, 6) a[6 * z.v + i] = __builtin_elementwise_fma(a[6 * z.v + i], f2{c1, c1}, f2{c0, c0}); fill(IC<6 + z.v>{}); });
  sfor<0, 2>([&](auto z) { FORU(i, 6) a[6 * z.v + i] *= v[6 * z.v + i]; fill(IC<8 + z.v>{}); });
  sfor<0, 6>([&](auto z) { FORU(i, 2) a[2 * z.v + i] = f2{__builtin_amdgcn_exp2f(a[2 * z.v + i].x), __builtin_amdgcn_exp2f(a[2 * z.v + i].y)}; fill(IC<10 + z.v>{}); });
  sfor<0, 2>([&](auto z) { FORU(i, 6) a[6 * z.v + i] += 1.0f; fill(IC<16 + z.v>{}); });
  sfor<0, 6>([&](auto z) { FORU(i, 2) a[2 * z.v + i] = f2{__builtin_amdgcn_rcpf(a[2 * z.v + i].x), __builtin_amdgcn_rcpf(a[2 * z.v + i].y)}; fill(IC<18 + z.v>{}); });
  sfor<0, 2>([&](auto z) { FORU(i, 6) v[6 * z.v + i] *= a[6 * z.v + i]; fill(IC<24 + z.v>{}); });
}
DI float ln_den(float var) { return rbf(__builtin_amdgcn_sqrtf(rbf(rbf(var) + 1e-5f))); }
template <class T> DI T gld(const void* base, u32 off) { return *(const GAS T*)((const GAS char*)base + off); }
#define MF "v_mfma_f32_16x16x32_bf16 "
DI void m_av(f4& c, v4u a, v4u b) { asm volatile(MF "%0, %1, %2, %0" : "+v"(c) : "a"(a), "v"(b)); }
DI void m_va(f4& c, v4u a, v4u b) { asm volatile(MF "%0, %1, %2, %0" : "+v"(c) : "v"(a), "a"(b)); }
DI void m_vv(f4& c, v4u a, v4u b) { asm volatile(MF "%0, %1, %2, %0" : "+v"(c) : "v"(a), "v"(b)); }
DI void m_av0(f4& c, v4u a, v4u b) { asm volatile(MF "%0, %1, %2, 0" : "=&v"(c) : "a"(a), "v"(b)); }
DI void m_va0(f4& c, v4u a, v4u b) { asm volatile(MF "%0, %1, %2, 0" : "=&v"(c) : "v"(a), "a"(b)); }
DI void m_vv0(f4& c, v4u a, v4u b) { asm volatile(MF "%0, %1, %2, 0" : "=&v"(c) : "v"(a), "v"(b)); }
DI v4u aget(v4u a) {
  v4u r;
  _Pragma("unroll") for (int e = 0; e < 4; ++e) { u32 t; asm volatile("v_accvgpr_read_b32 %0, %1" : "=v"(t) : "a"(a[e])); r[e] = t; }
  return r;
}
DI u32 ssel(bool c, u32 a, u32 b) { asm("" : "+s"(a)); asm("" : "+s"(b)); return c ? a : b; }
DI u32 smod(u32 v, u32 m) { u32 t = v - m; asm("" : "+s"(t)); return (int)t >= 0 ? t : v; }
DI u32 fresh_lane() { u32 l; asm volatile("v_mbcnt_lo_u32_b32 %0, -1, 0\n\tv_mbcnt_hi_u32_b32 %0, -1, %0" : "=v"(l)); return l; }
template <class T> DI void pin(T& v) { asm volatile("" : "+v"(v)); }
#define MM_HEAD() do { SB(); asm volatile("s_nop 7"); SB(); } while (0)
#define MM_TAIL() do { SB(); asm volatile("s_nop 7"); SB(); } while (0)
DI int toe_addr(int pos) { return 2 * pos + 8 * (pos >> 4); }

#define STAMP(n)
#define STAMP_FLUSH()
#define STAMP_INIT()
#define CNT(n,v)

template <int BAND> struct Band {
  static constexpr int R = 128 - 32 * BAND, NT = R / 16, NS = 15 + NT / 2;
  static constexpr int YO = BAND == 0 ? 0 : BAND == 1 ? 8448 : BAND == 2 ? 14848 : 19200;
  lptr al, ah, bb, yp;
  f4 acc[NT];
  v2u lo[32], hi[32];
  v4u bf[NS];
  DI void load(int s, int i) {
    if (i < 4) {
      const int n = 2 * s + (i >> 1);
      if (n < 32) { if (i & 1) hi[n] = *(LAS v2u*)(ah + 40 * (31 - n)); else lo[n] = *(LAS v2u*)(al + 40 * (31 - n)); }
    } else if (s < NS) bf[s] = *(LAS v4u*)(bb + 80 * (NT / 2 - 1 - s + 15));
  }
  DI void mma(int s, int t) {
    const int n = t - NT + 2 + 2 * s;
    if (s < NS && n >= 0 && n < 32) {
      if (n < 2) m_vv0(acc[t], v4u{lo[n].x, lo[n].y, hi[n].x, hi[n].y}, bf[s]); else m_vv(acc[t], v4u{lo[n].x, lo[n].y, hi[n].x, hi[n].y}, bf[s]);
    }
  }
  DI void flush(int s, int t) {
    const int n = t - NT + 2 + 2 * (s - 1);
    if (n == 30 || n == 31) *(LAS f4*)(yp + 64 * t) = acc[t];
  }
};
template <int B1, int B2, class F> DI void conv2(const lptr (&bp)[8], F side) {
  Band<B1> u; Band<B2> v;
  u.al = bp[0]; u.ah = bp[1]; u.bb = bp[2]; u.yp = bp[3]; v.al = bp[4]; v.ah = bp[5]; v.bb = bp[6]; v.yp = bp[7];
  constexpr int N1 = Band<B1>::NT, NS = Band<B1>::NS;
#pragma unroll
  for (int s = 0; s < 2; ++s)
#pragma unroll
    for (int i = 0; i < 5; ++i) { u.load(s, i); v.load(s, i); SB(); }
  sfor<0, 10 * (NS + 1)>([&](auto z) {
    constexpr int s = z.v / 10, i = z.v % 10;
    if (s >= 17 && i == 0) asm volatile("s_nop %0" ::"i"(s == NS ? 7 : s == NS - 1 ? 5 : 2));
    if (i < N1) { u.mma(s, i); u.flush(s, i); } else { v.mma(s, i - N1); v.flush(s, i - N1); }
    if (i < 5) u.load(s + 2, i); else v.load(s + 2, i - 5);
    side(IC<s>{}, IC<i>{});
    SB();
  });
}

__global__ void __launch_bounds__(256, 1)
fused_kernel(const u16* __restrict__ x, const u16* __restrict__ lar, const u16* __restrict__ aim, const u16* __restrict__ cre,
             const u16* __restrict__ cim, const u16* __restrict__ ldt, const u16* __restrict__ dsk, const u16* __restrict__ win,
             const u16* __restrict__ wsp, const u16* __restrict__ bsp, const u16* __restrict__ wout, const u16* __restrict__ cvec,
             const u16* __restrict__ wmod, u16* __restrict__ out, unsigned char* ws) {
  extern __shared__ __attribute__((aligned(16))) unsigned char smem[];
  lptr L = (lptr)smem;
  const u32 b = blockIdx.x, tid = threadIdx.x, lane = tid & 63;
  STAMP_INIT();
  STAMP(0);
  const u32 w = __builtin_amdgcn_readfirstlane(tid >> 6);
  const u32 xc = b & 7, rr = b >> 3, cp = xc * 32 + rr;
  const u32 c16 = lane & 15, q4 = lane >> 4;
  u32 pf0 = 0, pf1 = 0;
  {
    u64 pc;
    u32 len;
    asm volatile("s_getpc_b64 %0\nfk_text_a:\n\ts_mov_b32 %1, fk_text_b-fk_text_a" : "=s"(pc), "=s"(len));
    const u32 o = (rr * 16 + tid) * 128;
    if (tid < 16 && o < len) pf0 = gld<u32>((const void*)pc, o);
  }
  CFENCE();
  const u32 tag = *(const CAS u32*)(ws + WS_E) + 1u;
  const u32 tb = 512 * (b >> 6) + 32 * (b & 15) + 2 * ((b >> 4) & 3);
  const v4u sr0 = gld<v4u>(x, (tb + 8 * w) * 1024 + lane * 16), sr1 = gld<v4u>(x, (tb + 8 * w) * 1024 + 1024 + lane * 16);
  const u32 xgo = (tb + 8 * (lane & 3)) * 1024 + (16 * w + (lane >> 2)) * 16;
  const v4u xg0 = gld<v4u>(x, xgo), xg1 = gld<v4u>(x, xgo + 1024);
  const u32 mG = b % 48, mP = min(b / 48, 3u), mrow = mP * 128 + (lane >> 1);
  v4u wm[4];
  u32 cv[4];
#pragma unroll
  for (int n = 0; n < 4; ++n) wm[n] = gld<v4u>(wmod, (mrow + 32 * n) * 6144 + mG * 128 + w * 32 + (lane & 1) * 16);
#pragma unroll
  for (int n = 0; n < 4; ++n) cv[n] = gld<u16>(cvec, (mrow + 32 * n) * 2);
  const u32 pidx = (cp * 256 + tid) * 2;
  const u32 p_lar = gld<u16>(lar, pidx), p_aim = gld<u16>(aim, pidx), p_cr = gld<u16>(cre, pidx), p_ci = gld<u16>(cim, pidx);
  const u32 p_cr2 = gld<u16>(cre, pidx ^ 128), p_ci2 = gld<u16>(cim, pidx ^ 128);
  const u32 p_ldt = *(const CAS u32*)(ldt + 2 * cp), p_dsk = *(const CAS u32*)(dsk + 2 * cp);
  CFENCE();
  const u32 pi = (xc >> 1) * 8 + (rr & 7), pj = (xc & 1) * 4 + (rr >> 3);
  const u32 nb = pj < 3 ? 6 : 5, blk0 = 5 * pj + min(pj, 3u);
  const bool two = w + 4 < nb;
  const rsrc_t rs = __builtin_amdgcn_make_buffer_rsrc((void*)ws, 0, WS_END, 0x00020000);
  const v4u SENT = {~0u, ~0u, ~0u, ~0u};
  const int x1w = WS_X1 + ((tid >> 3) * 512 + 2 * cp) * 128 + (tid & 7) * 16;
  const u32 wio_src = ((32 * (pi >> 1) + (lane >> 1)) * 1376 + (pi & 1) * 688 + 16 * (blk0 + w)) * 2 + (lane & 1) * 16;
  const v4u wi0 = gld<v4u>(win, wio_src), wi1 = gld<v4u>(win, two ? wio_src + 128 : wio_src);
  const rsrc_t ro = __builtin_amdgcn_make_buffer_rsrc((void*)wout, 0, 688 * 1024, 0x00020000);
  const v4u wo0 = __builtin_amdgcn_raw_buffer_load_b128(ro, (lane >> 1) * 1024 + (lane & 1) * 16, b < 176 ? (b % 22) * 32768 + (b / 22) * 128 + w * 32 : OOB, 0);
  CFENCE();
  if (tid < 40) *(LAS v4u*)(L + L_TOE + (tid / 5) * TOE_C + 5120 + (tid % 5) * 16) = v4u{0, 0, 0, 0};
  if (tid < 150) *(LAS v4u*)(L + L_U + (tid / 75) * U_CH + (tid % 75) * 16) = v4u{0, 0, 0, 0};
  lptr ka[5], pa[3], t0, t1, up, bp[8], yp[4];
  int mo, mo2, wio, bwo;
  {
    const u32 hi = w & 1, ch = w >> 1;
    lptr tb_ = L + L_TAB + ch * TAB_CH + 16 * q4;
    const u32 i2 = (c16 + ((c16 >> 1) & 1)) >> 1, i4 = (c16 + 1 + ((c16 >> 2) & 1)) >> 2, i8 = (c16 + 3 + ((c16 >> 3) & 1)) >> 3;
    const u32 row[5] = {hi ? 35 + i8 : c16, hi ? 38 + i8 : 16 + i2, hi ? 41 + i8 : 25 + i4, hi ? 44 + i8 : 30 + i4, 47 + c16};
    FORU(i, 5) { ka[i] = tb_ + row[i] * TABP; pin(ka[i]); }
    FORU(a, 3) { pa[a] = L + (a == 0 ? L_PKI + w * 1024 : a == 1 ? L_PKI + 4096 + hi * 1024 : L_PKW + w * 8192) + (8 * q4 + (c16 >> 2)) * 32 + 8 * (c16 & 3); pin(pa[a]); }
    const int i0 = 2044 - (1024 * hi + 768 + 16 * c16 + 4 * q4);
    t0 = L + L_TOE + ch * TOE_CH + toe_addr(i0); t1 = L + L_TOE + ch * TOE_CH + toe_addr(i0 + 4);
    up = L + L_U + 80 * (15 + (tid >> 2)) + 16 * (tid & 3);
    pin(t0); pin(t1); pin(up);
    FORU(n, 2) {
      const u32 B = n ? 3 - hi : hi, R = 128 - 32 * B, c = (3 - c16) & 3, P = 2048 - 512 * B - 496 - 4 * (c16 >> 2) + 8 * q4;
      bp[4 * n] = L + L_TOE + ch * TOE_CH + c * TOE_C + toe_addr(P);
      bp[4 * n + 1] = L + L_TOE + ch * TOE_CH + c * TOE_C + toe_addr(P + 4);
      bp[4 * n + 2] = L + L_U + ch * U_CH + 80 * (4 - B) * c16 + 16 * q4;
      bp[4 * n + 3] = L + L_Y + ch * Y_CH + (n ? (hi ? 14848 : 19200) : (hi ? 8448 : 0)) + c16 * (4 * R + 16) + 16 * q4;
    }
    FORU(n, 8) pin(bp[n]);
    FORU(B, 4) {
      const u32 tp = 8 * tid - 512 * B;
      yp[B] = L + L_Y + (B == 0 ? 0 : B == 1 ? 8448 : B == 2 ? 14848 : 19200) + ((tp & 2047) / (128 - 32 * B)) * 16 + tp * 4;
      pin(yp[B]);
    }
    mo = WS_MOD + ((lane & 3) * 1536 + 16 * (pi >> 1) + (lane >> 2)) * 16;
    mo2 = WS_MOD + ((lane & 3) * 1536 + 256 * min(lane >> 2, 2u) + cp) * 16;
    wio = WS_WINP + (((pj * 2 + (pi & 1)) * 6 + w) * 16 + (pi >> 1)) * 1024 + lane * 16;
    bwo = WS_BWP + (((pj * 2 + (pi & 1)) * 6 + w) * 16 + (pi >> 1)) * 128 + (lane & 31) * 4;
    bwo = lane < 32 ? bwo : two ? bwo + 8192 : OOB;
    pin(mo); pin(mo2); pin(wio); pin(bwo);
  }
  STAMP(23);
  {
    f2 a[8];
#pragma unroll
    for (int e = 0; e < 4; ++e) { a[2 * e] = f2{bflo(sr0[e]), bflo(sr1[e])}; a[2 * e + 1] = f2{bfhi(sr0[e]), bfhi(sr1[e])}; }
    f2 s = ((a[0] + a[1]) + (a[2] + a[3])) + ((a[4] + a[5]) + (a[6] + a[7]));
    float t = rowsum(sw32sum(s.x, s.y));
    t = sw16sum(t, t) * (1.f / 512);
    auto mm = __builtin_amdgcn_permlane32_swap(__float_as_uint(t), __float_as_uint(t), false, false);
    const f2 m = {__uint_as_float(mm[0]), __uint_as_float(mm[1])};
    s = f2{0.f, 0.f};
#pragma unroll
    for (int e = 0; e < 8; ++e) { const f2 u = a[e] - m; s = __builtin_elementwise_fma(u, u, s); }
    float v = rowsum(sw32sum(s.x, s.y));
    v = sw16sum(v, v) * (1.f / 512);
    st64(rs, (lane & 31) == 0 ? WS_STAT1 + b * 64 + w * 16 + (lane >> 5) * 8 : OOB, v2u{pkbf(t, ln_den(v)), tag});
  }
  {
    const int o = WS_XG + (4 * (16 * w + (lane >> 2))) * 16384 + b * 64 + (lane & 3) * 16;
    st128(rs, o, v4u{xg0.x, tag, xg1.x, tag});
    st128s(rs, o, 16384, v4u{xg0.y, tag, xg1.y, tag});
    st128s(rs, o, 32768, v4u{xg0.z, tag, xg1.z, tag});
    st128s(rs, o, 49152, v4u{xg0.w, tag, xg1.w, tag});
  }
  CFENCE();
  STAMP(22);
  {
    f2 a[4];
#pragma unroll
    for (int n = 0; n < 4; ++n) {
      const float c = bflo(cv[n]);
      const float s = rbf(c * __builtin_amdgcn_rcpf(1.f + __builtin_amdgcn_exp2f(-1.4426950408889634f * c)));
#pragma unroll
      for (int e = 0; e < 4; ++e) a[e] = n ? a[e] + bf2f(wm[n][e]) * s : bf2f(wm[n][e]) * s;
    }
    const float u0 = sw32sum(a[0].x, a[2].x), u1 = sw32sum(a[0].y, a[2].y), u2 = sw32sum(a[1].x, a[3].x), u3 = sw32sum(a[1].y, a[3].y);
    float t0 = sw16sum(u0, u2), t1 = sw16sum(u1, u3);
    t0 = dppadd<0x4E>(dppadd<0x124>(dppadd<0x128>(t0)));
    t1 = dppadd<0x4E>(dppadd<0x124>(dppadd<0x128>(t1)));
    st128(rs, (c16 < 2 && b < 192) ? WS_MOD + (mP * 1536 + mG * 32 + w * 8 + c16 * 4 + q4) * 16 : OOB, v4u{__float_as_uint(t0), tag, __float_as_uint(t1), tag});
  }
  CFENCE();
  STAMP(1);
  const u32 xw = 2 * (w & 1) + (w >> 1);
  const u32 pfo = (64 * pi + 8 * pj + 2 * w + ((lane >> 4) & 1)) * 4096 + ((lane >> 2) & 3) * 1024 + (lane & 3) * 128;
  u32 pf2 = 0, pf3;
  if (lane < 32) pf2 = gld<u32>(wsp, pfo);
  st128(rs, x1w, SENT);
  st128(rs, x1w + 128, SENT);
  CFENCE();
  const rsrc_t rw = __builtin_amdgcn_make_buffer_rsrc((void*)wsp, 0, 2048 * 4096, 0x00020000);
  const u32 wvo = (lane >> 3) * 4096 + (lane & 7) * 16;
  const u32 wsb[4] = {(64 * pi + 16 * xw) * 4096 + w * 1024, (64 * pi + 16 * (xw ^ 1)) * 4096 + w * 1024,
                      (64 * pi + 16 * (xw ^ 2)) * 4096 + w * 1024, (64 * pi + 16 * (xw ^ 3)) * 4096 + w * 1024};
  v4u wraw[64], wsf[16][4];
  auto rq = [&](int g) { return (g + pj) & 3; };
  auto rk = [&](int g) { return g ^ (pj & 1); };
  auto ldw = [&](int n) { wraw[n] = __builtin_amdgcn_raw_buffer_load_b128(rw, (wvo + rk(n >> 4) * 256) + ((n >> 3) & 1) * 128, wsb[(n & 7) >> 1] + (n & 1) * 32768, 0); };
  const u32 wwa = (u32)(size_t)L + L_SHF + w * 8192 + (lane >> 3) * 128 + ((lane & 7) ^ (lane >> 3)) * 16;
  auto shuf = [&](auto Z) {
    constexpr int c = Z.v >> 4, j = Z.v & 15, i = j & 7;
    if constexpr (j < 8) asm volatile("ds_write_b128 %0, %1 offset:%2" ::"v"(wwa), "a"(wraw[8 * c + i]), "i"(i * 1024) : "memory");
    else wsf[2 * c + (i & 1)][i >> 1] = *(LAS v4u*)(L + L_SHF + w * 8192 + (i >> 1) * 2048 + c16 * 128 + (((4 * (i & 1) + q4) ^ (c16 & 7)) * 16));
  };
  float kscale;
  {
    const float L2E = 1.4426950408889634f;
    const float dt = rbf(__builtin_amdgcn_exp2f(((w >> 1) ? bfhi(p_ldt) : bflo(p_ldt)) * L2E));
    const float al2 = (-rbf(__builtin_amdgcn_exp2f(bflo(p_lar) * L2E)) * dt) * L2E;
    const float rev = (bflo(p_aim) * dt) * 0.15915494309189535f;
    u32 mx = max(max(p_cr & 0x7fffu, p_ci & 0x7fffu), max(p_cr2 & 0x7fffu, p_ci2 & 0x7fffu));
    mx = max(mx, dppu<0xB1>(mx)); mx = max(mx, dppu<0x4E>(mx)); mx = max(mx, dppu<0x141>(mx)); mx = max(mx, dppu<0x140>(mx));
    { auto r = __builtin_amdgcn_permlane16_swap(mx, mx, false, false); mx = max(r[0], r[1]); }
    { auto r = __builtin_amdgcn_permlane32_swap(mx, mx, false, false); mx = max(r[0], r[1]); }
    u32 ex = mx >> 7;
    ex = min(max(ex, 1u), 253u);
    const float dn = __uint_as_float((254u - ex) << 23), up = __uint_as_float(ex << 23);
    kscale = 2.0f * dt * up;
    LAS u32* tp = (LAS u32*)(L + L_TAB + (tid >> 7) * TAB_CH + (tid & 127) * 4);
    f2 U[17], Y[16], V[8], X[31], wp[4], t[31];
    u32 pk[31];
    wp[0] = cexp_p(al2, rev, 1.f);
    Y[1] = cexp_p(al2, -rev, 16.f);
    V[1] = cexp_p(al2, rev, 256.f);
    Y[0] = f2{1.f, 0.f};
    U[0] = f2{bflo(p_cr) * dn, bflo(p_ci) * dn};
    SB();
    FORU(l, 4) {
      const int h = 1 << l;
      FORU(i, h) t[i] = cm1(U[i], wp[l]);
      if (l < 3) { t[8] = cm1(wp[l], wp[l]); t[9] = cm1(Y[h], Y[h]); }
      if (l < 2) t[10] = cm1(V[h], V[h]);
      FORU(i, h - 1) t[11 + i] = cm1(Y[1 + i], Y[h]);
      if (l < 3) { FORU(i, h - 1) t[18 + i] = cm1(V[1 + i], V[h]); }
      SB();
      FORU(i, h) U[h + i] = cm2(U[i], wp[l], t[i]);
      if (l < 3) { wp[l + 1] = cm2(wp[l], wp[l], t[8]); Y[2 * h] = cm2(Y[h], Y[h], t[9]); }
      if (l < 2) V[2 * h] = cm2(V[h], V[h], t[10]);
      FORU(i, h - 1) Y[h + 1 + i] = cm2(Y[1 + i], Y[h], t[11 + i]);
      if (l < 3) { FORU(i, h - 1) V[h + 1 + i] = cm2(V[1 + i], V[h], t[18 + i]); }
      SB();
    }
    auto xa = [](int i) { return i < 9 ? 1 : i < 19 ? 2 + (i - 9) / 5 : 4 + (i - 19) / 3; };
    auto xo = [](int i) { return i < 9 ? 2 * i : i < 19 ? 4 * ((i - 9) % 5) : 8 * ((i - 19) % 3); };
    u32 py[16], pu[16];
    t[0] = cm1(U[8], wp[3]);
    FORU(i, 16) { py[i] = pkh(Y[i]); if (i == 2) U[16] = cm2(U[8], wp[3], t[0]); if (i & 1) tp[(47 + (i >> 1)) * (TABP / 4)] = py[i >> 1]; SB(); }
    FORU(i, 16) { pu[i] = pkh(U[i]); if (i & 1) tp[(55 + (i >> 1)) * (TABP / 4)] = py[8 + (i >> 1)]; SB(); }
    FORU(i, 31) { t[i] = cm1(U[xo(i)], V[xa(i)]); if (!(i & 1)) tp[(i >> 1) * (TABP / 4)] = pu[i >> 1]; SB(); }
    FORU(i, 35) {
      if (i < 31) X[i] = cm2(U[xo(i)], V[xa(i)], t[i]);
      if (i >= 2 && i < 33) pk[i - 2] = pkh(X[i - 2]);
      if (i >= 4) tp[(12 + i) * (TABP / 4)] = pk[i - 4];
      SB();
    }
  }
  *(LAS v4u*)(L + L_PKI + w * 1024 + lane * 16) = wi0;
  if (w < 2) *(LAS v4u*)(L + L_PKI + 4096 + w * 1024 + lane * 16) = wi1;
  *(LAS v4u*)(L + L_PKW + w * 8192 + lane * 16) = wo0;
  v4u gx[4], gs[4], gm[3];
  auto s1_ld = [&](int n) {
    if (n < 4) gx[n] = ld128s(rs, lane * 16 + n * 1024, WS_XG + cp * 16384 + w * 4096);
    else if (n < 8) gs[n - 4] = ld128s(rs, lane * 16 + (n - 4) * 1024, WS_STAT1 + w * 4096);
    else gm[n - 8] = n == 8 ? ld128(rs, mo2) : ld128s(rs, mo, (n == 9 ? 768 : 1024) * 16);
  };
  FORU(n, 3) s1_ld(n);
  __syncthreads();
  STAMP(2);
  f4 kacc[4];
  v2u pl[3], ph[3];
  {
    v4u yf[8], xf[8][4];
    auto ldk = [&](int k, int i) {
      if (k < 8) { if (i < 4) xf[k][i] = *(LAS v4u*)(ka[i] + 64 * k); else yf[k] = *(LAS v4u*)(ka[4] + 64 * k); }
    };
    FORU(k, 2) FORU(i, 5) ldk(k, 4 - i);
    SB();
    FORU(k, 8) FORU(a, 4) {
      if (k) asm volatile("v_mfma_f32_16x16x32_f16 %0, %1, %2, %0" : "+v"(kacc[3 - a]) : "v"(xf[k][3 - a]), "v"(yf[k]));
      else asm volatile("v_mfma_f32_16x16x32_f16 %0, %1, %2, 0" : "=&v"(kacc[3 - a]) : "v"(xf[k][3 - a]), "v"(yf[k]));
      if (a == 0) ldk(k + 2, 4);
      ldk(k + 2, 3 - a);
      if (k < 4 && (a & 1)) s1_ld(3 + 2 * k + (a >> 1));
      if (k >= 6 && a < 3) {
        if (k == 6) pl[a] = trread(pa[a]); else ph[a] = trread(pa[a] + 128);
      }
      SB();
    }
  }
  MM_TAIL();
  f2 pv[8];
  {
    u32 P0[4], P1[4], M[4];
    FORU(a, 4) kacc[a] *= kscale;
    SB();
    FORU(a, 4) { P0[a] = pkbf_a(kacc[a].w, kacc[a].z); P1[a] = pkbf_a(kacc[a].y, kacc[a].x); M[a] = pkbf_a(kacc[a].z, kacc[a].y); }
    SB();
    FORU(a, 4) {
      lptr p0 = t0 + 640 * (3 - a), p1 = t1 + 640 * (3 - a);
      *(LAS v2u*)(p1) = v2u{P0[a], P1[a]};
      *(LAS u16*)(p0 + TOE_C + 6) = (u16)P0[a]; *(LAS u32*)(p1 + TOE_C) = M[a]; *(LAS u16*)(p1 + TOE_C + 4) = (u16)(P1[a] >> 16);
      *(LAS u32*)(p0 + 2 * TOE_C + 4) = P0[a]; *(LAS u32*)(p1 + 2 * TOE_C) = P1[a];
      *(LAS u16*)(p0 + 3 * TOE_C + 2) = (u16)P0[a]; *(LAS u32*)(p0 + 3 * TOE_C + 4) = M[a]; *(LAS u16*)(p1 + 3 * TOE_C) = (u16)(P1[a] >> 16);
      FORU(j, 2) { pv[2 * a + j] = bf2f((a & 1) ? ph[a >> 1][j] : pl[a >> 1][j]); pin(pv[2 * a + j]); }
      SB();
    }
    st128s(rs, lane * 16, b < 176 ? WS_WOP + (b * 4 + w) * 1024 : OOB, v4u{pl[2].x, pl[2].y, ph[2].x, ph[2].y});
  }
  asm volatile("" ::"v"(pf0), "v"(pf2));
  STAMP(3);
  auto s1_bad = [&]() {
    u32 mx = umax3(gm[0].y, gm[0].w, tag), mn = umin3(gm[0].y, gm[0].w, tag);
    FORU(k, 2) { mx = umax3(mx, gm[1 + k].y, gm[1 + k].w); mn = umin3(mn, gm[1 + k].y, gm[1 + k].w); }
    FORU(k, 4) {
      mx = umax3(umax3(mx, gx[k].y, gx[k].w), gs[k].y, gs[k].w);
      mn = umin3(umin3(mn, gx[k].y, gx[k].w), gs[k].y, gs[k].w);
    }
    return __any(mx != mn);
  };
  if (RARE(s1_bad())) {
#pragma nounroll
    for (int sp = 0; sp < SPIN_CAP; ++sp) {
      FORU(n, 4) { reld(gx[n], rs, lane * 16, WS_XG + cp * 16384 + w * 4096 + n * 1024); reld(gs[n], rs, lane * 16, WS_STAT1 + w * 4096 + n * 1024); }
      reld(gm[0], rs, mo2, 0); reld(gm[1], rs, mo, 768 * 16); reld(gm[2], rs, mo, 1024 * 16);
      asm volatile("s_waitcnt vmcnt(0)" : "+v"(gx[0]), "+v"(gx[1]), "+v"(gx[2]), "+v"(gx[3]), "+v"(gs[0]), "+v"(gs[1]), "+v"(gs[2]), "+v"(gs[3]), "+v"(gm[0]), "+v"(gm[1]), "+v"(gm[2])::"memory");
      CNT(29, 1);
      if (!s1_bad()) break;
    }
  }
  STAMP(4);
  u32 xv[8];
  u32 h0[4], h1[4];
  u32 gav;
  {
    float qs[6];
    f2 m[4], r[4], a[4], c[4];
    int wn = 0;
    auto fill = [&]() { SB(); ldw(wn); ++wn; SB(); };
    FORU(i, 6) qs[i] = dppadd<0xB1>(__uint_as_float((i & 1) ? gm[i >> 1].z : gm[i >> 1].x));
    FORU(k, 4) { xv[2 * k] = gx[k].x; xv[2 * k + 1] = gx[k].z; r[k] = f2{bfhi(gs[k].x), bfhi(gs[k].z)}; }
    fill();
    FORU(i, 6) qs[i] = dppadd<0x4E>(qs[i]);
    FORU(k, 4) m[k] = f2{bflo(gs[k].x), bflo(gs[k].z)};
    fill();
    const u32 mv = pkbf(qs[0], qs[1]), sbp = pkbf(qs[2], qs[3]), cbp = pkbf(qs[4], qs[5]);
    FORU(k, 4) r[k] = f2{__builtin_amdgcn_rcpf(r[k].x), __builtin_amdgcn_rcpf(r[k].y)};
    fill();
    const u32 sav = __builtin_amdgcn_readlane(mv, 0), cav = __builtin_amdgcn_readlane(mv, 4);
    gav = __builtin_amdgcn_readlane(mv, 8);
    const u32 scp = pkbf(bf2f(cbp) + 1.0f);
    FORU(k, 4) { a[k] = f2{bflo(gx[k].x), bflo(gx[k].z)}; c[k] = f2{bfhi(gx[k].x), bfhi(gx[k].z)}; }
    fill();
    const u32 s1v = pkbf(bf2f(cav) + 1.0f);
    FORU(k, 4) { a[k] -= m[k]; c[k] -= m[k]; }
    const u32 scb[4] = {dppu<0x150>(scp), dppu<0x154>(scp), dppu<0x158>(scp), dppu<0x15C>(scp)};
    fill();
    FORU(k, 4) { a[k] *= r[k]; c[k] *= r[k]; }
    f2 sc[4], sb[4], t[8], bw[2], sw[2];
    FORU(j, 4) sc[j] = bf2f(scb[j]);
    fill();
    FORU(k, 4) { a[k] = a[k] * bflo(s1v) + bflo(sav); c[k] = c[k] * bfhi(s1v) + bfhi(sav); }
    FORU(j, 8) t[j] = pv[j] * sc[j & 3];
    fill();
    v4u wpk[2];
    FORU(k, 4) { h0[k] = pkbf(a[k]); h1[k] = pkbf(c[k]); }
    FORU(j, 8) wpk[j >> 2][j & 3] = pkbf(t[j]);
    fill();
    *(LAS v4u*)up = v4u{h0[0], h0[1], h0[2], h0[3]};
    *(LAS v4u*)(up + U_CH) = v4u{h1[0], h1[1], h1[2], h1[3]};
    st128(rs, wio, wpk[0]);
    st128s(rs, wio, two ? 65536 : OOB, wpk[1]);
    const u32 sbb[4] = {dppu<0x150>(sbp), dppu<0x154>(sbp), dppu<0x158>(sbp), dppu<0x15C>(sbp)};
    FORU(j, 8) t[j] = bf2f(wpk[j >> 2][j & 3]);
    SB();
    FORU(j, 4) sb[j] = bf2f(sbb[j]);
    FORU(f, 2) sw[f] = (t[4 * f] + t[4 * f + 1]) + (t[4 * f + 2] + t[4 * f + 3]);
    SB();
    FORU(j, 8) pv[j] *= sb[j & 3];
    SB();
    FORU(f, 2) bw[f] = (pv[4 * f] + pv[4 * f + 1]) + (pv[4 * f + 2] + pv[4 * f + 3]);
    const float e0 = sw16sum(bw[0].x + bw[0].y, sw[0].x + sw[0].y), e1 = sw16sum(bw[1].x + bw[1].y, sw[1].x + sw[1].y);
    __builtin_amdgcn_raw_buffer_store_b32(__float_as_uint(sw32sum(e0, e1)), rs, bwo, 0, SC1);
  }
  __syncthreads();
  STAMP(5);
  v4u cdv;
  const u32 fpo = lane < 48 ? WS_WINP + pj * 196608 + (((pi & 7) * 4 + w) * 48 + lane) * 128 : OOB;
  auto side = [&](auto S, auto I) {
    constexpr int s = S.v, i = I.v, z = 10 * s + i;
    if constexpr (z % 4 == 1 && z / 4 < 24) ldw(8 + z / 4);
    if (z == 99) pf3 = __builtin_amdgcn_raw_buffer_load_b32(rw, lane < 32 ? pfo : OOB, 512, 0);
    if (z == 43) {
      asm volatile("s_waitcnt vmcnt(11)" ::: "memory");
      __builtin_amdgcn_raw_buffer_store_b32(tag, rs, lane == 0 ? WS_CD + b * 64 + w * 4 : OOB, 0, SC1);
    }
    if (z == 123) pf1 = __builtin_amdgcn_raw_buffer_load_b32(rs, fpo, 0, SC1);
    if (z == 163) cdv = ld128(rs, WS_CD + tid * 64);
  };
  if (w & 1) conv2<1, 2>(bp, side);
  else conv2<0, 3>(bp, side);
  STAMP(21);
  {
    f2 y[2][4], t[2][4];
    const float ga0 = bflo(gav), ga1 = bfhi(gav);
    FORU(k, 4) { t[0][k] = bf2f(h0[k]); t[1][k] = bf2f(h1[k]); }
    FORU(k, 4) { y[0][k] = f2{bflo(xv[2 * k]), bflo(xv[2 * k + 1])}; y[1][k] = f2{bfhi(xv[2 * k]), bfhi(xv[2 * k + 1])}; }
    SB();
    FORU(k, 4) { t[0][k] *= bflo(p_dsk); t[1][k] *= bfhi(p_dsk); pin(y[0][k]); pin(y[1][k]); pin(t[0][k]); pin(t[1][k]); }
    __syncthreads();
    STAMP(6);
    f4 q[4][4];
    auto yld = [&](int B) { FORU(e, 4) q[B][e] = *(LAS f4*)(yp[B] + (e >> 1) * Y_CH + (e & 1) * 16); SB(); };
    auto yadd = [&](int B) { FORU(e, 4) { t[e >> 1][2 * (e & 1)] += f2{q[B][e].x, q[B][e].y}; t[e >> 1][2 * (e & 1) + 1] += f2{q[B][e].z, q[B][e].w}; } SB(); };
    yld(0);
    if (w >= 2) {
      yld(1); yld(2);
      if (w == 3) { yld(3); yadd(3); }
      yadd(1); yadd(2);
    } else if (w == 1) { yld(1); yadd(1); }
    yadd(0);
    FORU(k, 4) { y[0][k] += t[0][k] * ga0; y[1][k] += t[1][k] * ga1; }
    SB();
    v4u o0, o1;
    FORU(k, 4) { o0[k] = pkbf(y[0][k]); o1[k] = pkbf(y[1][k]); }
    SB();
    st128(rs, x1w, san4(o0));
    st128(rs, x1w + 128, san4(o1));
  }
  CFENCE();
  asm volatile("" ::"v"(pf1), "v"(pf3));
  auto cd_bad = [&]() { return __any(umax3(umax3(cdv.x, cdv.y, cdv.z), cdv.w, tag) != umin3(umin3(cdv.x, cdv.y, cdv.z), cdv.w, tag)); };
  if (RARE(cd_bad())) {
#pragma nounroll
    for (int sp = 0; sp < SPIN_CAP; ++sp) {
      asm volatile("" ::: "memory");
      cdv = ld128(rs, WS_CD + tid * 64);
      if (!cd_bad()) break;
    }
  }
  __syncthreads();
  STAMP(7);
  const u32 blkS = 4 + (w >> 1);
  const bool hasS = blkS < nb;
  const u32 fo[4] = {((pj * 2 + 1) * 6 + w), ((pj * 2 + 1) * 6 + (hasS ? blkS : 4)), (pj * 12 + w), (pj * 12 + (hasS ? blkS : 4))};
  v4u ones = {0x3f803f80u, 0x3f803f80u, 0x3f803f80u, 0x3f803f80u};
  pin(ones);
  v4u wf[4][16];
  auto ldf = [&](int s, int k) { wf[s][k] = ld128s(rs, lane * 16 + (k & 3) * 1024, WS_WINP + fo[s] * 16384 + (k >> 2) * 4096); };
  f4 sW[4];
  f4 bW[4];
  auto ldb = [&](int s) {
    bW[s] = __builtin_bit_cast(f4, ld128s(rs, c16 * 128 + q4 * 16, WS_BWP + fo[s] * 2048));
    sW[s] = __builtin_bit_cast(f4, ld128s(rs, c16 * 128 + q4 * 16, WS_BWP + fo[s] * 2048 + 64));
  };
  auto redb = [&](int s) {
    FORU(e, 16) { f4& v = (e & 4) ? sW[s + (e >> 3)] : bW[s + (e >> 3)]; float t = dppadd<0xB1>(v[e & 3]); pin(t); v[e & 3] = t; }
    SB();
    FORU(e, 16) { f4& v = (e & 4) ? sW[s + (e >> 3)] : bW[s + (e >> 3)]; float t = dppadd<0x4E>(v[e & 3]); pin(t); v[e & 3] = t; }
    SB();
    FORU(e, 16) { f4& v = (e & 4) ? sW[s + (e >> 3)] : bW[s + (e >> 3)]; float t = dppadd<0x141>(v[e & 3]); pin(t); v[e & 3] = t; }
    SB();
    FORU(e, 16) { f4& v = (e & 4) ? sW[s + (e >> 3)] : bW[s + (e >> 3)]; float t = dppadd<0x140>(v[e & 3]); pin(t); v[e & 3] = t; }
    SB();
  };
  {
    ldb(0); ldb(1);
    v4u xr[16];
    const int xo = WS_X1 + pi * 65536 + w * 16384;
    sfor<0, 32>([&](auto z) {
      constexpr int n = z.v;
      if constexpr (n < 16) ldf(n & 1, n >> 1); else xr[n - 16] = ld128s(rs, lane * 16 + (n & 3) * 1024, xo + rq((n - 16) >> 2) * 4096);
      shuf(IC<2 * n>{}); shuf(IC<2 * n + 1>{});
      SB();
    });
    redb(0);
    pin(bW[0]); pin(bW[1]); pin(sW[0]); pin(sW[1]);
    STAMP(8);
    lptr xi = L + L_XIMG + w * 16384 + (lane >> 3) * 128 + (lane & 1) * 16;
    const u32 xs0 = (((lane & 7) >> 1) ^ ((lane >> 4) & 1)) * 32;
    u32 m = 0;
    FORU(k, 16) { *(LAS v4u*)(xi + rq(k >> 2) * 4096 + (k & 3) * 1024 + (xs0 ^ ((k & 1) * 64))) = xr[k]; m = vmax(m, xr[k]); ldf(k & 1, 8 + (k >> 1)); SB(); }
    if (RARE(__any(m == ~0u))) {
#pragma nounroll
      for (int sp = 0; sp < SPIN_CAP; ++sp) {
        asm volatile("" ::: "memory");
        FORU(k, 16) xr[k] = ld128s(rs, lane * 16, xo + rq(k >> 2) * 4096 + (k & 3) * 1024);
        SB();
        m = 0;
        FORU(k, 16) { *(LAS v4u*)(xi + rq(k >> 2) * 4096 + (k & 3) * 1024 + (xs0 ^ ((k & 1) * 64))) = xr[k]; m = vmax(m, xr[k]); SB(); }
        CNT(29, 100);
        if (!__any(m == ~0u)) break;
      }
    }
    STAMP(9);
  }
  __syncthreads();
  STAMP(10);
  const u32 xsw = ((c16 >> 3) & 1) | ((q4 & 1) << 1), xbo = (8 * q4 + (c16 >> 2)) * 128 + 8 * (c16 & 3);
  lptr xb[4];
#pragma unroll
  for (int t = 0; t < 4; ++t) xb[t] = L + xbo + ((t ^ xw ^ xsw) * 32);
  v2u bl[3][4], bh[3][4];
  auto ldx = [&](int k, int i) {
    if (k < 16) { if (i & 1) bh[k % 3][i >> 1] = trread(xb[i >> 1] + 4096 * k + 512); else bl[k % 3][i >> 1] = trread(xb[i >> 1] + 4096 * k); }
  };
  auto bx = [&](int k, int t) { return v4u{bl[k % 3][t].x, bl[k % 3][t].y, bh[k % 3][t].x, bh[k % 3][t].y}; };
  auto xwait = [&](int k) { asm volatile("" ::"v"(bh[k % 3][3])); };
  const int pco = WS_PB + ((pi * 4 + w) * 44 + blk0) * 512, pc2 = 128 + lane < 32 * nb ? lane * 16 : OOB;
  const int zco = WS_Z2N + ((pj * 6 + (w >> 1)) * 64 + 2 * pi + (w & 1)) * 1024, zc2 = w + 8 < 2 * nb ? zco + 262144 : OOB;
  f4 z2a[6];
  f2 rn[4];
  {
    f4 st0, st1;
    v2u sl[16], sh[16];
    FORU(k, 4) { sl[k] = trread(xb[0] + 4096 * k); sh[k] = trread(xb[0] + 4096 * k + 512); }
    MM_HEAD();
    sfor<0, 16>([&](auto z) {
      constexpr int k = z.v;
      const v4u v = {sl[k].x, sl[k].y, sh[k].x, sh[k].y};
      if (k) m_vv(st0, ones, v); else m_vv0(st0, ones, v);
      if constexpr (k + 4 < 16) sl[k + 4] = trread(xb[0] + 4096 * (k + 4)); else { ldx((k - 12) >> 1, 4 * (k & 1)); ldx((k - 12) >> 1, 4 * (k & 1) + 1); }
      SB();
      if (k) m_vv(st1, v, v); else m_vv0(st1, v, v);
      if constexpr (k + 4 < 16) sh[k + 4] = trread(xb[0] + 4096 * (k + 4) + 512); else { ldx((k - 12) >> 1, 4 * (k & 1) + 2); ldx((k - 12) >> 1, 4 * (k & 1) + 3); }
      if (k == 1) st128s(rs, lane * 16, pco, SENT);
      if (k == 5) st128s(rs, lane * 16, pco + 1024, SENT);
      if (k == 9) st128s(rs, pc2, pco + 2048, SENT);
      if (k == 3) st128s(rs, lane * 16, zco, SENT);
      if (k == 7) st128s(rs, lane * 16, zco + 131072, SENT);
      if (k == 11) st128s(rs, lane * 16, zc2, SENT);
      SB();
    });
    float sv0 = 0.f, sv1 = 0.f;
    FORU(k, 16) FORU(i, 6) {
      const int u = i == 0 ? 0 : i == 1 ? 4 : i == 2 ? 1 : i == 3 ? 5 : i - 2;
      if (i == 0) xwait(k);
      if (k) m_vv(z2a[u], wf[u >> 2][k], bx(k, u & 3)); else m_vv0(z2a[u], wf[u >> 2][k], bx(k, u & 3));
      if (i < 2) { ldx(k + 2, 2 * i); ldx(k + 2, 2 * i + 1); } else ldx(k + 2, i + 2);
      if (i == 2 && k) ldf(2 + ((k - 1) & 1), (k - 1) >> 1);
      if (i == 4 && (k & 1)) ldw(32 + (k >> 1));
      if (i == 3 || i == 5) {
        const int z = 2 * k + (i == 5);
        if (z == 4) sv1 = (c16 & 1) ? st1.y : st1.x;
        if (z == 5) sv0 = (c16 & 1) ? st1.w : st1.z;
        if (z == 6) sv1 = (c16 & 2) ? sv0 : sv1;
        if (z == 7) { pin(st0); sv0 = st0.x * (1.f / 512); }
        if (z == 8) sv1 *= (1.f / 512);
        if (z == 9) sv1 = fmaxf(__builtin_fmaf(-sv0, sv0, sv1), 0.f);
        if (z == 10) sv1 = rbf(sv1);
        if (z == 11) sv0 = rbf(sv0);
        if (z == 12) sv1 = rbf(sv1 + 1e-5f);
        if (z == 13) sv1 = __builtin_amdgcn_sqrtf(sv1);
        if (z == 14) sv1 = rbf(sv1);
        if (z == 15) sv1 = __builtin_amdgcn_rcpf(sv1);
        if (z == 16) sv0 *= -sv1;
        if (z == 17) *(volatile LAS f2*)(L + (q4 == (c16 >> 2) ? L_XST + (16 * xw + c16) * 8 : L_XST + 512 + lane * 8)) = f2{sv1, sv0};
        if (z >= 4 && z <= 16) { pin(sv0); pin(sv1); }
        if (z == 24) __builtin_amdgcn_s_barrier();
        if (z >= 26 && z < 30) rn[z - 26] = *(volatile LAS f2*)(L + L_XST + (16 * ((z - 26) ^ xw) + c16) * 8);
      }
      SB();
    }
    MM_TAIL();
  }
  STAMP(24);
  auto shuf2 = [&](auto C, auto N) {
    constexpr int n = N.v % 12, z = 16 * (C.v + N.v / 12);
    if constexpr (N.v < 24) { if constexpr (n < 8) shuf(IC<z + n>{}); else { shuf(IC<z + 2 * n - 8>{}); shuf(IC<z + 2 * n - 7>{}); } }
  };
  auto epi = [&](f4 (&a)[6], int s0, auto fill) {
    f2 v[12];
    sfor<0, 2>([&](auto z) {
      FORU(j, 6) {
        const int i = 6 * z.v + j;
        const f4 &sw = sW[s0 + (i >> 3)], &bw = bW[s0 + (i >> 3)];
        v[i] = ((i & 1) ? f2{sw.z, sw.w} : f2{sw.x, sw.y}) * rn[(i >> 1) & 3].y + ((i & 1) ? f2{bw.z, bw.w} : f2{bw.x, bw.y});
      }
      fill(IC<z.v>{});
    });
    sfor<0, 2>([&](auto z) {
      FORU(j, 6) { const int i = 6 * z.v + j; v[i] = ((i & 1) ? f2{a[i >> 1].z, a[i >> 1].w} : f2{a[i >> 1].x, a[i >> 1].y}) * rn[(i >> 1) & 3].x + v[i]; }
      fill(IC<2 + z.v>{});
    });
    gelu_n<12>(v, fill);
    FORU(i, 6) { a[i] = f4{v[2 * i].x, v[2 * i].y, v[2 * i + 1].x, v[2 * i + 1].y}; pin(a[i]); }
  };
  {
    epi(z2a, 0, [&](auto N) {
      constexpr int n = N.v;
      SB();
      if constexpr ((n & 1) && n < 22) ldf(2 + ((15 + n / 2) & 1), (15 + n / 2) >> 1);
      if constexpr (!(n & 1) && n < 24) ldw(40 + n / 2);
      SB();
    });
    if (!hasS) { z2a[4] = f4{0.f, 0.f, 0.f, 0.f}; z2a[5] = z2a[4]; }
    f2 zs[6], zq[6];
    FORU(i, 6) { zs[i] = f2{z2a[i].x, z2a[i].y} + f2{z2a[i].z, z2a[i].w}; zq[i] = f2{z2a[i].x, z2a[i].y} * f2{z2a[i].x, z2a[i].y}; }
    SB();
    FORU(i, 6) zq[i] = __builtin_elementwise_fma(f2{z2a[i].z, z2a[i].w}, f2{z2a[i].z, z2a[i].w}, zq[i]);
    FORU(i, 2) zs[i] += zs[4 + i];
    SB();
    float us[4], uq[4], u[4];
    FORU(i, 2) zq[i] += zq[4 + i];
    FORU(t, 4) us[t] = zs[t].x + zs[t].y;
    SB();
    FORU(t, 4) uq[3 - t] = zq[3 - t].x + zq[3 - t].y;
    SB();
    FORU(t, 4) u[3 - t] = sw32sum(us[3 - t], uq[3 - t]);
    LAS float* zp = (LAS float*)(L + L_ZST) + (w * 2 + (lane >> 5)) * 64 + c16;
    zp[16 * ((2 * (q4 & 1) + 1) ^ xw)] = sw16sum(u[1], u[3]);
    zp[16 * ((2 * (q4 & 1)) ^ xw)] = sw16sum(u[0], u[2]);
  }
  asm volatile("s_waitcnt vmcnt(20)" ::: "memory");
  __syncthreads();
  if (w == 0) {
    LAS float* zp = (LAS float*)(L + L_ZST) + lane;
    const float s = (zp[0] + zp[128]) + (zp[256] + zp[384]), ss = (zp[64] + zp[192]) + (zp[320] + zp[448]);
    st128(rs, WS_ST2 + ((pi * 8 + pj) * 64 + lane) * 16, v4u{__float_as_uint(s), tag, __float_as_uint(ss), tag});
  }
  __builtin_amdgcn_raw_buffer_store_b32(tag, rs, tid == 64 ? WS_F3 + (pj * 32 + pi) * 128 : OOB, 0, SC1);
  CFENCE();
  STAMP(11);
  v4u wo[6][4];
  const u32 wob = WS_WOP + (pj * 22 + 6 * w) * 4096, wobh = ssel(w < 3, wob, OOB);
  auto ldo = [&](int n) { wo[n >> 2][n & 3] = ld128s(rs, lane * 16 + (n & 3) * 1024, (n < 16 ? wob : wobh) + (n >> 2) * 4096); };
  f4 z1a[6];
  v2u xres[4];
  v4u g[8];
  u32 fv;
  auto f_ld = [&]() { fv = __builtin_amdgcn_raw_buffer_load_b32(rs, (lane & 7) * 128, WS_F3 + (pj * 32 + 8 * w) * 128, SC1); };
  auto s2b_ld = [&](int n) { g[n] = ld128s(rs, (2 * q4 * 64 + c16) * 16 + (n & 1) * 1024, WS_ST2 + pi * 8192 + ((n >> 1) ^ xw) * 256); };
  {
    FORU(i, 16) ldx(i >> 3, i & 7);
    MM_HEAD();
    sfor<0, 96>([&](auto z) {
      constexpr int k = z.v / 6, i = z.v % 6, u = i == 0 ? 0 : i == 1 ? 4 : i == 2 ? 1 : i == 3 ? 5 : i - 2;
      if (i == 0) xwait(k);
      if (k) m_vv(z1a[u], wf[2 + (u >> 2)][k], bx(k, u & 3)); else m_vv0(z1a[u], wf[2 + (u >> 2)][k], bx(k, u & 3));
      if (i < 2) { ldx(k + 2, 2 * i); ldx(k + 2, 2 * i + 1); } else ldx(k + 2, i + 2);
      if (k < 3 && (i == 2 || i == 4)) ldf(1 + (i >> 1), 13 + k);
      if (k >= 3 && k < 9 && i == 3) ldw(49 + k);
      if ((k == 9 || k == 10) && i >= 2) s2b_ld(4 * (k - 9) + i - 2);
      if (k >= 11 && k < 14 && (i == 2 || i == 4)) ldw(58 + 2 * (k - 11) + (i >> 2));
      if (k == 14 && (i == 2 || i == 4)) ldb(1 + (i >> 1));
      if (k == 15 && i == 3) f_ld();
      SB();
    });
    MM_TAIL();
#pragma unroll
    for (int n = 0; n < 4; ++n) {
      const u32 row = 16 * n + 4 * q4 + (c16 >> 2);
      xres[n] = trread(L + L_XIMG + pj * 8192 + row * 128 + ((w ^ (((row >> 1) & 1) | (((row >> 3) & 1) << 1))) * 32) + 8 * (c16 & 3));
    }
  }
  lptr twa[6], tra[4];
  {
    lptr tw = L + L_TRS + w * TRS_W;
    FORU(i, 6) { twa[i] = tw + (16 * ((i & 3) ^ xw) + c16) * 64 + 8 * (((i & 4) + q4) ^ (c16 >> 1)); pin(twa[i]); }
    const u32 r0 = 8 * q4 + (c16 >> 2);
    FORU(i, 4) {
      tra[i] = tw + ((i & 2) ? (w & 1) * 2048 : 0) + r0 * 64 + (i & 1) * 256 + 8 * ((2 * (i & 2) + (c16 & 3)) ^ (((r0 + 4 * (i & 1)) >> 1) & 7));
      pin(tra[i]);
    }
  }
  STAMP(12);
  {
    auto bad = [&]() { u32 mx = tag, mn = tag; FORU(n, 8) { mx = umax3(mx, g[n].y, g[n].w); mn = umin3(mn, g[n].y, g[n].w); } return __any(mx != mn); };
    if (RARE(bad())) {
#pragma nounroll
      for (int sp = 0; sp < SPIN_CAP; ++sp) {
        FORU(n, 8) reld(g[n], rs, (2 * q4 * 64 + c16) * 16, WS_ST2 + pi * 8192 + ((n >> 1) ^ xw) * 256 + (n & 1) * 1024);
        asm volatile("s_waitcnt vmcnt(0)" : "+v"(g[0]), "+v"(g[1]), "+v"(g[2]), "+v"(g[3]), "+v"(g[4]), "+v"(g[5]), "+v"(g[6]), "+v"(g[7])::"memory");
        CNT(29, 10000);
        if (!bad()) break;
      }
    }
    STAMP(13);
    float a[4], c[4];
    FORU(t, 4) { a[t] = __uint_as_float(g[2 * t].x) + __uint_as_float(g[2 * t + 1].x); c[t] = __uint_as_float(g[2 * t].z) + __uint_as_float(g[2 * t + 1].z); }
    SB();
    const float a0 = sw32sum(a[0], a[2]), a1 = sw32sum(a[1], a[3]), c0 = sw32sum(c[0], c[2]), c1 = sw32sum(c[1], c[3]);
    const float m = sw16sum(a0, a1) * (1.f / 688), var = fmaxf(sw16sum(c0, c1) * (1.f / 688) - m * m, 0.f);
    const float r = __builtin_amdgcn_rcpf(ln_den(var)), n0 = -rbf(m) * r;
    auto gather = [&](float v, float (&o)[4]) {
      auto e = __builtin_amdgcn_permlane16_swap(__float_as_uint(v), __float_as_uint(v), false, false);
      auto e0 = __builtin_amdgcn_permlane32_swap(e[0], e[0], false, false);
      auto e1 = __builtin_amdgcn_permlane32_swap(e[1], e[1], false, false);
      o[0] = __uint_as_float(e0[0]); o[2] = __uint_as_float(e0[1]); o[1] = __uint_as_float(e1[0]); o[3] = __uint_as_float(e1[1]);
    };
    gather(r, a); gather(n0, c);
    SB();
    FORU(i, 6) z2a[i] = z2a[i] * a[i & 3] + c[i & 3];
    SB();
    v2u zp[6], zl[3], zh[3];
    FORU(i, 6) zp[i] = v2u{pkbf(z2a[i].x, z2a[i].y), pkbf(z2a[i].z, z2a[i].w)};
    SB();
    FORU(i, 6) *(LAS v2u*)twa[i] = zp[i];
    FORU(n, 3) { zl[n] = trread(tra[n & 2] + (n & 1) * 2048); zh[n] = trread(tra[(n & 2) + 1] + (n & 1) * 2048); }
    SB();
    const u32 zo = WS_Z2N + (pj * 6 * 64 + 2 * pi) * 1024;
    FORU(n, 3) st128s(rs, lane * 16, n < 2 ? zo + w * 65536 + n * 1024 : (hasS ? zo + blkS * 65536 + (w & 1) * 1024 : OOB), san4(v4u{zl[n].x, zl[n].y, zh[n].x, zh[n].y}));
  }
  CFENCE();
  STAMP(14);
  u32 bsr[4], gbp[2];
  v4u gg[2];
  FORU(t, 4) bsr[t] = gld<u16>(bsp, (64 * pi + 16 * (t ^ xw) + c16) * 2);
  FORU(m, 2) gg[m] = ld128(rs, WS_MOD + ((lane & 3) * 1536 + 1280 + 32 * pj + 16 * m + 8 * (c16 >> 3) + 2 * q4 + ((c16 >> 2) & 1)) * 16);
  if (RARE(__any(fv != tag))) {
#pragma nounroll
    for (int sp = 0; sp < SPIN_CAP; ++sp) {
      asm volatile("" ::: "memory");
      f_ld();
      if (!__any(fv != tag)) break;
    }
  }
  CFENCE();
  v2u z1[6];
  f4 ga[2][4], q[20];
  lptr qa[6];
  auto ldq = [&](int n) { q[n] = *(volatile LAS f4*)(n < 12 ? qa[n & 3] + (n >> 2) * 4096 : qa[4 + (n & 1)] + ((n - 12) >> 1) * 4096); };
  {
    const u32 zo = WS_Z2N + (pj * 6 * 64 + 16 * w) * 1024;
    const u32 rot = ((nb == 6 ? 0x1420u : 0x1320u) >> (4 * (pi & 3))) & 15;
    v4u zr[16];
    u32 zv[4];
    FORU(g, 4) zv[g] = lane * 16 + rk(g) * 4096;
    u32 so[6], rsl[6];
    FORU(it, 5) {
      const u32 blk = smod(w + 1 + smod((it > 1 ? it + nb - 6 : it) + rot, nb - 1), nb);
      so[it] = zo + blk * 65536;
      rsl[it + 1] = ssel(blk < 4, blk * 3 + ((w - blk - 1) & 3), 4 * blk - 4 + w) * 4096;
    }
    so[5] = zo + w * 65536;
    auto ldz = [&](int k, u32 s0) { zr[k] = ld128s(rs, zv[k >> 2] + (k & 3) * 1024, s0); };
    FORU(n, 7) ldo(n);
    redb(2);
    epi(z1a, 2, [&](auto N) {
      constexpr int n = N.v;
      SB();
      shuf2(IC<4>{}, N);
      if constexpr (n < 5) ldo(7 + n);
      if constexpr (n >= 6 && n < 22) ldz(n - 6, so[0]);
      SB();
    });
    FORU(i, 6) { z1[i] = v2u{pkbf(z1a[i].x, z1a[i].y), pkbf(z1a[i].z, z1a[i].w)}; pin(z1[i]); }
    FORU(m, 2) { gbp[m] = pkbf(quadsum(__uint_as_float(gg[m].x)), quadsum(__uint_as_float(gg[m].z))); pin(gbp[m]); }
    STAMP(25);
    __syncthreads();
    {
      auto bad = [&]() { u32 m = 0; FORU(k, 16) m = vmax(m, zr[k]); return __any(m == ~0u); };
      if (RARE(bad())) {
#pragma nounroll
        for (int sp = 0; sp < SPIN_CAP; ++sp) {
          asm volatile("" ::: "memory");
          FORU(k, 16) ldz(k, so[0]);
          CNT(28, 100);
          if (!bad()) break;
        }
      }
    }
    STAMP(15);
    u32 ra = 0, badm = 0;
    float ck = 0.f;
    v4u oth;
    MM_HEAD();
    auto slot = [&](auto z) {
      constexpr int it = z.v / 64, k = (z.v / 4) % 16, t = z.v % 4, h = it & 1;
      if (k) m_va(ga[h][t], zr[k], wsf[k][t]); else m_va0(ga[h][t], zr[k], wsf[k][t]);
      constexpr int pz = z.v / 4 - 1, pit = pz / 16, pk = pz % 16;
      if constexpr (t == 1 && pz >= 0) {
        if constexpr (pit < 5) ldz(pk, so[pit + 1]);
        else if constexpr (pk < 6) ldo(12 + pk);
      }
      if constexpr (it == 0 && t != 1 && 3 * k + t - (t > 0) < 32) shuf(IC<96 + 3 * k + t - (t > 0)>{});
      if constexpr (it > 0) {
        if (k == 2 && t == 0) { ck = ga[h ^ 1][0].x + ga[h ^ 1][0].y; pin(ck); }
        if (k == 3 && t == 0) { ck += ga[h ^ 1][0].z; pin(ck); }
        if (k == 4 && t == 0) { ck += ga[h ^ 1][0].w; pin(ck); }
        if (k == 5 && t == 0) badm |= __any(ck != ck) ? 1u << (it - 1) : 0u;
        if (k >= 2 && k < 6 && t == 2) { ra = rsl[it] + lane * 16 + (((k - 2) ^ xw) * 1024); pin(ra); }
        if constexpr (k >= 2 && k < 6 && t == 3) *(volatile LAS f4*)(L + L_RED + ra) = ga[h ^ 1][k - 2];
      }
      if constexpr (it == 5) {
        if constexpr (z.v >= 320 + 28 && z.v < 320 + 34) {
          constexpr int i = z.v - 348;
          u32 l16 = lane * 16;
          pin(l16);
          qa[i] = L + l16 + (L_RED + ((i < 4 ? w * 12 : (blkS * 4 - 4) * 4) + ((i & 3) ^ xw)) * 1024);
          pin(qa[i]);
        }
        if (k == 6 && t == 2) *(volatile LAS u32*)(L + L_XST + w * 4) = badm;
        if (k == 8 && t == 3) { asm volatile("s_waitcnt lgkmcnt(0)" ::: "memory"); __builtin_amdgcn_s_barrier(); }
        if (k == 9 && t == 0) oth = *(volatile LAS v4u*)(L + L_XST);
        if constexpr (z.v >= 320 + 37 && z.v < 320 + 57) ldq(z.v - 357);
      }
      SB();
    };
    sfor<0, 64>(slot);
    if (nb == 6) sfor<64, 128>(slot);
    else {
      MM_TAIL();
      const float t = (ga[0][0].x + ga[0][0].y) + (ga[0][0].z + ga[0][0].w);
      badm = __any(t != t) ? 1u : 0u;
      FORU(t, 4) { *(volatile LAS f4*)(L + L_RED + rsl[1] + lane * 16 + ((t ^ xw) * 1024)) = ga[0][t]; ga[1][t] = f4{0.f, 0.f, 0.f, 0.f}; }
      MM_HEAD();
    }
    sfor<128, 384>(slot);
    MM_TAIL();
    { const float t = (ga[1][0].x + ga[1][0].y) + (ga[1][0].z + ga[1][0].w); badm |= __any(t != t) ? 32u : 0u; }
    const u32 anyo = __builtin_amdgcn_readfirstlane(oth.x | oth.y | oth.z | oth.w);
    if (RARE(badm | anyo)) {
#pragma nounroll
      for (u32 it = 0; it < 6; ++it) {
        if (!((badm >> it) & 1)) continue;
        CNT(28, 1);
        const u32 s0 = it == 0 ? so[0] : it == 1 ? so[1] : it == 2 ? so[2] : it == 3 ? so[3] : it == 4 ? so[4] : so[5];
        const u32 r0 = it == 0 ? rsl[1] : it == 1 ? rsl[2] : it == 2 ? rsl[3] : it == 3 ? rsl[4] : rsl[5];
#pragma nounroll
        for (int sp = 0; sp < SPIN_CAP; ++sp) {
          u32 m = 0;
          FORU(k, 16) ldz(k, s0);
          FORU(k, 16) m = vmax(m, zr[k]);
          asm volatile("" ::: "memory");
          if (!__any(m == ~0u)) break;
        }
        MM_HEAD();
        FORU(k, 16) FORU(t, 4) { if (k) m_va(ga[0][t], zr[k], wsf[k][t]); else m_va0(ga[0][t], zr[k], wsf[k][t]); }
        MM_TAIL();
        FORU(t, 4) {
          if (it < 5) *(LAS f4*)(L + L_RED + r0 + lane * 16 + ((t ^ xw) * 1024)) = ga[0][t];
          else ga[1][t] = ga[0][t];
        }
      }
      if (anyo) __syncthreads();
      FORU(n, 20) ldq(n);
    }
  }
  __builtin_amdgcn_raw_buffer_store_b32(tag, rs, lane == 0 ? 0 : OOB, w == 0 ? WS_H4 + (pi * 8 + pj) * 128 : OOB, SC1);
  STAMP(26);
  {
    const u32 lane = fresh_lane(), c16 = lane & 15, q4 = lane >> 4;
    f2 zf[12], gs2[12];
    auto half = [](const f4& v, int hh) { return hh ? f2{v.z, v.w} : f2{v.x, v.y}; };
    FORU(i, 12) gs2[i] = i < 8 ? half(q[i >> 1], i & 1) + half(q[4 + (i >> 1)], i & 1) : half(q[8 + (i >> 1)], i & 1) + half(q[10 + (i >> 1)], i & 1);
    FORU(i, 12) zf[i] = i < 8 ? half(ga[1][i >> 1], i & 1) + half(q[8 + (i >> 1)], i & 1) : half(q[12 + (i >> 1)], i & 1) + half(q[14 + (i >> 1)], i & 1);
    SB();
    FORU(i, 12) gs2[i] += bflo(bsr[(i >> 1) & 3]);
    SB();
    FORU(i, 12) gs2[i] += zf[i];
    SB();
    FORU(i, 12) zf[i] = bf2f(z1[i >> 1][i & 1]);
    SB();
    FORU(i, 12) gs2[i] *= zf[i];
    SB();
    v2u P[6];
    FORU(i, 12) P[i >> 1][i & 1] = pkbf(gs2[i]);
    SB();
#pragma unroll
    for (int h = 0; h < 3; ++h) {
      auto rx = __builtin_amdgcn_permlane16_swap(P[2 * h].x, P[2 * h + 1].x, false, false);
      auto ry = __builtin_amdgcn_permlane16_swap(P[2 * h].y, P[2 * h + 1].y, false, false);
      const u32 tt = (2 * (h & 1) + (q4 & 1)) ^ xw;
      st128s(rs, WS_PB + ((pi * 4 + tt) * 44 + blk0) * 512 + (q4 >> 1) * 256 + c16 * 16, h < 2 ? w * 512 : (hasS ? blkS * 512 : OOB),
             san4(v4u{rx[0], ry[0], rx[1], ry[1]}));
    }
    FORU(n, 6) ldo(18 + n);
  }
  STAMP(17);
  {
    const u32 lane = fresh_lane(), c16 = lane & 15, q4 = lane >> 4;
    f4 oa[2][4];
    v4u pr[4][6];
    u32 pb[4], pbh[4];
    u32 rd[3];
    FORU(u, 4) {
      const u32 d = u < 3 ? 1 + smod(u + smod(pj & 3, 3), 3) : 4;
      pb[u] = WS_PB + ((pi * 4 + ((w + d) & 3)) * 44 + 12 * w) * 512; pbh[u] = ssel(w < 3, pb[u] + 4096, OOB);
      if (u < 3) rd[u] = L_RED2 + (((w + d) & 3) * 3 + 3 - d) * 4096 + lane * 16;
    }
    const u32 pv3 = (w == 3 && lane >= 32) ? OOB : lane * 16 + 3072;
    auto ldp = [&](int n) {
      const int u = n / 6, k = n % 6;
      pr[u][k] = ld128s(rs, k == 3 ? pv3 : lane * 16 + (k & 3) * 1024, k < 4 ? pb[u] : pbh[u]);
    };
#pragma nounroll
    for (int sp = 0; sp < SPIN_CAP; ++sp) {
      asm volatile("" ::: "memory");
      if (!__any(__builtin_amdgcn_raw_buffer_load_b32(rs, (lane & 7) * 128, WS_H4 + pi * 1024, SC1) != tag)) break;
      CNT(28, 10000);
      __builtin_amdgcn_s_sleep(2);
    }
    STAMP(18);
    FORU(n, 12) { ldp(n); SB(); }
    u32 bad = 0;
    MM_HEAD();
    sfor<0, 96>([&](auto z) {
      constexpr int u = z.v / 24, k = (z.v / 4) % 6, n = z.v % 4;
      if (k) m_vv(oa[u & 1][n], wo[k][n], pr[u][k]); else m_vv0(oa[u & 1][n], wo[k][n], pr[u][k]);
      if constexpr (n == 1 && z.v / 4 < 12) ldp(12 + z.v / 4);
      if constexpr (u > 0 && k == 2 && n == 0) bad |= __any(oa[(u & 1) ^ 1][0].x != oa[(u & 1) ^ 1][0].x) ? 1u << (u - 1) : 0u;
      if constexpr (u > 0 && k >= 2 && n == 2) *(LAS f4*)(L + rd[u - 1] + (k - 2) * 1024) = oa[(u & 1) ^ 1][k - 2];
      SB();
    });
    MM_TAIL();
    bad |= __any(oa[1][0].x != oa[1][0].x) ? 8u : 0u;
    if (RARE(bad)) {
#pragma nounroll
      for (u32 u = 0; u < 4; ++u) {
        if (!((bad >> u) & 1)) continue;
        CNT(28, 1000000);
        pb[0] = u == 0 ? pb[0] : u == 1 ? pb[1] : u == 2 ? pb[2] : pb[3];
        pbh[0] = u == 0 ? pbh[0] : u == 1 ? pbh[1] : u == 2 ? pbh[2] : pbh[3];
#pragma nounroll
        for (int sp = 0; sp < SPIN_CAP; ++sp) {
          u32 m = 0;
          asm volatile("" ::: "memory");
          FORU(k, 6) ldp(k);
          FORU(k, 6) m = vmax(m, pr[0][k]);
          if (!__any(m == ~0u)) break;
        }
        asm volatile("fk_text_b:");
        MM_HEAD();
        FORU(k, 6) FORU(n, 4) { if (k) m_vv(oa[0][n], wo[k][n], pr[0][k]); else m_vv0(oa[0][n], wo[k][n], pr[0][k]); }
        MM_TAIL();
        FORU(n, 4) {
          if (u < 3) *(LAS f4*)(L + (u == 0 ? rd[0] : u == 1 ? rd[1] : rd[2]) + n * 1024) = oa[0][n];
          else oa[1][n] = oa[0][n];
        }
      }
    }
    __syncthreads();
    f4 o[4];
    FORU(n, 4) {
      lptr p = L + L_RED2 + (w * 12 + n) * 1024 + lane * 16;
      o[n] = (oa[1][n] + *(LAS f4*)p) + (*(LAS f4*)(p + 4096) + *(LAS f4*)(p + 8192));
    }
    STAMP(19);
    v2u O[4];
#pragma unroll
    for (int n = 0; n < 4; ++n) {
      const u32 g0 = (n & 1) ? dppu<0x158>(gbp[n >> 1]) : dppu<0x150>(gbp[n >> 1]), g1 = (n & 1) ? dppu<0x15C>(gbp[n >> 1]) : dppu<0x154>(gbp[n >> 1]);
      O[n] = v2u{pkbf(bf2f(xres[n].x) + bf2f(g0) * f2{o[n].x, o[n].y}), pkbf(bf2f(xres[n].y) + bf2f(g1) * f2{o[n].z, o[n].w})};
    }
#pragma unroll
    for (int h = 0; h < 2; ++h) {
      auto rx = __builtin_amdgcn_permlane16_swap(O[2 * h].x, O[2 * h + 1].x, false, false);
      auto ry = __builtin_amdgcn_permlane16_swap(O[2 * h].y, O[2 * h + 1].y, false, false);
      *(v4u*)(out + (64 * pi + 16 * w + c16) * 512 + 64 * pj + 16 * (2 * h + (q4 & 1)) + 8 * (q4 >> 1)) = v4u{rx[0], ry[0], rx[1], ry[1]};
    }
  }
  if (b == 0 && w == 0) __builtin_amdgcn_raw_buffer_store_b32(tag, rs, WS_E, 0, SC1);
  STAMP(20);
  STAMP_FLUSH();
}

extern "C" void kernel_launch(void* const* d_in, const int* in_sizes, int n_in, void* d_out, int out_size, void* d_ws, size_t ws_size,
                              hipStream_t stream) {
  (void)in_sizes; (void)n_in; (void)out_size; (void)ws_size;
  (void)hipFuncSetAttribute((const void*)fused_kernel, hipFuncAttributeMaxDynamicSharedMemorySize, LDS_BYTES);
  fused_kernel<<<256, 256, LDS_BYTES, stream>>>((const u16*)d_in[0], (const u16*)d_in[1], (const u16*)d_in[2], (const u16*)d_in[3],
                                                (const u16*)d_in[4], (const u16*)d_in[5], (const u16*)d_in[6], (const u16*)d_in[8],
                                                (const u16*)d_in[9], (const u16*)d_in[10], (const u16*)d_in[11], (const u16*)d_in[12],
                                                (const u16*)d_in[13], (u16*)d_out, (unsigned char*)d_ws);
}
```

```cpp
#include <hip/hip_runtime.h>
#include <stdint.h>

typedef unsigned u32;
typedef unsigned short u16;
typedef unsigned long long u64;
typedef u32 v4u __attribute__((ext_vector_type(4)));
typedef u32 v2u __attribute__((ext_vector_type(2)));
typedef float f4 __attribute__((ext_vector_type(4)));
typedef float f2 __attribute__((ext_vector_type(2)));
typedef __bf16 bf8 __attribute__((ext_vector_type(8)));
typedef __bf16 bf2 __attribute__((ext_vector_type(2)));
typedef _Float16 h8 __attribute__((ext_vector_type(8)));
typedef _Float16 h2 __attribute__((ext_vector_type(2)));
typedef short s4 __attribute__((ext_vector_type(4)));
#define LAS __attribute__((address_space(3)))
#define GAS __attribute__((address_space(1)))
#define CAS __attribute__((address_space(4)))
#define DI __device__ __forceinline__
typedef __amdgpu_buffer_rsrc_t rsrc_t;
typedef LAS unsigned char* lptr;

#define XMAP 2

constexpr int WS_E = 0, WS_STAT1 = 4096, WS_CD = 32768, WS_ST2 = 65536, WS_F3 = 327680, WS_H4 = 360448, WS_MOD = 524288, WS_BWP = 655360;
constexpr int WS_XG = 1 << 20, WS_X1 = 5 << 20, WS_Z2N = 7 << 20, WS_PB = 10 << 20, WS_WINP = 13 << 20, WS_WOP = 29 << 19;
constexpr int WS_DBG = 24 << 20, WS_END = 25 << 20;
constexpr int OOB = 0x40000000;
constexpr int TABP = 544, TAB_CH = 63 * TABP;
constexpr int L_TAB = 0, L_Y = 0, Y_CH = 21504;
constexpr int L_PKW = 75776, L_SHF = 81920;
constexpr int L_PKI = 68544;
constexpr int L_TOE = 108544, TOE_C = 5312, TOE_CH = 4 * TOE_C;
constexpr int L_U = 151040, U_CH = 6320;
constexpr int L_XIMG = 0, L_TRS = 65536, TRS_W = 4096;
constexpr int L_RED = 0, L_RED2 = 81920;
constexpr int L_XST = 155648, L_ZST = L_XST + 2048, L_BW = L_ZST + 2048;
constexpr int LDS_BYTES = 163840;
constexpr int SC1 = 16;
constexpr int SPIN_CAP = 1 << 15;
static_assert(LDS_BYTES <= 163840, "LDS");

DI float bflo(u32 v) { return __uint_as_float(v << 16); }
DI float bfhi(u32 v) { return __uint_as_float(v & 0xffff0000u); }
DI f2 bf2f(u32 v) { return f2{bflo(v), bfhi(v)}; }
DI u32 pkbf(float a, float b) { f2 v = {a, b}; return __builtin_bit_cast(u32, __builtin_convertvector(v, bf2)); }
DI u32 pkbf(f2 v) { return __builtin_bit_cast(u32, __builtin_convertvector(v, bf2)); }
DI u32 pkbf_a(float a, float b) { u32 r; asm("v_cvt_pk_bf16_f32 %0, %1, %2" : "=v"(r) : "v"(a), "v"(b)); return r; }
DI u32 pkh(f2 v) { return __builtin_bit_cast(u32, __builtin_convertvector(v, h2)); }
DI float rbf(float a) { return bflo(pkbf(a, a)); }
DI u32 san(u32 v) { return v < 0xFFFFFFFEu ? v : 0xFFFFFFFEu; }
DI v4u san4(v4u v) { return v4u{san(v.x), san(v.y), san(v.z), san(v.w)}; }
DI u32 umax3(u32 a, u32 b, u32 c) { return max(max(a, b), c); }
DI u32 umin3(u32 a, u32 b, u32 c) { return min(min(a, b), c); }
DI u32 vmax(u32 m, v4u v) { return umax3(umax3(m, v.x, v.y), v.z, v.w); }
DI v4u ld128(rsrc_t rs, int off) { return __builtin_amdgcn_raw_buffer_load_b128(rs, off, 0, SC1); }
DI v4u ld128s(rsrc_t rs, int off, int soff) { return __builtin_amdgcn_raw_buffer_load_b128(rs, off, soff, SC1); }
DI void st128(rsrc_t rs, int off, v4u v) { __builtin_amdgcn_raw_buffer_store_b128(v, rs, off, 0, SC1); }
DI void st128s(rsrc_t rs, int off, int soff, v4u v) { __builtin_amdgcn_raw_buffer_store_b128(v, rs, off, soff, SC1); }
DI void st64(rsrc_t rs, int off, v2u v) { __builtin_amdgcn_raw_buffer_store_b64(v, rs, off, 0, SC1); }
DI void reld(v4u& v, rsrc_t rs, u32 voff, u32 soff) {
  asm volatile("s_nop 4\n\tbuffer_load_dwordx4 %0, %1, %2, %3 offen sc1" : "+v"(v) : "v"(voff), "s"(rs), "s"(soff) : "memory");
}
#define RARE(c) __builtin_expect(!!(c), 0)
#define DRAIN() asm volatile("s_waitcnt vmcnt(0)" ::: "memory")
#define SB() __builtin_amdgcn_sched_barrier(0)
#define CFENCE() do { asm volatile("" ::: "memory"); SB(); } while (0)
template <int C> DI u32 dppu(u32 v) { return (u32)__builtin_amdgcn_update_dpp(0, (int)v, C, 0xF, 0xF, true); }
template <int C> DI float dppadd(float v) { return v + __uint_as_float(dppu<C>(__float_as_uint(v))); }
DI float sw16sum(float a, float b) { auto r = __builtin_amdgcn_permlane16_swap(__float_as_uint(a), __float_as_uint(b), false, false); return __uint_as_float(r[0]) + __uint_as_float(r[1]); }
DI float sw32sum(float a, float b) { auto r = __builtin_amdgcn_permlane32_swap(__float_as_uint(a), __float_as_uint(b), false, false); return __uint_as_float(r[0]) + __uint_as_float(r[1]); }
DI float quadsum(float v) { v = dppadd<0xB1>(v); return dppadd<0x4E>(v); }
DI float rowsum(float v) { v = quadsum(v); v = dppadd<0x141>(v); return dppadd<0x140>(v); }
DI f4 mfma_bf(v4u a, v4u b, f4 c) { return __builtin_amdgcn_mfma_f32_16x16x32_bf16(__builtin_bit_cast(bf8, a), __builtin_bit_cast(bf8, b), c, 0, 0, 0); }
DI f4 mfma_h(v4u a, v4u b, f4 c) { return __builtin_amdgcn_mfma_f32_16x16x32_f16(__builtin_bit_cast(h8, a), __builtin_bit_cast(h8, b), c, 0, 0, 0); }
DI v2u trread(lptr p) { return __builtin_bit_cast(v2u, __builtin_amdgcn_ds_read_tr16_b64_v4i16((LAS s4*)p)); }
template <int N> struct IC { static constexpr int v = N; };
template <int I, int N, class F> DI void sfor(F f) { if constexpr (I < N) { f(IC<I>{}); sfor<I + 1, N>(f); } }
#define FORU(i, n) _Pragma("unroll") for (int i = 0; i < (n); ++i)
DI f2 cm1(f2 a, f2 b) { f2 t; asm("v_pk_mul_f32 %0, %1, %2 op_sel:[0,0] op_sel_hi:[0,1]" : "=v"(t) : "v"(a), "v"(b)); return t; }
DI f2 cm2(f2 a, f2 b, f2 t) { f2 r; asm("v_pk_fma_f32 %0, %1, %2, %3 op_sel:[1,1,0] op_sel_hi:[1,0,1] neg_lo:[0,1,0]" : "=v"(r) : "v"(a), "v"(b), "v"(t)); return r; }
DI f2 cexp_p(float al2, float rev, float p) {
  float m = __builtin_amdgcn_exp2f(al2 * p), f = __builtin_amdgcn_fractf(rev * p);
  return f2{m * __builtin_amdgcn_cosf(f), m * __builtin_amdgcn_sinf(f)};
}
template <int N, class F> DI void gelu_n(f2 (&v)[N], F fill) {
  const float c0 = -2.0f * 0.7978845608028654f * 1.4426950408889634f, c1 = c0 * 0.044715f;
  f2 a[N];
  sfor<0, 2>([&](auto z) { FORU(i, 6) a[6 * z.v + i] = v[6 * z.v + i] * v[6 * z.v + i]; fill(IC<4 + z.v>{}); });
  sfor<0, 2>([&](auto z) { FORU(i, 6) a[6 * z.v + i] = __builtin_elementwise_fma(a[6 * z.v + i], f2{c1, c1}, f2{c0, c0}); fill(IC<6 + z.v>{}); });
  sfor<0, 2>([&](auto z) { FORU(i, 6) a[6 * z.v + i] *= v[6 * z.v + i]; fill(IC<8 + z.v>{}); });
  sfor<0, 6>([&](auto z) { FORU(i, 2) a[2 * z.v + i] = f2{__builtin_amdgcn_exp2f(a[2 * z.v + i].x), __builtin_amdgcn_exp2f(a[2 * z.v + i].y)}; fill(IC<10 + z.v>{}); });
  sfor<0, 2>([&](auto z) { FORU(i, 6) a[6 * z.v + i] += 1.0f; fill(IC<16 + z.v>{}); });
  sfor<0, 6>([&](auto z) { FORU(i, 2) a[2 * z.v + i] = f2{__builtin_amdgcn_rcpf(a[2 * z.v + i].x), __builtin_amdgcn_rcpf(a[2 * z.v + i].y)}; fill(IC<18 + z.v>{}); });
  sfor<0, 2>([&](auto z) { FORU(i, 6) v[6 * z.v + i] *= a[6 * z.v + i]; fill(IC<24 + z.v>{}); });
}
DI float ln_den(float var) { return rbf(__builtin_amdgcn_sqrtf(rbf(rbf(var) + 1e-5f))); }
template <class T> DI T gld(const void* base, u32 off) { return *(const GAS T*)((const GAS char*)base + off); }
#define MF "v_mfma_f32_16x16x32_bf16 "
DI void m_av(f4& c, v4u a, v4u b) { asm volatile(MF "%0, %1, %2, %0" : "+v"(c) : "a"(a), "v"(b)); }
DI void m_va(f4& c, v4u a, v4u b) { asm volatile(MF "%0, %1, %2, %0" : "+v"(c) : "v"(a), "a"(b)); }
DI void m_vv(f4& c, v4u a, v4u b) { asm volatile(MF "%0, %1, %2, %0" : "+v"(c) : "v"(a), "v"(b)); }
DI void m_av0(f4& c, v4u a, v4u b) { asm volatile(MF "%0, %1, %2, 0" : "=&v"(c) : "a"(a), "v"(b)); }
DI void m_va0(f4& c, v4u a, v4u b) { asm volatile(MF "%0, %1, %2, 0" : "=&v"(c) : "v"(a), "a"(b)); }
DI void m_vv0(f4& c, v4u a, v4u b) { asm volatile(MF "%0, %1, %2, 0" : "=&v"(c) : "v"(a), "v"(b)); }
DI v4u aget(v4u a) {
  v4u r;
  _Pragma("unroll") for (int e = 0; e < 4; ++e) { u32 t; asm volatile("v_accvgpr_read_b32 %0, %1" : "=v"(t) : "a"(a[e])); r[e] = t; }
  return r;
}
DI u32 ssel(bool c, u32 a, u32 b) { asm("" : "+s"(a)); asm("" : "+s"(b)); return c ? a : b; }
DI u32 smod(u32 v, u32 m) { u32 t = v - m; asm("" : "+s"(t)); return (int)t >= 0 ? t : v; }
DI u32 fresh_lane() { u32 l; asm volatile("v_mbcnt_lo_u32_b32 %0, -1, 0\n\tv_mbcnt_hi_u32_b32 %0, -1, %0" : "=v"(l)); return l; }
template <class T> DI void pin(T& v) { asm volatile("" : "+v"(v)); }
#define MM_HEAD() do { SB(); asm volatile("s_nop 7"); SB(); } while (0)
#define MM_TAIL() do { SB(); asm volatile("s_nop 7"); SB(); } while (0)
DI int toe_addr(int pos) { return 2 * pos + 8 * (pos >> 4); }

#define DA 15
#define DB 26
#define STAMP(n) do { if ((n) == DA || (n) == DB) { SB(); u32 t_ = (u32)__builtin_amdgcn_s_memrealtime(); if ((n) == DA) dta = t_; else dtb = t_; SB(); } } while (0)
#define STAMP_FLUSH() do { u32 d_ = min(2u * (dtb - dta), 20000u), s_ = (u32)__builtin_amdgcn_s_memrealtime(); for (int q_ = 0; q_ < 20000; ++q_) { if ((u32)__builtin_amdgcn_s_memrealtime() - s_ >= d_) break; __builtin_amdgcn_s_sleep(1); } } while (0)
#define CNT(n,v)
#define STAMP_INIT() u32 dta = 0, dtb = 0

template <int BAND> struct Band {
  static constexpr int R = 128 - 32 * BAND, NT = R / 16, NS = 15 + NT / 2;
  static constexpr int YO = BAND == 0 ? 0 : BAND == 1 ? 8448 : BAND == 2 ? 14848 : 19200;
  lptr al, ah, bb, yp;
  f4 acc[NT];
  v2u lo[32], hi[32];
  v4u bf[NS];
  DI void load(int s, int i) {
    if (i < 4) {
      const int n = 2 * s + (i >> 1);
      if (n < 32) { if (i & 1) hi[n] = *(LAS v2u*)(ah + 40 * (31 - n)); else lo[n] = *(LAS v2u*)(al + 40 * (31 - n)); }
    } else if (s < NS) bf[s] = *(LAS v4u*)(bb + 80 * (NT / 2 - 1 - s + 15));
  }
  DI void mma(int s, int t) {
    const int n = t - NT + 2 + 2 * s;
    if (s < NS && n >= 0 && n < 32) {
      if (n < 2) m_vv0(acc[t], v4u{lo[n].x, lo[n].y, hi[n].x, hi[n].y}, bf[s]); else m_vv(acc[t], v4u{lo[n].x, lo[n].y, hi[n].x, hi[n].y}, bf[s]);
    }
  }
  DI void flush(int s, int t) {
    const int n = t - NT + 2 + 2 * (s - 1);
    if (n == 30 || n == 31) *(LAS f4*)(yp + 64 * t) = acc[t];
  }
};
template <int B1, int B2, class F> DI void conv2(const lptr (&bp)[8], F side) {
  Band<B1> u; Band<B2> v;
  u.al = bp[0]; u.ah = bp[1]; u.bb = bp[2]; u.yp = bp[3]; v.al = bp[4]; v.ah = bp[5]; v.bb = bp[6]; v.yp = bp[7];
  constexpr int N1 = Band<B1>::NT, NS = Band<B1>::NS;
#pragma unroll
  for (int s = 0; s < 2; ++s)
#pragma unroll
    for (int i = 0; i < 5; ++i) { u.load(s, i); v.load(s, i); SB(); }
  sfor<0, 10 * (NS + 1)>([&](auto z) {
    constexpr int s = z.v / 10, i = z.v % 10;
    if (s >= 17 && i == 0) asm volatile("s_nop %0" ::"i"(s == NS ? 7 : s == NS - 1 ? 5 : 2));
    if (i < N1) { u.mma(s, i); u.flush(s, i); } else { v.mma(s, i - N1); v.flush(s, i - N1); }
    if (i < 5) u.load(s + 2, i); else v.load(s + 2, i - 5);
    side(IC<s>{}, IC<i>{});
    SB();
  });
}

__global__ void __launch_bounds__(256, 1)
fused_kernel(const u16* __restrict__ x, const u16* __restrict__ lar, const u16* __restrict__ aim, const u16* __restrict__ cre,
             const u16* __restrict__ cim, const u16* __restrict__ ldt, const u16* __restrict__ dsk, const u16* __restrict__ win,
             const u16* __restrict__ wsp, const u16* __restrict__ bsp, const u16* __restrict__ wout, const u16* __restrict__ cvec,
             const u16* __restrict__ wmod, u16* __restrict__ out, unsigned char* ws) {
  extern __shared__ __attribute__((aligned(16))) unsigned char smem[];
  lptr L = (lptr)smem;
  const u32 b = blockIdx.x, tid = threadIdx.x, lane = tid & 63;
  STAMP_INIT();
  STAMP(0);
  const u32 w = __builtin_amdgcn_readfirstlane(tid >> 6);
  const u32 xc = b & 7, rr = b >> 3, cp = xc * 32 + rr;
  const u32 c16 = lane & 15, q4 = lane >> 4;
  u32 pf0 = 0, pf1 = 0;
  {
    u64 pc;
    u32 len;
    asm volatile("s_getpc_b64 %0\nfk_text_a:\n\ts_mov_b32 %1, fk_text_b-fk_text_a" : "=s"(pc), "=s"(len));
    const u32 o = (rr * 16 + tid) * 128;
    if (tid < 16 && o < len) pf0 = gld<u32>((const void*)pc, o);
  }
  CFENCE();
  const u32 tag = *(const CAS u32*)(ws + WS_E) + 1u;
  const u32 tb = 512 * (b >> 6) + 32 * (b & 15) + 2 * ((b >> 4) & 3);
  const v4u sr0 = gld<v4u>(x, (tb + 8 * w) * 1024 + lane * 16), sr1 = gld<v4u>(x, (tb + 8 * w) * 1024 + 1024 + lane * 16);
  const u32 xgo = (tb + 8 * (lane & 3)) * 1024 + (16 * w + (lane >> 2)) * 16;
  const v4u xg0 = gld<v4u>(x, xgo), xg1 = gld<v4u>(x, xgo + 1024);
  const u32 mG = b % 48, mP = min(b / 48, 3u), mrow = mP * 128 + (lane >> 1);
  v4u wm[4];
  u32 cv[4];
#pragma unroll
  for (int n = 0; n < 4; ++n) wm[n] = gld<v4u>(wmod, (mrow + 32 * n) * 6144 + mG * 128 + w * 32 + (lane & 1) * 16);
#pragma unroll
  for (int n = 0; n < 4; ++n) cv[n] = gld<u16>(cvec, (mrow + 32 * n) * 2);
  const u32 pidx = (cp * 256 + tid) * 2;
  const u32 p_lar = gld<u16>(lar, pidx), p_aim = gld<u16>(aim, pidx), p_cr = gld<u16>(cre, pidx), p_ci = gld<u16>(cim, pidx);
  const u32 p_cr2 = gld<u16>(cre, pidx ^ 128), p_ci2 = gld<u16>(cim, pidx ^ 128);
  const u32 p_ldt = *(const CAS u32*)(ldt + 2 * cp), p_dsk = *(const CAS u32*)(dsk + 2 * cp);
  CFENCE();
  const u32 pi = (xc >> 1) * 8 + (rr & 7), pj = (xc & 1) * 4 + (rr >> 3);
  const u32 nb = pj < 3 ? 6 : 5, blk0 = 5 * pj + min(pj, 3u);
  const bool two = w + 4 < nb;
  const rsrc_t rs = __builtin_amdgcn_make_buffer_rsrc((void*)ws, 0, WS_END, 0x00020000);
  const v4u SENT = {~0u, ~0u, ~0u, ~0u};
  const int x1w = WS_X1 + ((tid >> 3) * 512 + 2 * cp) * 128 + (tid & 7) * 16;
  const u32 wio_src = ((32 * (pi >> 1) + (lane >> 1)) * 1376 + (pi & 1) * 688 + 16 * (blk0 + w)) * 2 + (lane & 1) * 16;
  const v4u wi0 = gld<v4u>(win, wio_src), wi1 = gld<v4u>(win, two ? wio_src + 128 : wio_src);
  const rsrc_t ro = __builtin_amdgcn_make_buffer_rsrc((void*)wout, 0, 688 * 1024, 0x00020000);
  const v4u wo0 = __builtin_amdgcn_raw_buffer_load_b128(ro, (lane >> 1) * 1024 + (lane & 1) * 16, b < 176 ? (b % 22) * 32768 + (b / 22) * 128 + w * 32 : OOB, 0);
  CFENCE();
  if (tid < 40) *(LAS v4u*)(L + L_TOE + (tid / 5) * TOE_C + 5120 + (tid % 5) * 16) = v4u{0, 0, 0, 0};
  if (tid < 150) *(LAS v4u*)(L + L_U + (tid / 75) * U_CH + (tid % 75) * 16) = v4u{0, 0, 0, 0};
  lptr ka[5], pa[3], t0, t1, up, bp[8], yp[4];
  int mo, mo2, wio, bwo;
  {
    const u32 hi = w & 1, ch = w >> 1;
    lptr tb_ = L + L_TAB + ch * TAB_CH + 16 * q4;
    const u32 i2 = (c16 + ((c16 >> 1) & 1)) >> 1, i4 = (c16 + 1 + ((c16 >> 2) & 1)) >> 2, i8 = (c16 + 3 + ((c16 >> 3) & 1)) >> 3;
    const u32 row[5] = {hi ? 35 + i8 : c16, hi ? 38 + i8 : 16 + i2, hi ? 41 + i8 : 25 + i4, hi ? 44 + i8 : 30 + i4, 47 + c16};
    FORU(i, 5) { ka[i] = tb_ + row[i] * TABP; pin(ka[i]); }
    FORU(a, 3) { pa[a] = L + (a == 0 ? L_PKI + w * 1024 : a == 1 ? L_PKI + 4096 + hi * 1024 : L_PKW + w * 8192) + (8 * q4 + (c16 >> 2)) * 32 + 8 * (c16 & 3); pin(pa[a]); }
    const int i0 = 2044 - (1024 * hi + 768 + 16 * c16 + 4 * q4);
    t0 = L + L_TOE + ch * TOE_CH + toe_addr(i0); t1 = L + L_TOE + ch * TOE_CH + toe_addr(i0 + 4);
    up = L + L_U + 80 * (15 + (tid >> 2)) + 16 * (tid & 3);
    pin(t0); pin(t1); pin(up);
    FORU(n, 2) {
      const u32 B = n ? 3 - hi : hi, R = 128 - 32 * B, c = (3 - c16) & 3, P = 2048 - 512 * B - 496 - 4 * (c16 >> 2) + 8 * q4;
      bp[4 * n] = L + L_TOE + ch * TOE_CH + c * TOE_C + toe_addr(P);
      bp[4 * n + 1] = L + L_TOE + ch * TOE_CH + c * TOE_C + toe_addr(P + 4);
      bp[4 * n + 2] = L + L_U + ch * U_CH + 80 * (4 - B) * c16 + 16 * q4;
      bp[4 * n + 3] = L + L_Y + ch * Y_CH + (n ? (hi ? 14848 : 19200) : (hi ? 8448 : 0)) + c16 * (4 * R + 16) + 16 * q4;
    }
    FORU(n, 8) pin(bp[n]);
    FORU(B, 4) {
      const u32 tp = 8 * tid - 512 * B;
      yp[B] = L + L_Y + (B == 0 ? 0 : B == 1 ? 8448 : B == 2 ? 14848 : 19200) + ((tp & 2047) / (128 - 32 * B)) * 16 + tp * 4;
      pin(yp[B]);
    }
    mo = WS_MOD + ((lane & 3) * 1536 + 16 * (pi >> 1) + (lane >> 2)) * 16;
    mo2 = WS_MOD + ((lane & 3) * 1536 + 256 * min(lane >> 2, 2u) + cp) * 16;
    wio = WS_WINP + (((pj * 2 + (pi & 1)) * 6 + w) * 16 + (pi >> 1)) * 1024 + lane * 16;
    bwo = WS_BWP + (((pj * 2 + (pi & 1)) * 6 + w) * 16 + (pi >> 1)) * 128 + (lane & 31) * 4;
    bwo = lane < 32 ? bwo : two ? bwo + 8192 : OOB;
    pin(mo); pin(mo2); pin(wio); pin(bwo);
  }
  STAMP(23);
  {
    f2 a[8];
#pragma unroll
    for (int e = 0; e < 4; ++e) { a[2 * e] = f2{bflo(sr0[e]), bflo(sr1[e])}; a[2 * e + 1] = f2{bfhi(sr0[e]), bfhi(sr1[e])}; }
    f2 s = ((a[0] + a[1]) + (a[2] + a[3])) + ((a[4] + a[5]) + (a[6] + a[7]));
    float t = rowsum(sw32sum(s.x, s.y));
    t = sw16sum(t, t) * (1.f / 512);
    auto mm = __builtin_amdgcn_permlane32_swap(__float_as_uint(t), __float_as_uint(t), false, false);
    const f2 m = {__uint_as_float(mm[0]), __uint_as_float(mm[1])};
    s = f2{0.f, 0.f};
#pragma unroll
    for (int e = 0; e < 8; ++e) { const f2 u = a[e] - m; s = __builtin_elementwise_fma(u, u, s); }
    float v = rowsum(sw32sum(s.x, s.y));
    v = sw16sum(v, v) * (1.f / 512);
    st64(rs, (lane & 31) == 0 ? WS_STAT1 + b * 64 + w * 16 + (lane >> 5) * 8 : OOB, v2u{pkbf(t, ln_den(v)), tag});
  }
  {
    const int o = WS_XG + (4 * (16 * w + (lane >> 2))) * 16384 + b * 64 + (lane & 3) * 16;
    st128(rs, o, v4u{xg0.x, tag, xg1.x, tag});
    st128s(rs, o, 16384, v4u{xg0.y, tag, xg1.y, tag});
    st128s(rs, o, 32768, v4u{xg0.z, tag, xg1.z, tag});
    st128s(rs, o, 49152, v4u{xg0.w, tag, xg1.w, tag});
  }
  CFENCE();
  STAMP(22);
  {
    f2 a[4];
#pragma unroll
    for (int n = 0; n < 4; ++n) {
      const float c = bflo(cv[n]);
      const float s = rbf(c * __builtin_amdgcn_rcpf(1.f + __builtin_amdgcn_exp2f(-1.4426950408889634f * c)));
#pragma unroll
      for (int e = 0; e < 4; ++e) a[e] = n ? a[e] + bf2f(wm[n][e]) * s : bf2f(wm[n][e]) * s;
    }
    const float u0 = sw32sum(a[0].x, a[2].x), u1 = sw32sum(a[0].y, a[2].y), u2 = sw32sum(a[1].x, a[3].x), u3 = sw32sum(a[1].y, a[3].y);
    float t0 = sw16sum(u0, u2), t1 = sw16sum(u1, u3);
    t0 = dppadd<0x4E>(dppadd<0x124>(dppadd<0x128>(t0)));
    t1 = dppadd<0x4E>(dppadd<0x124>(dppadd<0x128>(t1)));
    st128(rs, (c16 < 2 && b < 192) ? WS_MOD + (mP * 1536 + mG * 32 + w * 8 + c16 * 4 + q4) * 16 : OOB, v4u{__float_as_uint(t0), tag, __float_as_uint(t1), tag});
  }
  CFENCE();
  STAMP(1);
  const u32 xw = 2 * (w & 1) + (w >> 1);
  const u32 pfo = (64 * pi + 8 * pj + 2 * w + ((lane >> 4) & 1)) * 4096 + ((lane >> 2) & 3) * 1024 + (lane & 3) * 128;
  u32 pf2 = 0, pf3;
  if (lane < 32) pf2 = gld<u32>(wsp, pfo);
  st128(rs, x1w, SENT);
  st128(rs, x1w + 128, SENT);
  CFENCE();
  const rsrc_t rw = __builtin_amdgcn_make_buffer_rsrc((void*)wsp, 0, 2048 * 4096, 0x00020000);
  const u32 wvo = (lane >> 3) * 4096 + (lane & 7) * 16;
  const u32 wsb[4] = {(64 * pi + 16 * xw) * 4096 + w * 1024, (64 * pi + 16 * (xw ^ 1)) * 4096 + w * 1024,
                      (64 * pi + 16 * (xw ^ 2)) * 4096 + w * 1024, (64 * pi + 16 * (xw ^ 3)) * 4096 + w * 1024};
  v4u wraw[64], wsf[16][4];
  auto rq = [&](int g) { return (g + pj) & 3; };
  auto rk = [&](int g) { return g ^ (pj & 1); };
  auto ldw = [&](int n) { wraw[n] = __builtin_amdgcn_raw_buffer_load_b128(rw, (wvo + rk(n >> 4) * 256) + ((n >> 3) & 1) * 128, wsb[(n & 7) >> 1] + (n & 1) * 32768, 0); };
  const u32 wwa = (u32)(size_t)L + L_SHF + w * 8192 + (lane >> 3) * 128 + ((lane & 7) ^ (lane >> 3)) * 16;
  auto shuf = [&](auto Z) {
    constexpr int c = Z.v >> 4, j = Z.v & 15, i = j & 7;
    if constexpr (j < 8) asm volatile("ds_write_b128 %0, %1 offset:%2" ::"v"(wwa), "a"(wraw[8 * c + i]), "i"(i * 1024) : "memory");
    else wsf[2 * c + (i & 1)][i >> 1] = *(LAS v4u*)(L + L_SHF + w * 8192 + (i >> 1) * 2048 + c16 * 128 + (((4 * (i & 1) + q4) ^ (c16 & 7)) * 16));
  };
  float kscale;
  {
    const float L2E = 1.4426950408889634f;
    const float dt = rbf(__builtin_amdgcn_exp2f(((w >> 1) ? bfhi(p_ldt) : bflo(p_ldt)) * L2E));
    const float al2 = (-rbf(__builtin_amdgcn_exp2f(bflo(p_lar) * L2E)) * dt) * L2E;
    const float rev = (bflo(p_aim) * dt) * 0.15915494309189535f;
    u32 mx = max(max(p_cr & 0x7fffu, p_ci & 0x7fffu), max(p_cr2 & 0x7fffu, p_ci2 & 0x7fffu));
    mx = max(mx, dppu<0xB1>(mx)); mx = max(mx, dppu<0x4E>(mx)); mx = max(mx, dppu<0x141>(mx)); mx = max(mx, dppu<0x140>(mx));
    { auto r = __builtin_amdgcn_permlane16_swap(mx, mx, false, false); mx = max(r[0], r[1]); }
    { auto r = __builtin_amdgcn_permlane32_swap(mx, mx, false, false); mx = max(r[0], r[1]); }
    u32 ex = mx >> 7;
    ex = min(max(ex, 1u), 253u);
    const float dn = __uint_as_float((254u - ex) << 23), up = __uint_as_float(ex << 23);
    kscale = 2.0f * dt * up;
    LAS u32* tp = (LAS u32*)(L + L_TAB + (tid >> 7) * TAB_CH + (tid & 127) * 4);
    f2 U[17], Y[16], V[8], X[31], wp[4], t[31];
    u32 pk[31];
    wp[0] = cexp_p(al2, rev, 1.f);
    Y[1] = cexp_p(al2, -rev, 16.f);
    V[1] = cexp_p(al2, rev, 256.f);
    Y[0] = f2{1.f, 0.f};
    U[0] = f2{bflo(p_cr) * dn, bflo(p_ci) * dn};
    SB();
    FORU(l, 4) {
      const int h = 1 << l;
      FORU(i, h) t[i] = cm1(U[i], wp[l]);
      if (l < 3) { t[8] = cm1(wp[l], wp[l]); t[9] = cm1(Y[h], Y[h]); }
      if (l < 2) t[10] = cm1(V[h], V[h]);
      FORU(i, h - 1) t[11 + i] = cm1(Y[1 + i], Y[h]);
      if (l < 3) { FORU(i, h - 1) t[18 + i] = cm1(V[1 + i], V[h]); }
      SB();
      FORU(i, h) U[h + i] = cm2(U[i], wp[l], t[i]);
      if (l < 3) { wp[l + 1] = cm2(wp[l], wp[l], t[8]); Y[2 * h] = cm2(Y[h], Y[h], t[9]); }
      if (l < 2) V[2 * h] = cm2(V[h], V[h], t[10]);
      FORU(i, h - 1) Y[h + 1 + i] = cm2(Y[1 + i], Y[h], t[11 + i]);
      if (l < 3) { FORU(i, h - 1) V[h + 1 + i] = cm2(V[1 + i], V[h], t[18 + i]); }
      SB();
    }
    auto xa = [](int i) { return i < 9 ? 1 : i < 19 ? 2 + (i - 9) / 5 : 4 + (i - 19) / 3; };
    auto xo = [](int i) { return i < 9 ? 2 * i : i < 19 ? 4 * ((i - 9) % 5) : 8 * ((i - 19) % 3); };
    u32 py[16], pu[16];
    t[0] = cm1(U[8], wp[3]);
    FORU(i, 16) { py[i] = pkh(Y[i]); if (i == 2) U[16] = cm2(U[8], wp[3], t[0]); if (i & 1) tp[(47 + (i >> 1)) * (TABP / 4)] = py[i >> 1]; SB(); }
    FORU(i, 16) { pu[i] = pkh(U[i]); if (i & 1) tp[(55 + (i >> 1)) * (TABP / 4)] = py[8 + (i >> 1)]; SB(); }
    FORU(i, 31) { t[i] = cm1(U[xo(i)], V[xa(i)]); if (!(i & 1)) tp[(i >> 1) * (TABP / 4)] = pu[i >> 1]; SB(); }
    FORU(i, 35) {
      if (i < 31) X[i] = cm2(U[xo(i)], V[xa(i)], t[i]);
      if (i >= 2 && i < 33) pk[i - 2] = pkh(X[i - 2]);
      if (i >= 4) tp[(12 + i) * (TABP / 4)] = pk[i - 4];
      SB();
    }
  }
  *(LAS v4u*)(L + L_PKI + w * 1024 + lane * 16) = wi0;
  if (w < 2) *(LAS v4u*)(L + L_PKI + 4096 + w * 1024 + lane * 16) = wi1;
  *(LAS v4u*)(L + L_PKW + w * 8192 + lane * 16) = wo0;
  v4u gx[4], gs[4], gm[3];
  auto s1_ld = [&](int n) {
    if (n < 4) gx[n] = ld128s(rs, lane * 16 + n * 1024, WS_XG + cp * 16384 + w * 4096);
    else if (n < 8) gs[n - 4] = ld128s(rs, lane * 16 + (n - 4) * 1024, WS_STAT1 + w * 4096);
    else gm[n - 8] = n == 8 ? ld128(rs, mo2) : ld128s(rs, mo, (n == 9 ? 768 : 1024) * 16);
  };
  FORU(n, 3) s1_ld(n);
  __syncthreads();
  STAMP(2);
  f4 kacc[4];
  v2u pl[3], ph[3];
  {
    v4u yf[8], xf[8][4];
    auto ldk = [&](int k, int i) {
      if (k < 8) { if (i < 4) xf[k][i] = *(LAS v4u*)(ka[i] + 64 * k); else yf[k] = *(LAS v4u*)(ka[4] + 64 * k); }
    };
    FORU(k, 2) FORU(i, 5) ldk(k, 4 - i);
    SB();
    FORU(k, 8) FORU(a, 4) {
      if (k) asm volatile("v_mfma_f32_16x16x32_f16 %0, %1, %2, %0" : "+v"(kacc[3 - a]) : "v"(xf[k][3 - a]), "v"(yf[k]));
      else asm volatile("v_mfma_f32_16x16x32_f16 %0, %1, %2, 0" : "=&v"(kacc[3 - a]) : "v"(xf[k][3 - a]), "v"(yf[k]));
      if (a == 0) ldk(k + 2, 4);
      ldk(k + 2, 3 - a);
      if (k < 4 && (a & 1)) s1_ld(3 + 2 * k + (a >> 1));
      if (k >= 6 && a < 3) {
        if (k == 6) pl[a] = trread(pa[a]); else ph[a] = trread(pa[a] + 128);
      }
      SB();
    }
  }
  MM_TAIL();
  f2 pv[8];
  {
    u32 P0[4], P1[4], M[4];
    FORU(a, 4) kacc[a] *= kscale;
    SB();
    FORU(a, 4) { P0[a] = pkbf_a(kacc[a].w, kacc[a].z); P1[a] = pkbf_a(kacc[a].y, kacc[a].x); M[a] = pkbf_a(kacc[a].z, kacc[a].y); }
    SB();
    FORU(a, 4) {
      lptr p0 = t0 + 640 * (3 - a), p1 = t1 + 640 * (3 - a);
      *(LAS v2u*)(p1) = v2u{P0[a], P1[a]};
      *(LAS u16*)(p0 + TOE_C + 6) = (u16)P0[a]; *(LAS u32*)(p1 + TOE_C) = M[a]; *(LAS u16*)(p1 + TOE_C + 4) = (u16)(P1[a] >> 16);
      *(LAS u32*)(p0 + 2 * TOE_C + 4) = P0[a]; *(LAS u32*)(p1 + 2 * TOE_C) = P1[a];
      *(LAS u16*)(p0 + 3 * TOE_C + 2) = (u16)P0[a]; *(LAS u32*)(p0 + 3 * TOE_C + 4) = M[a]; *(LAS u16*)(p1 + 3 * TOE_C) = (u16)(P1[a] >> 16);
      FORU(j, 2) { pv[2 * a + j] = bf2f((a & 1) ? ph[a >> 1][j] : pl[a >> 1][j]); pin(pv[2 * a + j]); }
      SB();
    }
    st128s(rs, lane * 16, b < 176 ? WS_WOP + (b * 4 + w) * 1024 : OOB, v4u{pl[2].x, pl[2].y, ph[2].x, ph[2].y});
  }
  asm volatile("" ::"v"(pf0), "v"(pf2));
  STAMP(3);
  auto s1_bad = [&]() {
    u32 mx = umax3(gm[0].y, gm[0].w, tag), mn = umin3(gm[0].y, gm[0].w, tag);
    FORU(k, 2) { mx = umax3(mx, gm[1 + k].y, gm[1 + k].w); mn = umin3(mn, gm[1 + k].y, gm[1 + k].w); }
    FORU(k, 4) {
      mx = umax3(umax3(mx, gx[k].y, gx[k].w), gs[k].y, gs[k].w);
      mn = umin3(umin3(mn, gx[k].y, gx[k].w), gs[k].y, gs[k].w);
    }
    return __any(mx != mn);
  };
  if (RARE(s1_bad())) {
#pragma nounroll
    for (int sp = 0; sp < SPIN_CAP; ++sp) {
      FORU(n, 4) { reld(gx[n], rs, lane * 16, WS_XG + cp * 16384 + w * 4096 + n * 1024); reld(gs[n], rs, lane * 16, WS_STAT1 + w * 4096 + n * 1024); }
      reld(gm[0], rs, mo2, 0); reld(gm[1], rs, mo, 768 * 16); reld(gm[2], rs, mo, 1024 * 16);
      asm volatile("s_waitcnt vmcnt(0)" : "+v"(gx[0]), "+v"(gx[1]), "+v"(gx[2]), "+v"(gx[3]), "+v"(gs[0]), "+v"(gs[1]), "+v"(gs[2]), "+v"(gs[3]), "+v"(gm[0]), "+v"(gm[1]), "+v"(gm[2])::"memory");
      CNT(29, 1);
      if (!s1_bad()) break;
    }
  }
  STAMP(4);
  u32 xv[8];
  u32 h0[4], h1[4];
  u32 gav;
  {
    float qs[6];
    f2 m[4], r[4], a[4], c[4];
    auto fill = [&]() { SB(); };
    FORU(i, 6) qs[i] = dppadd<0xB1>(__uint_as_float((i & 1) ? gm[i >> 1].z : gm[i >> 1].x));
    FORU(k, 4) { xv[2 * k] = gx[k].x; xv[2 * k + 1] = gx[k].z; r[k] = f2{bfhi(gs[k].x), bfhi(gs[k].z)}; }
    fill();
    FORU(i, 6) qs[i] = dppadd<0x4E>(qs[i]);
    FORU(k, 4) m[k] = f2{bflo(gs[k].x), bflo(gs[k].z)};
    fill();
    const u32 mv = pkbf(qs[0], qs[1]), sbp = pkbf(qs[2], qs[3]), cbp = pkbf(qs[4], qs[5]);
    FORU(k, 4) r[k] = f2{__builtin_amdgcn_rcpf(r[k].x), __builtin_amdgcn_rcpf(r[k].y)};
    fill();
    const u32 sav = __builtin_amdgcn_readlane(mv, 0), cav = __builtin_amdgcn_readlane(mv, 4);
    gav = __builtin_amdgcn_readlane(mv, 8);
    const u32 scp = pkbf(bf2f(cbp) + 1.0f);
    FORU(k, 4) { a[k] = f2{bflo(gx[k].x), bflo(gx[k].z)}; c[k] = f2{bfhi(gx[k].x), bfhi(gx[k].z)}; }
    fill();
    const u32 s1v = pkbf(bf2f(cav) + 1.0f);
    FORU(k, 4) { a[k] -= m[k]; c[k] -= m[k]; }
    const u32 scb[4] = {dppu<0x150>(scp), dppu<0x154>(scp), dppu<0x158>(scp), dppu<0x15C>(scp)};
    fill();
    FORU(k, 4) { a[k] *= r[k]; c[k] *= r[k]; }
    f2 sc[4], sb[4], t[8], bw[2], sw[2];
    FORU(j, 4) sc[j] = bf2f(scb[j]);
    fill();
    FORU(k, 4) { a[k] = a[k] * bflo(s1v) + bflo(sav); c[k] = c[k] * bfhi(s1v) + bfhi(sav); }
    FORU(j, 8) t[j] = pv[j] * sc[j & 3];
    fill();
    v4u wpk[2];
    FORU(k, 4) { h0[k] = pkbf(a[k]); h1[k] = pkbf(c[k]); }
    FORU(j, 8) wpk[j >> 2][j & 3] = pkbf(t[j]);
    fill();
    *(LAS v4u*)up = v4u{h0[0], h0[1], h0[2], h0[3]};
    *(LAS v4u*)(up + U_CH) = v4u{h1[0], h1[1], h1[2], h1[3]};
    st128(rs, wio, wpk[0]);
    st128s(rs, wio, two ? 65536 : OOB, wpk[1]);
    const u32 sbb[4] = {dppu<0x150>(sbp), dppu<0x154>(sbp), dppu<0x158>(sbp), dppu<0x15C>(sbp)};
    FORU(j, 8) t[j] = bf2f(wpk[j >> 2][j & 3]);
    SB();
    FORU(j, 4) sb[j] = bf2f(sbb[j]);
    FORU(f, 2) sw[f] = (t[4 * f] + t[4 * f + 1]) + (t[4 * f + 2] + t[4 * f + 3]);
    SB();
    FORU(j, 8) pv[j] *= sb[j & 3];
    SB();
    FORU(f, 2) bw[f] = (pv[4 * f] + pv[4 * f + 1]) + (pv[4 * f + 2] + pv[4 * f + 3]);
    const float e0 = sw16sum(bw[0].x + bw[0].y, sw[0].x + sw[0].y), e1 = sw16sum(bw[1].x + bw[1].y, sw[1].x + sw[1].y);
    __builtin_amdgcn_raw_buffer_store_b32(__float_as_uint(sw32sum(e0, e1)), rs, bwo, 0, SC1);
  }
  __syncthreads();
  STAMP(5);
  v4u cdv;
  const u32 fpo = lane < 48 ? WS_WINP + pj * 196608 + (((pi & 7) * 4 + w) * 48 + lane) * 128 : OOB;
  auto side = [&](auto S, auto I) {
    constexpr int s = S.v, i = I.v, z = 10 * s + i;
    if constexpr (z % 4 == 1 && z / 4 < 32) ldw(z / 4);
    if (z == 131) pf3 = __builtin_amdgcn_raw_buffer_load_b32(rw, lane < 32 ? pfo : OOB, 512, 0);
    if (z == 27) {
      asm volatile("s_waitcnt vmcnt(7)" ::: "memory");
      __builtin_amdgcn_raw_buffer_store_b32(tag, rs, lane == 0 ? WS_CD + b * 64 + w * 4 : OOB, 0, SC1);
    }
    if (z == 103) pf1 = __builtin_amdgcn_raw_buffer_load_b32(rs, fpo, 0, SC1);
    if (z == 163) cdv = ld128(rs, WS_CD + tid * 64);
  };
  if (w & 1) conv2<1, 2>(bp, side);
  else conv2<0, 3>(bp, side);
  STAMP(21);
  {
    f2 y[2][4], t[2][4];
    const float ga0 = bflo(gav), ga1 = bfhi(gav);
    FORU(k, 4) { t[0][k] = bf2f(h0[k]); t[1][k] = bf2f(h1[k]); }
    FORU(k, 4) { y[0][k] = f2{bflo(xv[2 * k]), bflo(xv[2 * k + 1])}; y[1][k] = f2{bfhi(xv[2 * k]), bfhi(xv[2 * k + 1])}; }
    SB();
    FORU(k, 4) { t[0][k] *= bflo(p_dsk); t[1][k] *= bfhi(p_dsk); pin(y[0][k]); pin(y[1][k]); pin(t[0][k]); pin(t[1][k]); }
    __syncthreads();
    STAMP(6);
    f4 q[4][4];
    auto yld = [&](int B) { FORU(e, 4) q[B][e] = *(LAS f4*)(yp[B] + (e >> 1) * Y_CH + (e & 1) * 16); SB(); };
    auto yadd = [&](int B) { FORU(e, 4) { t[e >> 1][2 * (e & 1)] += f2{q[B][e].x, q[B][e].y}; t[e >> 1][2 * (e & 1) + 1] += f2{q[B][e].z, q[B][e].w}; } SB(); };
    yld(0);
    if (w >= 2) {
      yld(1); yld(2);
      if (w == 3) { yld(3); yadd(3); }
      yadd(1); yadd(2);
    } else if (w == 1) { yld(1); yadd(1); }
    yadd(0);
    FORU(k, 4) { y[0][k] += t[0][k] * ga0; y[1][k] += t[1][k] * ga1; }
    SB();
    v4u o0, o1;
    FORU(k, 4) { o0[k] = pkbf(y[0][k]); o1[k] = pkbf(y[1][k]); }
    SB();
    st128(rs, x1w, san4(o0));
    st128(rs, x1w + 128, san4(o1));
  }
  CFENCE();
  asm volatile("" ::"v"(pf1), "v"(pf3));
  auto cd_bad = [&]() { return __any(umax3(umax3(cdv.x, cdv.y, cdv.z), cdv.w, tag) != umin3(umin3(cdv.x, cdv.y, cdv.z), cdv.w, tag)); };
  if (RARE(cd_bad())) {
#pragma nounroll
    for (int sp = 0; sp < SPIN_CAP; ++sp) {
      asm volatile("" ::: "memory");
      cdv = ld128(rs, WS_CD + tid * 64);
      if (!cd_bad()) break;
    }
  }
  __syncthreads();
  STAMP(7);
  const u32 blkS = 4 + (w >> 1);
  const bool hasS = blkS < nb;
  const u32 fo[4] = {((pj * 2 + 1) * 6 + w), ((pj * 2 + 1) * 6 + (hasS ? blkS : 4)), (pj * 12 + w), (pj * 12 + (hasS ? blkS : 4))};
  v4u ones = {0x3f803f80u, 0x3f803f80u, 0x3f803f80u, 0x3f803f80u};
  pin(ones);
  v4u wf[4][16];
  auto ldf = [&](int s, int k) { wf[s][k] = ld128s(rs, lane * 16 + (k & 3) * 1024, WS_WINP + fo[s] * 16384 + (k >> 2) * 4096); };
  f4 sW[4], bW[4];
  auto ldb = [&](int n) { ((n & 1) ? sW : bW)[n >> 1] = *(LAS f4*)(L + L_BW + w * 1024 + (n >> 1) * 256 + (n & 1) * 64 + q4 * 16); };
  {
    v4u xr[16];
    f4 bq[8];
    const int xo = WS_X1 + pi * 65536 + w * 16384;
    sfor<0, 40>([&](auto z) {
      constexpr int n = z.v;
      if constexpr (n < 8) bq[n] = __builtin_bit_cast(f4, ld128s(rs, lane * 16 + (n & 1) * 1024, WS_BWP + fo[n >> 1] * 2048));
      else if constexpr (n < 16) ldf(n & 1, (n - 8) >> 1);
      else if constexpr (n < 32) xr[n - 16] = ld128s(rs, lane * 16 + (n & 3) * 1024, xo + rq((n - 16) >> 2) * 4096);
      else ldf(n & 1, (n - 24) >> 1);
      if constexpr (n < 32) { shuf(IC<2 * n>{}); shuf(IC<2 * n + 1>{}); }
      SB();
      if constexpr (n == 31) {
        FORU(i, 4) bq[i] = bq[2 * i] + bq[2 * i + 1];
        SB();
        f4 u, v;
        FORU(e, 4) { u[e] = sw32sum(bq[0][e], bq[2][e]); v[e] = sw32sum(bq[1][e], bq[3][e]); }
        FORU(e, 4) u[e] = sw16sum(u[e], v[e]);
        FORU(e, 4) u[e] = dppadd<0x128>(u[e]);
        *(LAS f4*)(L + L_BW + w * 1024 + lane * 16) = u;
        SB();
      }
    });
    STAMP(8);
    lptr xi = L + L_XIMG + w * 16384 + (lane >> 3) * 128 + (lane & 1) * 16;
    const u32 xs0 = (((lane & 7) >> 1) ^ ((lane >> 4) & 1)) * 32;
    u32 m = 0;
    FORU(k, 16) { *(LAS v4u*)(xi + rq(k >> 2) * 4096 + (k & 3) * 1024 + (xs0 ^ ((k & 1) * 64))) = xr[k]; m = vmax(m, xr[k]); ldf(k & 1, 8 + (k >> 1)); SB(); }
    if (RARE(__any(m == ~0u))) {
#pragma nounroll
      for (int sp = 0; sp < SPIN_CAP; ++sp) {
        asm volatile("" ::: "memory");
        FORU(k, 16) xr[k] = ld128s(rs, lane * 16, xo + rq(k >> 2) * 4096 + (k & 3) * 1024);
        SB();
        m = 0;
        FORU(k, 16) { *(LAS v4u*)(xi + rq(k >> 2) * 4096 + (k & 3) * 1024 + (xs0 ^ ((k & 1) * 64))) = xr[k]; m = vmax(m, xr[k]); SB(); }
        CNT(29, 100);
        if (!__any(m == ~0u)) break;
      }
    }
    STAMP(9);
  }
  __syncthreads();
  STAMP(10);
  const u32 xsw = ((c16 >> 3) & 1) | ((q4 & 1) << 1), xbo = (8 * q4 + (c16 >> 2)) * 128 + 8 * (c16 & 3);
  lptr xb[4];
#pragma unroll
  for (int t = 0; t < 4; ++t) xb[t] = L + xbo + ((t ^ xw ^ xsw) * 32);
  v2u bl[3][4], bh[3][4];
  auto ldx = [&](int k, int i) {
    if (k < 16) { if (i & 1) bh[k % 3][i >> 1] = trread(xb[i >> 1] + 4096 * k + 512); else bl[k % 3][i >> 1] = trread(xb[i >> 1] + 4096 * k); }
  };
  auto bx = [&](int k, int t) { return v4u{bl[k % 3][t].x, bl[k % 3][t].y, bh[k % 3][t].x, bh[k % 3][t].y}; };
  auto xwait = [&](int k) { asm volatile("" ::"v"(bh[k % 3][3])); };
  const int pco = WS_PB + ((pi * 4 + w) * 44 + blk0) * 512, pc2 = 128 + lane < 32 * nb ? lane * 16 : OOB;
  const int zco = WS_Z2N + ((pj * 6 + (w >> 1)) * 64 + 2 * pi + (w & 1)) * 1024, zc2 = w + 8 < 2 * nb ? zco + 262144 : OOB;
  f4 z2a[6];
  f2 rn[4];
  {
    f4 st0, st1;
    v2u sl[16], sh[16];
    FORU(k, 4) { sl[k] = trread(xb[0] + 4096 * k); sh[k] = trread(xb[0] + 4096 * k + 512); }
    MM_HEAD();
    sfor<0, 16>([&](auto z) {
      constexpr int k = z.v;
      const v4u v = {sl[k].x, sl[k].y, sh[k].x, sh[k].y};
      if (k) m_vv(st0, ones, v); else m_vv0(st0, ones, v);
      if constexpr (k + 4 < 16) sl[k + 4] = trread(xb[0] + 4096 * (k + 4)); else { ldx((k - 12) >> 1, 4 * (k & 1)); ldx((k - 12) >> 1, 4 * (k & 1) + 1); }
      SB();
      if (k) m_vv(st1, v, v); else m_vv0(st1, v, v);
      if constexpr (k + 4 < 16) sh[k + 4] = trread(xb[0] + 4096 * (k + 4) + 512); else { ldx((k - 12) >> 1, 4 * (k & 1) + 2); ldx((k - 12) >> 1, 4 * (k & 1) + 3); }
      if (k == 1) st128s(rs, lane * 16, pco, SENT);
      if (k == 5) st128s(rs, lane * 16, pco + 1024, SENT);
      if (k == 9) st128s(rs, pc2, pco + 2048, SENT);
      if (k == 3) st128s(rs, lane * 16, zco, SENT);
      if (k == 7) st128s(rs, lane * 16, zco + 131072, SENT);
      if (k == 11) st128s(rs, lane * 16, zc2, SENT);
      SB();
    });
    float sv0 = 0.f, sv1 = 0.f;
    FORU(k, 16) FORU(i, 6) {
      const int u = i == 0 ? 0 : i == 1 ? 4 : i == 2 ? 1 : i == 3 ? 5 : i - 2;
      if (i == 0) xwait(k);
      if (k) m_vv(z2a[u], wf[u >> 2][k], bx(k, u & 3)); else m_vv0(z2a[u], wf[u >> 2][k], bx(k, u & 3));
      if (i < 2) { ldx(k + 2, 2 * i); ldx(k + 2, 2 * i + 1); } else ldx(k + 2, i + 2);
      if (i == 2 && k) ldf(2 + ((k - 1) & 1), (k - 1) >> 1);
      if (i == 4 && (k & 1)) ldw(32 + (k >> 1));
      if (i == 3 || i == 5) {
        const int z = 2 * k + (i == 5);
        if (z == 4) sv1 = (c16 & 1) ? st1.y : st1.x;
        if (z == 5) sv0 = (c16 & 1) ? st1.w : st1.z;
        if (z == 6) sv1 = (c16 & 2) ? sv0 : sv1;
        if (z == 7) { pin(st0); sv0 = st0.x * (1.f / 512); }
        if (z == 8) sv1 *= (1.f / 512);
        if (z == 9) sv1 = fmaxf(__builtin_fmaf(-sv0, sv0, sv1), 0.f);
        if (z == 10) sv1 = rbf(sv1);
        if (z == 11) sv0 = rbf(sv0);
        if (z == 12) sv1 = rbf(sv1 + 1e-5f);
        if (z == 13) sv1 = __builtin_amdgcn_sqrtf(sv1);
        if (z == 14) sv1 = rbf(sv1);
        if (z == 15) sv1 = __builtin_amdgcn_rcpf(sv1);
        if (z == 16) sv0 *= -sv1;
        if (z == 17) *(volatile LAS f2*)(L + (q4 == (c16 >> 2) ? L_XST + (16 * xw + c16) * 8 : L_XST + 512 + lane * 8)) = f2{sv1, sv0};
        if (z >= 4 && z <= 16) { pin(sv0); pin(sv1); }
        if (z >= 30) { ldb(2 * z - 60); ldb(2 * z - 59); }
        if (z == 24) __builtin_amdgcn_s_barrier();
        if (z >= 26 && z < 30) rn[z - 26] = *(volatile LAS f2*)(L + L_XST + (16 * ((z - 26) ^ xw) + c16) * 8);
      }
      SB();
    }
    MM_TAIL();
  }
  STAMP(24);
  auto shuf2 = [&](auto C, auto N) {
    constexpr int n = N.v % 12, z = 16 * (C.v + N.v / 12);
    if constexpr (N.v < 24) { if constexpr (n < 8) shuf(IC<z + n>{}); else { shuf(IC<z + 2 * n - 8>{}); shuf(IC<z + 2 * n - 7>{}); } }
  };
  auto epi = [&](f4 (&a)[6], int s0, auto fill) {
    f2 v[12];
    sfor<0, 2>([&](auto z) {
      FORU(j, 6) {
        const int i = 6 * z.v + j;
        const f4 &sw = sW[s0 + (i >> 3)], &bw = bW[s0 + (i >> 3)];
        v[i] = ((i & 1) ? f2{sw.z, sw.w} : f2{sw.x, sw.y}) * rn[(i >> 1) & 3].y + ((i & 1) ? f2{bw.z, bw.w} : f2{bw.x, bw.y});
      }
      fill(IC<z.v>{});
    });
    sfor<0, 2>([&](auto z) {
      FORU(j, 6) { const int i = 6 * z.v + j; v[i] = ((i & 1) ? f2{a[i >> 1].z, a[i >> 1].w} : f2{a[i >> 1].x, a[i >> 1].y}) * rn[(i >> 1) & 3].x + v[i]; }
      fill(IC<2 + z.v>{});
    });
    gelu_n<12>(v, fill);
    FORU(i, 6) { a[i] = f4{v[2 * i].x, v[2 * i].y, v[2 * i + 1].x, v[2 * i + 1].y}; pin(a[i]); }
  };
  {
    epi(z2a, 0, [&](auto N) {
      constexpr int n = N.v;
      SB();
      if constexpr ((n & 1) && n < 22) ldf(2 + ((15 + n / 2) & 1), (15 + n / 2) >> 1);
      if constexpr (!(n & 1) && n < 24) ldw(40 + n / 2);
      SB();
    });
    if (!hasS) { z2a[4] = f4{0.f, 0.f, 0.f, 0.f}; z2a[5] = z2a[4]; }
    f2 zs[6], zq[6];
    FORU(i, 6) { zs[i] = f2{z2a[i].x, z2a[i].y} + f2{z2a[i].z, z2a[i].w}; zq[i] = f2{z2a[i].x, z2a[i].y} * f2{z2a[i].x, z2a[i].y}; }
    SB();
    FORU(i, 6) zq[i] = __builtin_elementwise_fma(f2{z2a[i].z, z2a[i].w}, f2{z2a[i].z, z2a[i].w}, zq[i]);
    FORU(i, 2) zs[i] += zs[4 + i];
    SB();
    float us[4], uq[4], u[4];
    FORU(i, 2) zq[i] += zq[4 + i];
    FORU(t, 4) us[t] = zs[t].x + zs[t].y;
    SB();
    FORU(t, 4) uq[3 - t] = zq[3 - t].x + zq[3 - t].y;
    SB();
    FORU(t, 4) u[3 - t] = sw32sum(us[3 - t], uq[3 - t]);
    LAS float* zp = (LAS float*)(L + L_ZST) + (w * 2 + (lane >> 5)) * 64 + c16;
    zp[16 * ((2 * (q4 & 1) + 1) ^ xw)] = sw16sum(u[1], u[3]);
    zp[16 * ((2 * (q4 & 1)) ^ xw)] = sw16sum(u[0], u[2]);
  }
  asm volatile("s_waitcnt vmcnt(20)" ::: "memory");
  __syncthreads();
  if (w == 0) {
    LAS float* zp = (LAS float*)(L + L_ZST) + lane;
    const float s = (zp[0] + zp[128]) + (zp[256] + zp[384]), ss = (zp[64] + zp[192]) + (zp[320] + zp[448]);
    st128(rs, WS_ST2 + ((pi * 8 + pj) * 64 + lane) * 16, v4u{__float_as_uint(s), tag, __float_as_uint(ss), tag});
  }
  __builtin_amdgcn_raw_buffer_store_b32(tag, rs, tid == 64 ? WS_F3 + (pj * 32 + pi) * 128 : OOB, 0, SC1);
  CFENCE();
  STAMP(11);
  v4u wo[6][4];
  const u32 wob = WS_WOP + (pj * 22 + 6 * w) * 4096, wobh = ssel(w < 3, wob, OOB);
  auto ldo = [&](int n) { wo[n >> 2][n & 3] = ld128s(rs, lane * 16 + (n & 3) * 1024, (n < 16 ? wob : wobh) + (n >> 2) * 4096); };
  f4 z1a[6];
  v2u xres[4];
  v4u g[8];
  u32 fv;
  auto f_ld = [&]() { fv = __builtin_amdgcn_raw_buffer_load_b32(rs, (lane & 7) * 128, WS_F3 + (pj * 32 + 8 * w) * 128, SC1); };
  auto s2b_ld = [&](int n) { g[n] = ld128s(rs, (2 * q4 * 64 + c16) * 16 + (n & 1) * 1024, WS_ST2 + pi * 8192 + ((n >> 1) ^ xw) * 256); };
  {
    FORU(i, 16) ldx(i >> 3, i & 7);
    MM_HEAD();
    sfor<0, 96>([&](auto z) {
      constexpr int k = z.v / 6, i = z.v % 6, u = i == 0 ? 0 : i == 1 ? 4 : i == 2 ? 1 : i == 3 ? 5 : i - 2;
      if (i == 0) xwait(k);
      if (k) m_vv(z1a[u], wf[2 + (u >> 2)][k], bx(k, u & 3)); else m_vv0(z1a[u], wf[2 + (u >> 2)][k], bx(k, u & 3));
      if (i < 2) { ldx(k + 2, 2 * i); ldx(k + 2, 2 * i + 1); } else ldx(k + 2, i + 2);
      if (k < 3 && (i == 2 || i == 4)) ldf(1 + (i >> 1), 13 + k);
      if (k >= 3 && k < 9 && i == 3) ldw(49 + k);
      if ((k == 9 || k == 10) && i >= 2) s2b_ld(4 * (k - 9) + i - 2);
      if (k >= 11 && k < 14 && (i == 2 || i == 4)) ldw(58 + 2 * (k - 11) + (i >> 2));
      if (k == 14 && i >= 2) ldb(2 + i);
      if (k == 15 && i == 3) f_ld();
      SB();
    });
    MM_TAIL();
#pragma unroll
    for (int n = 0; n < 4; ++n) {
      const u32 row = 16 * n + 4 * q4 + (c16 >> 2);
      xres[n] = trread(L + L_XIMG + pj * 8192 + row * 128 + ((w ^ (((row >> 1) & 1) | (((row >> 3) & 1) << 1))) * 32) + 8 * (c16 & 3));
    }
  }
  lptr twa[6], tra[4];
  {
    lptr tw = L + L_TRS + w * TRS_W;
    FORU(i, 6) { twa[i] = tw + (16 * ((i & 3) ^ xw) + c16) * 64 + 8 * (((i & 4) + q4) ^ (c16 >> 1)); pin(twa[i]); }
    const u32 r0 = 8 * q4 + (c16 >> 2);
    FORU(i, 4) {
      tra[i] = tw + ((i & 2) ? (w & 1) * 2048 : 0) + r0 * 64 + (i & 1) * 256 + 8 * ((2 * (i & 2) + (c16 & 3)) ^ (((r0 + 4 * (i & 1)) >> 1) & 7));
      pin(tra[i]);
    }
  }
  STAMP(12);
  {
    auto bad = [&]() { u32 mx = tag, mn = tag; FORU(n, 8) { mx = umax3(mx, g[n].y, g[n].w); mn = umin3(mn, g[n].y, g[n].w); } return __any(mx != mn); };
    if (RARE(bad())) {
#pragma nounroll
      for (int sp = 0; sp < SPIN_CAP; ++sp) {
        FORU(n, 8) reld(g[n], rs, (2 * q4 * 64 + c16) * 16, WS_ST2 + pi * 8192 + ((n >> 1) ^ xw) * 256 + (n & 1) * 1024);
        asm volatile("s_waitcnt vmcnt(0)" : "+v"(g[0]), "+v"(g[1]), "+v"(g[2]), "+v"(g[3]), "+v"(g[4]), "+v"(g[5]), "+v"(g[6]), "+v"(g[7])::"memory");
        CNT(29, 10000);
        if (!bad()) break;
      }
    }
    STAMP(13);
    float a[4], c[4];
    FORU(t, 4) { a[t] = __uint_as_float(g[2 * t].x) + __uint_as_float(g[2 * t + 1].x); c[t] = __uint_as_float(g[2 * t].z) + __uint_as_float(g[2 * t + 1].z); }
    SB();
    const float a0 = sw32sum(a[0], a[2]), a1 = sw32sum(a[1], a[3]), c0 = sw32sum(c[0], c[2]), c1 = sw32sum(c[1], c[3]);
    const float m = sw16sum(a0, a1) * (1.f / 688), var = fmaxf(sw16sum(c0, c1) * (1.f / 688) - m * m, 0.f);
    const float r = __builtin_amdgcn_rcpf(ln_den(var)), n0 = -rbf(m) * r;
    auto gather = [&](float v, float (&o)[4]) {
      auto e = __builtin_amdgcn_permlane16_swap(__float_as_uint(v), __float_as_uint(v), false, false);
      auto e0 = __builtin_amdgcn_permlane32_swap(e[0], e[0], false, false);
      auto e1 = __builtin_amdgcn_permlane32_swap(e[1], e[1], false, false);
      o[0] = __uint_as_float(e0[0]); o[2] = __uint_as_float(e0[1]); o[1] = __uint_as_float(e1[0]); o[3] = __uint_as_float(e1[1]);
    };
    gather(r, a); gather(n0, c);
    SB();
    FORU(i, 6) z2a[i] = z2a[i] * a[i & 3] + c[i & 3];
    SB();
    v2u zp[6], zl[3], zh[3];
    FORU(i, 6) zp[i] = v2u{pkbf(z2a[i].x, z2a[i].y), pkbf(z2a[i].z, z2a[i].w)};
    SB();
    FORU(i, 6) *(LAS v2u*)twa[i] = zp[i];
    FORU(n, 3) { zl[n] = trread(tra[n & 2] + (n & 1) * 2048); zh[n] = trread(tra[(n & 2) + 1] + (n & 1) * 2048); }
    SB();
    const u32 zo = WS_Z2N + (pj * 6 * 64 + 2 * pi) * 1024;
    FORU(n, 3) st128s(rs, lane * 16, n < 2 ? zo + w * 65536 + n * 1024 : (hasS ? zo + blkS * 65536 + (w & 1) * 1024 : OOB), san4(v4u{zl[n].x, zl[n].y, zh[n].x, zh[n].y}));
  }
  CFENCE();
  STAMP(14);
  u32 bsr[4], gbp[2];
  v4u gg[2];
  FORU(t, 4) bsr[t] = gld<u16>(bsp, (64 * pi + 16 * (t ^ xw) + c16) * 2);
  FORU(m, 2) gg[m] = ld128(rs, WS_MOD + ((lane & 3) * 1536 + 1280 + 32 * pj + 16 * m + 8 * (c16 >> 3) + 2 * q4 + ((c16 >> 2) & 1)) * 16);
  if (RARE(__any(fv != tag))) {
#pragma nounroll
    for (int sp = 0; sp < SPIN_CAP; ++sp) {
      asm volatile("" ::: "memory");
      f_ld();
      if (!__any(fv != tag)) break;
    }
  }
  CFENCE();
  v2u z1[6];
  f4 ga[2][4], q[20];
  lptr qa[6];
  auto ldq = [&](int n) { q[n] = *(volatile LAS f4*)(n < 12 ? qa[n & 3] + (n >> 2) * 4096 : qa[4 + (n & 1)] + ((n - 12) >> 1) * 4096); };
  {
    const u32 zo = WS_Z2N + (pj * 6 * 64 + 16 * w) * 1024;
    const u32 rot = ((nb == 6 ? 0x1420u : 0x1320u) >> (4 * (pi & 3))) & 15;
    v4u zr[16];
    u32 zv[4];
    FORU(g, 4) zv[g] = lane * 16 + rk(g) * 4096;
    u32 so[6], rsl[6];
    FORU(it, 5) {
      const u32 blk = smod(w + 1 + smod((it > 1 ? it + nb - 6 : it) + rot, nb - 1), nb);
      so[it] = zo + blk * 65536;
      rsl[it + 1] = ssel(blk < 4, blk * 3 + ((w - blk - 1) & 3), 4 * blk - 4 + w) * 4096;
    }
    so[5] = zo + w * 65536;
    auto ldz = [&](int k, u32 s0) { zr[k] = ld128s(rs, zv[k >> 2] + (k & 3) * 1024, s0); };
    FORU(n, 7) ldo(n);
    epi(z1a, 2, [&](auto N) {
      constexpr int n = N.v;
      SB();
      shuf2(IC<4>{}, N);
      if constexpr (n < 5) ldo(7 + n);
      if constexpr (n >= 6 && n < 22) ldz(n - 6, so[0]);
      SB();
    });
    FORU(i, 6) { z1[i] = v2u{pkbf(z1a[i].x, z1a[i].y), pkbf(z1a[i].z, z1a[i].w)}; pin(z1[i]); }
    FORU(m, 2) { gbp[m] = pkbf(quadsum(__uint_as_float(gg[m].x)), quadsum(__uint_as_float(gg[m].z))); pin(gbp[m]); }
    STAMP(25);
    __syncthreads();
    {
      auto bad = [&]() { u32 m = 0; FORU(k, 16) m = vmax(m, zr[k]); return __any(m == ~0u); };
      if (RARE(bad())) {
#pragma nounroll
        for (int sp = 0; sp < SPIN_CAP; ++sp) {
          asm volatile("" ::: "memory");
          FORU(k, 16) ldz(k, so[0]);
          CNT(28, 100);
          if (!bad()) break;
        }
      }
    }
    STAMP(15);
    u32 ra = 0, badm = 0;
    float ck = 0.f;
    v4u oth;
    MM_HEAD();
    auto slot = [&](auto z) {
      constexpr int it = z.v / 64, k = (z.v / 4) % 16, t = z.v % 4, h = it & 1;
      if (k) m_va(ga[h][t], zr[k], wsf[k][t]); else m_va0(ga[h][t], zr[k], wsf[k][t]);
      constexpr int pz = z.v / 4 - 1, pit = pz / 16, pk = pz % 16;
      if constexpr (t == 1 && pz >= 0) {
        if constexpr (pit < 5) ldz(pk, so[pit + 1]);
        else if constexpr (pk < 6) ldo(12 + pk);
      }
      if constexpr (it == 0 && t != 1 && 3 * k + t - (t > 0) < 32) shuf(IC<96 + 3 * k + t - (t > 0)>{});
      if constexpr (it > 0) {
        if (k == 2 && t == 0) { ck = ga[h ^ 1][0].x + ga[h ^ 1][0].y; pin(ck); }
        if (k == 3 && t == 0) { ck += ga[h ^ 1][0].z; pin(ck); }
        if (k == 4 && t == 0) { ck += ga[h ^ 1][0].w; pin(ck); }
        if (k == 5 && t == 0) badm |= __any(ck != ck) ? 1u << (it - 1) : 0u;
        if (k >= 2 && k < 6 && t == 2) { ra = rsl[it] + lane * 16 + (((k - 2) ^ xw) * 1024); pin(ra); }
        if constexpr (k >= 2 && k < 6 && t == 3) *(volatile LAS f4*)(L + L_RED + ra) = ga[h ^ 1][k - 2];
      }
      if constexpr (it == 5) {
        if constexpr (z.v >= 320 + 28 && z.v < 320 + 34) {
          constexpr int i = z.v - 348;
          u32 l16 = lane * 16;
          pin(l16);
          qa[i] = L + l16 + (L_RED + ((i < 4 ? w * 12 : (blkS * 4 - 4) * 4) + ((i & 3) ^ xw)) * 1024);
          pin(qa[i]);
        }
        if (k == 6 && t == 2) *(volatile LAS u32*)(L + L_XST + w * 4) = badm;
        if (k == 8 && t == 3) { asm volatile("s_waitcnt lgkmcnt(0)" ::: "memory"); __builtin_amdgcn_s_barrier(); }
        if (k == 9 && t == 0) oth = *(volatile LAS v4u*)(L + L_XST);
        if constexpr (z.v >= 320 + 37 && z.v < 320 + 57) ldq(z.v - 357);
      }
      SB();
    };
    sfor<0, 64>(slot);
    if (nb == 6) sfor<64, 128>(slot);
    else {
      MM_TAIL();
      const float t = (ga[0][0].x + ga[0][0].y) + (ga[0][0].z + ga[0][0].w);
      badm = __any(t != t) ? 1u : 0u;
      FORU(t, 4) { *(volatile LAS f4*)(L + L_RED + rsl[1] + lane * 16 + ((t ^ xw) * 1024)) = ga[0][t]; ga[1][t] = f4{0.f, 0.f, 0.f, 0.f}; }
      MM_HEAD();
    }
    sfor<128, 384>(slot);
    MM_TAIL();
    { const float t = (ga[1][0].x + ga[1][0].y) + (ga[1][0].z + ga[1][0].w); badm |= __any(t != t) ? 32u : 0u; }
    const u32 anyo = __builtin_amdgcn_readfirstlane(oth.x | oth.y | oth.z | oth.w);
    if (RARE(badm | anyo)) {
#pragma nounroll
      for (u32 it = 0; it < 6; ++it) {
        if (!((badm >> it) & 1)) continue;
        CNT(28, 1);
        const u32 s0 = it == 0 ? so[0] : it == 1 ? so[1] : it == 2 ? so[2] : it == 3 ? so[3] : it == 4 ? so[4] : so[5];
        const u32 r0 = it == 0 ? rsl[1] : it == 1 ? rsl[2] : it == 2 ? rsl[3] : it == 3 ? rsl[4] : rsl[5];
#pragma nounroll
        for (int sp = 0; sp < SPIN_CAP; ++sp) {
          u32 m = 0;
          FORU(k, 16) ldz(k, s0);
          FORU(k, 16) m = vmax(m, zr[k]);
          asm volatile("" ::: "memory");
          if (!__any(m == ~0u)) break;
        }
        MM_HEAD();
        FORU(k, 16) FORU(t, 4) { if (k) m_va(ga[0][t], zr[k], wsf[k][t]); else m_va0(ga[0][t], zr[k], wsf[k][t]); }
        MM_TAIL();
        FORU(t, 4) {
          if (it < 5) *(LAS f4*)(L + L_RED + r0 + lane * 16 + ((t ^ xw) * 1024)) = ga[0][t];
          else ga[1][t] = ga[0][t];
        }
      }
      if (anyo) __syncthreads();
      FORU(n, 20) ldq(n);
    }
  }
  __builtin_amdgcn_raw_buffer_store_b32(tag, rs, lane == 0 ? 0 : OOB, w == 0 ? WS_H4 + (pi * 8 + pj) * 128 : OOB, SC1);
  STAMP(26);
  {
    const u32 lane = fresh_lane(), c16 = lane & 15, q4 = lane >> 4;
    f2 zf[12], gs2[12];
    auto half = [](const f4& v, int hh) { return hh ? f2{v.z, v.w} : f2{v.x, v.y}; };
    FORU(i, 12) gs2[i] = i < 8 ? half(q[i >> 1], i & 1) + half(q[4 + (i >> 1)], i & 1) : half(q[8 + (i >> 1)], i & 1) + half(q[10 + (i >> 1)], i & 1);
    FORU(i, 12) zf[i] = i < 8 ? half(ga[1][i >> 1], i & 1) + half(q[8 + (i >> 1)], i & 1) : half(q[12 + (i >> 1)], i & 1) + half(q[14 + (i >> 1)], i & 1);
    SB();
    FORU(i, 12) gs2[i] += bflo(bsr[(i >> 1) & 3]);
    SB();
    FORU(i, 12) gs2[i] += zf[i];
    SB();
    FORU(i, 12) zf[i] = bf2f(z1[i >> 1][i & 1]);
    SB();
    FORU(i, 12) gs2[i] *= zf[i];
    SB();
    v2u P[6];
    FORU(i, 12) P[i >> 1][i & 1] = pkbf(gs2[i]);
    SB();
#pragma unroll
    for (int h = 0; h < 3; ++h) {
      auto rx = __builtin_amdgcn_permlane16_swap(P[2 * h].x, P[2 * h + 1].x, false, false);
      auto ry = __builtin_amdgcn_permlane16_swap(P[2 * h].y, P[2 * h + 1].y, false, false);
      const u32 tt = (2 * (h & 1) + (q4 & 1)) ^ xw;
      st128s(rs, WS_PB + ((pi * 4 + tt) * 44 + blk0) * 512 + (q4 >> 1) * 256 + c16 * 16, h < 2 ? w * 512 : (hasS ? blkS * 512 : OOB),
             san4(v4u{rx[0], ry[0], rx[1], ry[1]}));
    }
    FORU(n, 6) ldo(18 + n);
  }
  STAMP(17);
  {
    const u32 lane = fresh_lane(), c16 = lane & 15, q4 = lane >> 4;
    f4 oa[2][4];
    v4u pr[4][6];
    u32 pb[4], pbh[4];
    u32 rd[3];
    FORU(u, 4) {
      const u32 d = u < 3 ? 1 + smod(u + smod(pj & 3, 3), 3) : 4;
      pb[u] = WS_PB + ((pi * 4 + ((w + d) & 3)) * 44 + 12 * w) * 512; pbh[u] = ssel(w < 3, pb[u] + 4096, OOB);
      if (u < 3) rd[u] = L_RED2 + (((w + d) & 3) * 3 + 3 - d) * 4096 + lane * 16;
    }
    const u32 pv3 = (w == 3 && lane >= 32) ? OOB : lane * 16 + 3072;
    auto ldp = [&](int n) {
      const int u = n / 6, k = n % 6;
      pr[u][k] = ld128s(rs, k == 3 ? pv3 : lane * 16 + (k & 3) * 1024, k < 4 ? pb[u] : pbh[u]);
    };
#pragma nounroll
    for (int sp = 0; sp < SPIN_CAP; ++sp) {
      asm volatile("" ::: "memory");
      if (!__any(__builtin_amdgcn_raw_buffer_load_b32(rs, (lane & 7) * 128, WS_H4 + pi * 1024, SC1) != tag)) break;
      CNT(28, 10000);
      __builtin_amdgcn_s_sleep(2);
    }
    STAMP(18);
    FORU(n, 12) { ldp(n); SB(); }
    u32 bad = 0;
    MM_HEAD();
    sfor<0, 96>([&](auto z) {
      constexpr int u = z.v / 24, k = (z.v / 4) % 6, n = z.v % 4;
      if (k) m_vv(oa[u & 1][n], wo[k][n], pr[u][k]); else m_vv0(oa[u & 1][n], wo[k][n], pr[u][k]);
      if constexpr (n == 1 && z.v / 4 < 12) ldp(12 + z.v / 4);
      if constexpr (u > 0 && k == 2 && n == 0) bad |= __any(oa[(u & 1) ^ 1][0].x != oa[(u & 1) ^ 1][0].x) ? 1u << (u - 1) : 0u;
      if constexpr (u > 0 && k >= 2 && n == 2) *(LAS f4*)(L + rd[u - 1] + (k - 2) * 1024) = oa[(u & 1) ^ 1][k - 2];
      SB();
    });
    MM_TAIL();
    bad |= __any(oa[1][0].x != oa[1][0].x) ? 8u : 0u;
    if (RARE(bad)) {
#pragma nounroll
      for (u32 u = 0; u < 4; ++u) {
        if (!((bad >> u) & 1)) continue;
        CNT(28, 1000000);
        pb[0] = u == 0 ? pb[0] : u == 1 ? pb[1] : u == 2 ? pb[2] : pb[3];
        pbh[0] = u == 0 ? pbh[0] : u == 1 ? pbh[1] : u == 2 ? pbh[2] : pbh[3];
#pragma nounroll
        for (int sp = 0; sp < SPIN_CAP; ++sp) {
          u32 m = 0;
          asm volatile("" ::: "memory");
          FORU(k, 6) ldp(k);
          FORU(k, 6) m = vmax(m, pr[0][k]);
          if (!__any(m == ~0u)) break;
        }
        asm volatile("fk_text_b:");
        MM_HEAD();
        FORU(k, 6) FORU(n, 4) { if (k) m_vv(oa[0][n], wo[k][n], pr[0][k]); else m_vv0(oa[0][n], wo[k][n], pr[0][k]); }
        MM_TAIL();
        FORU(n, 4) {
          if (u < 3) *(LAS f4*)(L + (u == 0 ? rd[0] : u == 1 ? rd[1] : rd[2]) + n * 1024) = oa[0][n];
          else oa[1][n] = oa[0][n];
        }
      }
    }
    __syncthreads();
    f4 o[4];
    FORU(n, 4) {
      lptr p = L + L_RED2 + (w * 12 + n) * 1024 + lane * 16;
      o[n] = (oa[1][n] + *(LAS f4*)p) + (*(LAS f4*)(p + 4096) + *(LAS f4*)(p + 8192));
    }
    STAMP(19);
    v2u O[4];
#pragma unroll
    for (int n = 0; n < 4; ++n) {
      const u32 g0 = (n & 1) ? dppu<0x158>(gbp[n >> 1]) : dppu<0x150>(gbp[n >> 1]), g1 = (n & 1) ? dppu<0x15C>(gbp[n >> 1]) : dppu<0x154>(gbp[n >> 1]);
      O[n] = v2u{pkbf(bf2f(xres[n].x) + bf2f(g0) * f2{o[n].x, o[n].y}), pkbf(bf2f(xres[n].y) + bf2f(g1) * f2{o[n].z, o[n].w})};
    }
#pragma unroll
    for (int h = 0; h < 2; ++h) {
      auto rx = __builtin_amdgcn_permlane16_swap(O[2 * h].x, O[2 * h + 1].x, false, false);
      auto ry = __builtin_amdgcn_permlane16_swap(O[2 * h].y, O[2 * h + 1].y, false, false);
      *(v4u*)(out + (64 * pi + 16 * w + c16) * 512 + 64 * pj + 16 * (2 * h + (q4 & 1)) + 8 * (q4 >> 1)) = v4u{rx[0], ry[0], rx[1], ry[1]};
    }
  }
  if (b == 0 && w == 0) __builtin_amdgcn_raw_buffer_store_b32(tag, rs, WS_E, 0, SC1);
  STAMP(20);
  STAMP_FLUSH();
}

extern "C" void kernel_launch(void* const* d_in, const int* in_sizes, int n_in, void* d_out, int out_size, void* d_ws, size_t ws_size,
                              hipStream_t stream) {
  (void)in_sizes; (void)n_in; (void)out_size; (void)ws_size;
  (void)hipFuncSetAttribute((const void*)fused_kernel, hipFuncAttributeMaxDynamicSharedMemorySize, LDS_BYTES);
  fused_kernel<<<256, 256, LDS_BYTES, stream>>>((const u16*)d_in[0], (const u16*)d_in[1], (const u16*)d_in[2], (const u16*)d_in[3],
                                                (const u16*)d_in[4], (const u16*)d_in[5], (const u16*)d_in[6], (const u16*)d_in[8],
                                                (const u16*)d_in[9], (const u16*)d_in[10], (const u16*)d_in[11], (const u16*)d_in[12],
                                                (const u16*)d_in[13], (u16*)d_out, (unsigned char*)d_ws);
}
```

```cpp
#include <hip/hip_runtime.h>
#include <stdint.h>

typedef unsigned u32;
typedef unsigned short u16;
typedef unsigned long long u64;
typedef u32 v4u __attribute__((ext_vector_type(4)));
typedef u32 v2u __attribute__((ext_vector_type(2)));
typedef float f4 __attribute__((ext_vector_type(4)));
typedef float f2 __attribute__((ext_vector_type(2)));
typedef __bf16 bf8 __attribute__((ext_vector_type(8)));
typedef __bf16 bf2 __attribute__((ext_vector_type(2)));
typedef _Float16 h8 __attribute__((ext_vector_type(8)));
typedef _Float16 h2 __attribute__((ext_vector_type(2)));
typedef short s4 __attribute__((ext_vector_type(4)));
#define LAS __attribute__((address_space(3)))
#define GAS __attribute__((address_space(1)))
#define CAS __attribute__((address_space(4)))
#define DI __device__ __forceinline__
typedef __amdgpu_buffer_rsrc_t rsrc_t;
typedef LAS unsigned char* lptr;

#define XMAP 2

constexpr int WS_E = 0, WS_STAT1 = 4096, WS_CD = 32768, WS_ST2 = 65536, WS_F3 = 327680, WS_H4 = 360448, WS_MOD = 524288, WS_BWP = 655360;
constexpr int WS_XG = 1 << 20, WS_X1 = 5 << 20, WS_Z2N = 7 << 20, WS_PB = 10 << 20, WS_WINP = 13 << 20, WS_WOP = 29 << 19;
constexpr int WS_DBG = 24 << 20, WS_END = 25 << 20;
constexpr int OOB = 0x40000000;
constexpr int TABP = 544, TAB_CH = 63 * TABP;
constexpr int L_TAB = 0, L_Y = 0, Y_CH = 21504;
constexpr int L_PKW = 75776, L_SHF = 81920;
constexpr int L_PKI = 68544;
constexpr int L_TOE = 108544, TOE_C = 5312, TOE_CH = 4 * TOE_C;
constexpr int L_U = 151040, U_CH = 6320;
constexpr int L_XIMG = 0, L_TRS = 65536, TRS_W = 4096;
constexpr int L_RED = 0, L_RED2 = 81920;
constexpr int L_XST = 155648, L_ZST = L_XST + 2048, L_BW = L_ZST + 2048;
constexpr int LDS_BYTES = 163840;
constexpr int SC1 = 16;
constexpr int SPIN_CAP = 1 << 15;
static_assert(LDS_BYTES <= 163840, "LDS");

DI float bflo(u32 v) { return __uint_as_float(v << 16); }
DI float bfhi(u32 v) { return __uint_as_float(v & 0xffff0000u); }
DI f2 bf2f(u32 v) { return f2{bflo(v), bfhi(v)}; }
DI u32 pkbf(float a, float b) { f2 v = {a, b}; return __builtin_bit_cast(u32, __builtin_convertvector(v, bf2)); }
DI u32 pkbf(f2 v) { return __builtin_bit_cast(u32, __builtin_convertvector(v, bf2)); }
DI u32 pkbf_a(float a, float b) { u32 r; asm("v_cvt_pk_bf16_f32 %0, %1, %2" : "=v"(r) : "v"(a), "v"(b)); return r; }
DI u32 pkh(f2 v) { return __builtin_bit_cast(u32, __builtin_convertvector(v, h2)); }
DI float rbf(float a) { return bflo(pkbf(a, a)); }
DI u32 san(u32 v) { return v < 0xFFFFFFFEu ? v : 0xFFFFFFFEu; }
DI v4u san4(v4u v) { return v4u{san(v.x), san(v.y), san(v.z), san(v.w)}; }
DI u32 umax3(u32 a, u32 b, u32 c) { return max(max(a, b), c); }
DI u32 umin3(u32 a, u32 b, u32 c) { return min(min(a, b), c); }
DI u32 vmax(u32 m, v4u v) { return umax3(umax3(m, v.x, v.y), v.z, v.w); }
DI v4u ld128(rsrc_t rs, int off) { return __builtin_amdgcn_raw_buffer_load_b128(rs, off, 0, SC1); }
DI v4u ld128s(rsrc_t rs, int off, int soff) { return __builtin_amdgcn_raw_buffer_load_b128(rs, off, soff, SC1); }
DI void st128(rsrc_t rs, int off, v4u v) { __builtin_amdgcn_raw_buffer_store_b128(v, rs, off, 0, SC1); }
DI void st128s(rsrc_t rs, int off, int soff, v4u v) { __builtin_amdgcn_raw_buffer_store_b128(v, rs, off, soff, SC1); }
DI void st64(rsrc_t rs, int off, v2u v) { __builtin_amdgcn_raw_buffer_store_b64(v, rs, off, 0, SC1); }
DI void reld(v4u& v, rsrc_t rs, u32 voff, u32 soff) {
  asm volatile("s_nop 4\n\tbuffer_load_dwordx4 %0, %1, %2, %3 offen sc1" : "+v"(v) : "v"(voff), "s"(rs), "s"(soff) : "memory");
}
#define RARE(c) __builtin_expect(!!(c), 0)
#define DRAIN() asm volatile("s_waitcnt vmcnt(0)" ::: "memory")
#define SB() __builtin_amdgcn_sched_barrier(0)
#define CFENCE() do { asm volatile("" ::: "memory"); SB(); } while (0)
template <int C> DI u32 dppu(u32 v) { return (u32)__builtin_amdgcn_update_dpp(0, (int)v, C, 0xF, 0xF, true); }
template <int C> DI float dppadd(float v) { return v + __uint_as_float(dppu<C>(__float_as_uint(v))); }
DI float sw16sum(float a, float b) { auto r = __builtin_amdgcn_permlane16_swap(__float_as_uint(a), __float_as_uint(b), false, false); return __uint_as_float(r[0]) + __uint_as_float(r[1]); }
DI float sw32sum(float a, float b) { auto r = __builtin_amdgcn_permlane32_swap(__float_as_uint(a), __float_as_uint(b), false, false); return __uint_as_float(r[0]) + __uint_as_float(r[1]); }
DI float quadsum(float v) { v = dppadd<0xB1>(v); return dppadd<0x4E>(v); }
DI float rowsum(float v) { v = quadsum(v); v = dppadd<0x141>(v); return dppadd<0x140>(v); }
DI f4 mfma_bf(v4u a, v4u b, f4 c) { return __builtin_amdgcn_mfma_f32_16x16x32_bf16(__builtin_bit_cast(bf8, a), __builtin_bit_cast(bf8, b), c, 0, 0, 0); }
DI f4 mfma_h(v4u a, v4u b, f4 c) { return __builtin_amdgcn_mfma_f32_16x16x32_f16(__builtin_bit_cast(h8, a), __builtin_bit_cast(h8, b), c, 0, 0, 0); }
DI v2u trread(lptr p) { return __builtin_bit_cast(v2u, __builtin_amdgcn_ds_read_tr16_b64_v4i16((LAS s4*)p)); }
template <int N> struct IC { static constexpr int v = N; };
template <int I, int N, class F> DI void sfor(F f) { if constexpr (I < N) { f(IC<I>{}); sfor<I + 1, N>(f); } }
#define FORU(i, n) _Pragma("unroll") for (int i = 0; i < (n); ++i)
DI f2 cm1(f2 a, f2 b) { f2 t; asm("v_pk_mul_f32 %0, %1, %2 op_sel:[0,0] op_sel_hi:[0,1]" : "=v"(t) : "v"(a), "v"(b)); return t; }
DI f2 cm2(f2 a, f2 b, f2 t) { f2 r; asm("v_pk_fma_f32 %0, %1, %2, %3 op_sel:[1,1,0] op_sel_hi:[1,0,1] neg_lo:[0,1,0]" : "=v"(r) : "v"(a), "v"(b), "v"(t)); return r; }
DI f2 cexp_p(float al2, float rev, float p) {
  float m = __builtin_amdgcn_exp2f(al2 * p), f = __builtin_amdgcn_fractf(rev * p);
  return f2{m * __builtin_amdgcn_cosf(f), m * __builtin_amdgcn_sinf(f)};
}
template <int N, class F> DI void gelu_n(f2 (&v)[N], F fill) {
  const float c0 = -2.0f * 0.7978845608028654f * 1.4426950408889634f, c1 = c0 * 0.044715f;
  f2 a[N];
  sfor<0, 2>([&](auto z) { FORU(i, 6) a[6 * z.v + i] = v[6 * z.v + i] * v[6 * z.v + i]; fill(IC<4 + z.v>{}); });
  sfor<0, 2>([&](auto z) { FORU(i, 6) a[6 * z.v + i] = __builtin_elementwise_fma(a[6 * z.v + i], f2{c1, c1}, f2{c0, c0}); fill(IC<6 + z.v>{}); });
  sfor<0, 2>([&](auto z) { FORU(i, 6) a[6 * z.v + i] *= v[6 * z.v + i]; fill(IC<8 + z.v>{}); });
  sfor<0, 6>([&](auto z) { FORU(i, 2) a[2 * z.v + i] = f2{__builtin_amdgcn_exp2f(a[2 * z.v + i].x), __builtin_amdgcn_exp2f(a[2 * z.v + i].y)}; fill(IC<10 + z.v>{}); });
  sfor<0, 2>([&](auto z) { FORU(i, 6) a[6 * z.v + i] += 1.0f; fill(IC<16 + z.v>{}); });
  sfor<0, 6>([&](auto z) { FORU(i, 2) a[2 * z.v + i] = f2{__builtin_amdgcn_rcpf(a[2 * z.v + i].x), __builtin_amdgcn_rcpf(a[2 * z.v + i].y)}; fill(IC<18 + z.v>{}); });
  sfor<0, 2>([&](auto z) { FORU(i, 6) v[6 * z.v + i] *= a[6 * z.v + i]; fill(IC<24 + z.v>{}); });
}
DI float ln_den(float var) { return rbf(__builtin_amdgcn_sqrtf(rbf(rbf(var) + 1e-5f))); }
template <class T> DI T gld(const void* base, u32 off) { return *(const GAS T*)((const GAS char*)base + off); }
#define MF "v_mfma_f32_16x16x32_bf16 "
DI void m_av(f4& c, v4u a, v4u b) { asm volatile(MF "%0, %1, %2, %0" : "+v"(c) : "a"(a), "v"(b)); }
DI void m_va(f4& c, v4u a, v4u b) { asm volatile(MF "%0, %1, %2, %0" : "+v"(c) : "v"(a), "a"(b)); }
DI void m_vv(f4& c, v4u a, v4u b) { asm volatile(MF "%0, %1, %2, %0" : "+v"(c) : "v"(a), "v"(b)); }
DI void m_av0(f4& c, v4u a, v4u b) { asm volatile(MF "%0, %1, %2, 0" : "=&v"(c) : "a"(a), "v"(b)); }
DI void m_va0(f4& c, v4u a, v4u b) { asm volatile(MF "%0, %1, %2, 0" : "=&v"(c) : "v"(a), "a"(b)); }
DI void m_vv0(f4& c, v4u a, v4u b) { asm volatile(MF "%0, %1, %2, 0" : "=&v"(c) : "v"(a), "v"(b)); }
DI v4u aget(v4u a) {
  v4u r;
  _Pragma("unroll") for (int e = 0; e < 4; ++e) { u32 t; asm volatile("v_accvgpr_read_b32 %0, %1" : "=v"(t) : "a"(a[e])); r[e] = t; }
  return r;
}
DI u32 ssel(bool c, u32 a, u32 b) { asm("" : "+s"(a)); asm("" : "+s"(b)); return c ? a : b; }
DI u32 smod(u32 v, u32 m) { u32 t = v - m; asm("" : "+s"(t)); return (int)t >= 0 ? t : v; }
DI u32 fresh_lane() { u32 l; asm volatile("v_mbcnt_lo_u32_b32 %0, -1, 0\n\tv_mbcnt_hi_u32_b32 %0, -1, %0" : "=v"(l)); return l; }
template <class T> DI void pin(T& v) { asm volatile("" : "+v"(v)); }
#define MM_HEAD() do { SB(); asm volatile("s_nop 7"); SB(); } while (0)
#define MM_TAIL() do { SB(); asm volatile("s_nop 7"); SB(); } while (0)
DI int toe_addr(int pos) { return 2 * pos + 8 * (pos >> 4); }

#define STAMP(n)
#define STAMP_FLUSH()
#define STAMP_INIT()
#define CNT(n,v)

template <int BAND> struct Band {
  static constexpr int R = 128 - 32 * BAND, NT = R / 16, NS = 15 + NT / 2;
  static constexpr int YO = BAND == 0 ? 0 : BAND == 1 ? 8448 : BAND == 2 ? 14848 : 19200;
  lptr al, ah, bb, yp;
  f4 acc[NT];
  v2u lo[32], hi[32];
  v4u bf[NS];
  DI void load(int s, int i) {
    if (i < 4) {
      const int n = 2 * s + (i >> 1);
      if (n < 32) { if (i & 1) hi[n] = *(LAS v2u*)(ah + 40 * (31 - n)); else lo[n] = *(LAS v2u*)(al + 40 * (31 - n)); }
    } else if (s < NS) bf[s] = *(LAS v4u*)(bb + 80 * (NT / 2 - 1 - s + 15));
  }
  DI void mma(int s, int t) {
    const int n = t - NT + 2 + 2 * s;
    if (s < NS && n >= 0 && n < 32) {
      if (n < 2) m_vv0(acc[t], v4u{lo[n].x, lo[n].y, hi[n].x, hi[n].y}, bf[s]); else m_vv(acc[t], v4u{lo[n].x, lo[n].y, hi[n].x, hi[n].y}, bf[s]);
    }
  }
  DI void flush(int s, int t) {
    const int n = t - NT + 2 + 2 * (s - 1);
    if (n == 30 || n == 31) *(LAS f4*)(yp + 64 * t) = acc[t];
  }
};
template <int B1, int B2, class F> DI void conv2(const lptr (&bp)[8], F side) {
  Band<B1> u; Band<B2> v;
  u.al = bp[0]; u.ah = bp[1]; u.bb = bp[2]; u.yp = bp[3]; v.al = bp[4]; v.ah = bp[5]; v.bb = bp[6]; v.yp = bp[7];
  constexpr int N1 = Band<B1>::NT, NS = Band<B1>::NS;
#pragma unroll
  for (int s = 0; s < 2; ++s)
#pragma unroll
    for (int i = 0; i < 5; ++i) { u.load(s, i); v.load(s, i); SB(); }
  sfor<0, 10 * (NS + 1)>([&](auto z) {
    constexpr int s = z.v / 10, i = z.v % 10;
    if (s >= 17 && i == 0) asm volatile("s_nop %0" ::"i"(s == NS ? 7 : s == NS - 1 ? 5 : 2));
    if (i < N1) { u.mma(s, i); u.flush(s, i); } else { v.mma(s, i - N1); v.flush(s, i - N1); }
    if (i < 5) u.load(s + 2, i); else v.load(s + 2, i - 5);
    side(IC<s>{}, IC<i>{});
    SB();
  });
}

__global__ void __launch_bounds__(256, 1)
fused_kernel(const u16* __restrict__ x, const u16* __restrict__ lar, const u16* __restrict__ aim, const u16* __restrict__ cre,
             const u16* __restrict__ cim, const u16* __restrict__ ldt, const u16* __restrict__ dsk, const u16* __restrict__ win,
             const u16* __restrict__ wsp, const u16* __restrict__ bsp, const u16* __restrict__ wout, const u16* __restrict__ cvec,
             const u16* __restrict__ wmod, u16* __restrict__ out, unsigned char* ws) {
  extern __shared__ __attribute__((aligned(16))) unsigned char smem[];
  lptr L = (lptr)smem;
  const u32 b = blockIdx.x, tid = threadIdx.x, lane = tid & 63;
  STAMP_INIT();
  STAMP(0);
  const u32 w = __builtin_amdgcn_readfirstlane(tid >> 6);
  const u32 xc = b & 7, rr = b >> 3, cp = xc * 32 + rr;
  const u32 c16 = lane & 15, q4 = lane >> 4;
  u32 pf0 = 0, pf1 = 0;
  {
    u64 pc;
    u32 len;
    asm volatile("s_getpc_b64 %0\nfk_text_a:\n\ts_mov_b32 %1, fk_text_b-fk_text_a" : "=s"(pc), "=s"(len));
    const u32 o = (rr * 16 + tid) * 128;
    if (tid < 16 && o < len) pf0 = gld<u32>((const void*)pc, o);
  }
  CFENCE();
  const u32 tag = *(const CAS u32*)(ws + WS_E) + 1u;
  const u32 tb = 512 * (b >> 6) + 32 * (b & 15) + 2 * ((b >> 4) & 3);
  const v4u sr0 = gld<v4u>(x, (tb + 8 * w) * 1024 + lane * 16), sr1 = gld<v4u>(x, (tb + 8 * w) * 1024 + 1024 + lane * 16);
  const u32 xgo = (tb + 8 * (lane & 3)) * 1024 + (16 * w + (lane >> 2)) * 16;
  const v4u xg0 = gld<v4u>(x, xgo), xg1 = gld<v4u>(x, xgo + 1024);
  const u32 mG = b % 48, mP = min(b / 48, 3u), mrow = mP * 128 + (lane >> 1);
  v4u wm[4];
  u32 cv[4];
#pragma unroll
  for (int n = 0; n < 4; ++n) wm[n] = gld<v4u>(wmod, (mrow + 32 * n) * 6144 + mG * 128 + w * 32 + (lane & 1) * 16);
#pragma unroll
  for (int n = 0; n < 4; ++n) cv[n] = gld<u16>(cvec, (mrow + 32 * n) * 2);
  const u32 pidx = (cp * 256 + tid) * 2;
  const u32 p_lar = gld<u16>(lar, pidx), p_aim = gld<u16>(aim, pidx), p_cr = gld<u16>(cre, pidx), p_ci = gld<u16>(cim, pidx);
  const u32 p_cr2 = gld<u16>(cre, pidx ^ 128), p_ci2 = gld<u16>(cim, pidx ^ 128);
  const u32 p_ldt = *(const CAS u32*)(ldt + 2 * cp), p_dsk = *(const CAS u32*)(dsk + 2 * cp);
  CFENCE();
  const u32 pi = (xc >> 1) * 8 + (rr & 7), pj = (xc & 1) * 4 + (rr >> 3);
  const u32 nb = pj < 3 ? 6 : 5, blk0 = 5 * pj + min(pj, 3u);
  const bool two = w + 4 < nb;
  const rsrc_t rs = __builtin_amdgcn_make_buffer_rsrc((void*)ws, 0, WS_END, 0x00020000);
  const v4u SENT = {~0u, ~0u, ~0u, ~0u};
  const int x1w = WS_X1 + ((tid >> 3) * 512 + 2 * cp) * 128 + (tid & 7) * 16;
  const u32 wio_src = ((32 * (pi >> 1) + (lane >> 1)) * 1376 + (pi & 1) * 688 + 16 * (blk0 + w)) * 2 + (lane & 1) * 16;
  const v4u wi0 = gld<v4u>(win, wio_src), wi1 = gld<v4u>(win, two ? wio_src + 128 : wio_src);
  const rsrc_t ro = __builtin_amdgcn_make_buffer_rsrc((void*)wout, 0, 688 * 1024, 0x00020000);
  const v4u wo0 = __builtin_amdgcn_raw_buffer_load_b128(ro, (lane >> 1) * 1024 + (lane & 1) * 16, b < 176 ? (b % 22) * 32768 + (b / 22) * 128 + w * 32 : OOB, 0);
  CFENCE();
  if (tid < 40) *(LAS v4u*)(L + L_TOE + (tid / 5) * TOE_C + 5120 + (tid % 5) * 16) = v4u{0, 0, 0, 0};
  if (tid < 150) *(LAS v4u*)(L + L_U + (tid / 75) * U_CH + (tid % 75) * 16) = v4u{0, 0, 0, 0};
  lptr ka[5], pa[3], t0, t1, up, bp[8], yp[4];
  int mo, mo2, wio, bwo;
  {
    const u32 hi = w & 1, ch = w >> 1;
    lptr tb_ = L + L_TAB + ch * TAB_CH + 16 * q4;
    const u32 i2 = (c16 + ((c16 >> 1) & 1)) >> 1, i4 = (c16 + 1 + ((c16 >> 2) & 1)) >> 2, i8 = (c16 + 3 + ((c16 >> 3) & 1)) >> 3;
    const u32 row[5] = {hi ? 35 + i8 : c16, hi ? 38 + i8 : 16 + i2, hi ? 41 + i8 : 25 + i4, hi ? 44 + i8 : 30 + i4, 47 + c16};
    FORU(i, 5) { ka[i] = tb_ + row[i] * TABP; pin(ka[i]); }
    FORU(a, 3) { pa[a] = L + (a == 0 ? L_PKI + w * 1024 : a == 1 ? L_PKI + 4096 + hi * 1024 : L_PKW + w * 8192) + (8 * q4 + (c16 >> 2)) * 32 + 8 * (c16 & 3); pin(pa[a]); }
    const int i0 = 2044 - (1024 * hi + 768 + 16 * c16 + 4 * q4);
    t0 = L + L_TOE + ch * TOE_CH + toe_addr(i0); t1 = L + L_TOE + ch * TOE_CH + toe_addr(i0 + 4);
    up = L + L_U + 80 * (15 + (tid >> 2)) + 16 * (tid & 3);
    pin(t0); pin(t1); pin(up);
    FORU(n, 2) {
      const u32 B = n ? 3 - hi : hi, R = 128 - 32 * B, c = (3 - c16) & 3, P = 2048 - 512 * B - 496 - 4 * (c16 >> 2) + 8 * q4;
      bp[4 * n] = L + L_TOE + ch * TOE_CH + c * TOE_C + toe_addr(P);
      bp[4 * n + 1] = L + L_TOE + ch * TOE_CH + c * TOE_C + toe_addr(P + 4);
      bp[4 * n + 2] = L + L_U + ch * U_CH + 80 * (4 - B) * c16 + 16 * q4;
      bp[4 * n + 3] = L + L_Y + ch * Y_CH + (n ? (hi ? 14848 : 19200) : (hi ? 8448 : 0)) + c16 * (4 * R + 16) + 16 * q4;
    }
    FORU(n, 8) pin(bp[n]);
    FORU(B, 4) {
      const u32 tp = 8 * tid - 512 * B;
      yp[B] = L + L_Y + (B == 0 ? 0 : B == 1 ? 8448 : B == 2 ? 14848 : 19200) + ((tp & 2047) / (128 - 32 * B)) * 16 + tp * 4;
      pin(yp[B]);
    }
    mo = WS_MOD + ((lane & 3) * 1536 + 16 * (pi >> 1) + (lane >> 2)) * 16;
    mo2 = WS_MOD + ((lane & 3) * 1536 + 256 * min(lane >> 2, 2u) + cp) * 16;
    wio = WS_WINP + (((pj * 2 + (pi & 1)) * 6 + w) * 16 + (pi >> 1)) * 1024 + lane * 16;
    bwo = WS_BWP + (((pj * 2 + (pi & 1)) * 6 + w) * 16 + (pi >> 1)) * 128 + (lane & 31) * 4;
    bwo = lane < 32 ? bwo : two ? bwo + 8192 : OOB;
    pin(mo); pin(mo2); pin(wio); pin(bwo);
  }
  STAMP(23);
  {
    f2 a[8];
#pragma unroll
    for (int e = 0; e < 4; ++e) { a[2 * e] = f2{bflo(sr0[e]), bflo(sr1[e])}; a[2 * e + 1] = f2{bfhi(sr0[e]), bfhi(sr1[e])}; }
    f2 s = ((a[0] + a[1]) + (a[2] + a[3])) + ((a[4] + a[5]) + (a[6] + a[7]));
    float t = rowsum(sw32sum(s.x, s.y));
    t = sw16sum(t, t) * (1.f / 512);
    auto mm = __builtin_amdgcn_permlane32_swap(__float_as_uint(t), __float_as_uint(t), false, false);
    const f2 m = {__uint_as_float(mm[0]), __uint_as_float(mm[1])};
    s = f2{0.f, 0.f};
#pragma unroll
    for (int e = 0; e < 8; ++e) { const f2 u = a[e] - m; s = __builtin_elementwise_fma(u, u, s); }
    float v = rowsum(sw32sum(s.x, s.y));
    v = sw16sum(v, v) * (1.f / 512);
    st64(rs, (lane & 31) == 0 ? WS_STAT1 + b * 64 + w * 16 + (lane >> 5) * 8 : OOB, v2u{pkbf(t, ln_den(v)), tag});
  }
  {
    const int o = WS_XG + (4 * (16 * w + (lane >> 2))) * 16384 + b * 64 + (lane & 3) * 16;
    st128(rs, o, v4u{xg0.x, tag, xg1.x, tag});
    st128s(rs, o, 16384, v4u{xg0.y, tag, xg1.y, tag});
    st128s(rs, o, 32768, v4u{xg0.z, tag, xg1.z, tag});
    st128s(rs, o, 49152, v4u{xg0.w, tag, xg1.w, tag});
  }
  CFENCE();
  STAMP(22);
  {
    f2 a[4];
#pragma unroll
    for (int n = 0; n < 4; ++n) {
      const float c = bflo(cv[n]);
      const float s = rbf(c * __builtin_amdgcn_rcpf(1.f + __builtin_amdgcn_exp2f(-1.4426950408889634f * c)));
#pragma unroll
      for (int e = 0; e < 4; ++e) a[e] = n ? a[e] + bf2f(wm[n][e]) * s : bf2f(wm[n][e]) * s;
    }
    const float u0 = sw32sum(a[0].x, a[2].x), u1 = sw32sum(a[0].y, a[2].y), u2 = sw32sum(a[1].x, a[3].x), u3 = sw32sum(a[1].y, a[3].y);
    float t0 = sw16sum(u0, u2), t1 = sw16sum(u1, u3);
    t0 = dppadd<0x4E>(dppadd<0x124>(dppadd<0x128>(t0)));
    t1 = dppadd<0x4E>(dppadd<0x124>(dppadd<0x128>(t1)));
    st128(rs, (c16 < 2 && b < 192) ? WS_MOD + (mP * 1536 + mG * 32 + w * 8 + c16 * 4 + q4) * 16 : OOB, v4u{__float_as_uint(t0), tag, __float_as_uint(t1), tag});
  }
  CFENCE();
  STAMP(1);
  const u32 xw = 2 * (w & 1) + (w >> 1);
  const u32 pfo = (64 * pi + 8 * pj + 2 * w + ((lane >> 4) & 1)) * 4096 + ((lane >> 2) & 3) * 1024 + (lane & 3) * 128;
  u32 pf2 = 0, pf3;
  if (lane < 32) pf2 = gld<u32>(wsp, pfo);
  st128(rs, x1w, SENT);
  st128(rs, x1w + 128, SENT);
  CFENCE();
  const rsrc_t rw = __builtin_amdgcn_make_buffer_rsrc((void*)wsp, 0, 2048 * 4096, 0x00020000);
  const u32 wvo = (lane >> 3) * 4096 + (lane & 7) * 16;
  const u32 wsb[4] = {(64 * pi + 16 * xw) * 4096 + w * 1024, (64 * pi + 16 * (xw ^ 1)) * 4096 + w * 1024,
                      (64 * pi + 16 * (xw ^ 2)) * 4096 + w * 1024, (64 * pi + 16 * (xw ^ 3)) * 4096 + w * 1024};
  v4u wraw[64], wsf[16][4];
  auto rq = [&](int g) { return (g + pj) & 3; };
  auto rk = [&](int g) { return g ^ (pj & 1); };
  auto ldw = [&](int n) { wraw[n] = __builtin_amdgcn_raw_buffer_load_b128(rw, (wvo + rk(n >> 4) * 256) + ((n >> 3) & 1) * 128, wsb[(n & 7) >> 1] + (n & 1) * 32768, 0); };
  const u32 wwa = (u32)(size_t)L + L_SHF + w * 8192 + (lane >> 3) * 128 + ((lane & 7) ^ (lane >> 3)) * 16;
  auto shuf = [&](auto Z) {
    constexpr int c = Z.v >> 4, j = Z.v & 15, i = j & 7;
    if constexpr (j < 8) asm volatile("ds_write_b128 %0, %1 offset:%2" ::"v"(wwa), "a"(wraw[8 * c + i]), "i"(i * 1024) : "memory");
    else wsf[2 * c + (i & 1)][i >> 1] = *(LAS v4u*)(L + L_SHF + w * 8192 + (i >> 1) * 2048 + c16 * 128 + (((4 * (i & 1) + q4) ^ (c16 & 7)) * 16));
  };
  float kscale;
  {
    const float L2E = 1.4426950408889634f;
    const float dt = rbf(__builtin_amdgcn_exp2f(((w >> 1) ? bfhi(p_ldt) : bflo(p_ldt)) * L2E));
    const float al2 = (-rbf(__builtin_amdgcn_exp2f(bflo(p_lar) * L2E)) * dt) * L2E;
    const float rev = (bflo(p_aim) * dt) * 0.15915494309189535f;
    u32 mx = max(max(p_cr & 0x7fffu, p_ci & 0x7fffu), max(p_cr2 & 0x7fffu, p_ci2 & 0x7fffu));
    mx = max(mx, dppu<0xB1>(mx)); mx = max(mx, dppu<0x4E>(mx)); mx = max(mx, dppu<0x141>(mx)); mx = max(mx, dppu<0x140>(mx));
    { auto r = __builtin_amdgcn_permlane16_swap(mx, mx, false, false); mx = max(r[0], r[1]); }
    { auto r = __builtin_amdgcn_permlane32_swap(mx, mx, false, false); mx = max(r[0], r[1]); }
    u32 ex = mx >> 7;
    ex = min(max(ex, 1u), 253u);
    const float dn = __uint_as_float((254u - ex) << 23), up = __uint_as_float(ex << 23);
    kscale = 2.0f * dt * up;
    LAS u32* tp = (LAS u32*)(L + L_TAB + (tid >> 7) * TAB_CH + (tid & 127) * 4);
    f2 U[17], Y[16], V[8], X[31], wp[4], t[31];
    u32 pk[31];
    wp[0] = cexp_p(al2, rev, 1.f);
    Y[1] = cexp_p(al2, -rev, 16.f);
    V[1] = cexp_p(al2, rev, 256.f);
    Y[0] = f2{1.f, 0.f};
    U[0] = f2{bflo(p_cr) * dn, bflo(p_ci) * dn};
    SB();
    FORU(l, 4) {
      const int h = 1 << l;
      FORU(i, h) t[i] = cm1(U[i], wp[l]);
      if (l < 3) { t[8] = cm1(wp[l], wp[l]); t[9] = cm1(Y[h], Y[h]); }
      if (l < 2) t[10] = cm1(V[h], V[h]);
      FORU(i, h - 1) t[11 + i] = cm1(Y[1 + i], Y[h]);
      if (l < 3) { FORU(i, h - 1) t[18 + i] = cm1(V[1 + i], V[h]); }
      SB();
      FORU(i, h) U[h + i] = cm2(U[i], wp[l], t[i]);
      if (l < 3) { wp[l + 1] = cm2(wp[l], wp[l], t[8]); Y[2 * h] = cm2(Y[h], Y[h], t[9]); }
      if (l < 2) V[2 * h] = cm2(V[h], V[h], t[10]);
      FORU(i, h - 1) Y[h + 1 + i] = cm2(Y[1 + i], Y[h], t[11 + i]);
      if (l < 3) { FORU(i, h - 1) V[h + 1 + i] = cm2(V[1 + i], V[h], t[18 + i]); }
      SB();
    }
    auto xa = [](int i) { return i < 9 ? 1 : i < 19 ? 2 + (i - 9) / 5 : 4 + (i - 19) / 3; };
    auto xo = [](int i) { return i < 9 ? 2 * i : i < 19 ? 4 * ((i - 9) % 5) : 8 * ((i - 19) % 3); };
    u32 py[16], pu[16];
    t[0] = cm1(U[8], wp[3]);
    FORU(i, 16) { py[i] = pkh(Y[i]); if (i == 2) U[16] = cm2(U[8], wp[3], t[0]); if (i & 1) tp[(47 + (i >> 1)) * (TABP / 4)] = py[i >> 1]; SB(); }
    FORU(i, 16) { pu[i] = pkh(U[i]); if (i & 1) tp[(55 + (i >> 1)) * (TABP / 4)] = py[8 + (i >> 1)]; SB(); }
    FORU(i, 31) { t[i] = cm1(U[xo(i)], V[xa(i)]); if (!(i & 1)) tp[(i >> 1) * (TABP / 4)] = pu[i >> 1]; SB(); }
    FORU(i, 35) {
      if (i < 31) X[i] = cm2(U[xo(i)], V[xa(i)], t[i]);
      if (i >= 2 && i < 33) pk[i - 2] = pkh(X[i - 2]);
      if (i >= 4) tp[(12 + i) * (TABP / 4)] = pk[i - 4];
      SB();
    }
  }
  *(LAS v4u*)(L + L_PKI + w * 1024 + lane * 16) = wi0;
  if (w < 2) *(LAS v4u*)(L + L_PKI + 4096 + w * 1024 + lane * 16) = wi1;
  *(LAS v4u*)(L + L_PKW + w * 8192 + lane * 16) = wo0;
  v4u gx[4], gs[4], gm[3];
  auto s1_ld = [&](int n) {
    if (n < 4) gx[n] = ld128s(rs, lane * 16 + n * 1024, WS_XG + cp * 16384 + w * 4096);
    else if (n < 8) gs[n - 4] = ld128s(rs, lane * 16 + (n - 4) * 1024, WS_STAT1 + w * 4096);
    else gm[n - 8] = n == 8 ? ld128(rs, mo2) : ld128s(rs, mo, (n == 9 ? 768 : 1024) * 16);
  };
  FORU(n, 3) s1_ld(n);
  __syncthreads();
  STAMP(2);
  f4 kacc[4];
  v2u pl[3], ph[3];
  {
    v4u yf[8], xf[8][4];
    auto ldk = [&](int k, int i) {
      if (k < 8) { if (i < 4) xf[k][i] = *(LAS v4u*)(ka[i] + 64 * k); else yf[k] = *(LAS v4u*)(ka[4] + 64 * k); }
    };
    FORU(k, 2) FORU(i, 5) ldk(k, 4 - i);
    SB();
    FORU(k, 8) FORU(a, 4) {
      if (k) asm volatile("v_mfma_f32_16x16x32_f16 %0, %1, %2, %0" : "+v"(kacc[3 - a]) : "v"(xf[k][3 - a]), "v"(yf[k]));
      else asm volatile("v_mfma_f32_16x16x32_f16 %0, %1, %2, 0" : "=&v"(kacc[3 - a]) : "v"(xf[k][3 - a]), "v"(yf[k]));
      if (a == 0) ldk(k + 2, 4);
      ldk(k + 2, 3 - a);
      if (k < 4 && (a & 1)) s1_ld(3 + 2 * k + (a >> 1));
      if (k >= 6 && a < 3) {
        if (k == 6) pl[a] = trread(pa[a]); else ph[a] = trread(pa[a] + 128);
      }
      SB();
    }
  }
  MM_TAIL();
  f2 pv[8];
  {
    u32 P0[4], P1[4], M[4];
    FORU(a, 4) kacc[a] *= kscale;
    SB();
    FORU(a, 4) { P0[a] = pkbf_a(kacc[a].w, kacc[a].z); P1[a] = pkbf_a(kacc[a].y, kacc[a].x); M[a] = pkbf_a(kacc[a].z, kacc[a].y); }
    SB();
    FORU(a, 4) {
      lptr p0 = t0 + 640 * (3 - a), p1 = t1 + 640 * (3 - a);
      *(LAS v2u*)(p1) = v2u{P0[a], P1[a]};
      *(LAS u16*)(p0 + TOE_C + 6) = (u16)P0[a]; *(LAS u32*)(p1 + TOE_C) = M[a]; *(LAS u16*)(p1 + TOE_C + 4) = (u16)(P1[a] >> 16);
      *(LAS u32*)(p0 + 2 * TOE_C + 4) = P0[a]; *(LAS u32*)(p1 + 2 * TOE_C) = P1[a];
      *(LAS u16*)(p0 + 3 * TOE_C + 2) = (u16)P0[a]; *(LAS u32*)(p0 + 3 * TOE_C + 4) = M[a]; *(LAS u16*)(p1 + 3 * TOE_C) = (u16)(P1[a] >> 16);
      FORU(j, 2) { pv[2 * a + j] = bf2f((a & 1) ? ph[a >> 1][j] : pl[a >> 1][j]); pin(pv[2 * a + j]); }
      SB();
    }
    st128s(rs, lane * 16, b < 176 ? WS_WOP + (b * 4 + w) * 1024 : OOB, v4u{pl[2].x, pl[2].y, ph[2].x, ph[2].y});
  }
  asm volatile("" ::"v"(pf0), "v"(pf2));
  STAMP(3);
  auto s1_bad = [&]() {
    u32 mx = umax3(gm[0].y, gm[0].w, tag), mn = umin3(gm[0].y, gm[0].w, tag);
    FORU(k, 2) { mx = umax3(mx, gm[1 + k].y, gm[1 + k].w); mn = umin3(mn, gm[1 + k].y, gm[1 + k].w); }
    FORU(k, 4) {
      mx = umax3(umax3(mx, gx[k].y, gx[k].w), gs[k].y, gs[k].w);
      mn = umin3(umin3(mn, gx[k].y, gx[k].w), gs[k].y, gs[k].w);
    }
    return __any(mx != mn);
  };
  if (RARE(s1_bad())) {
#pragma nounroll
    for (int sp = 0; sp < SPIN_CAP; ++sp) {
      FORU(n, 4) { reld(gx[n], rs, lane * 16, WS_XG + cp * 16384 + w * 4096 + n * 1024); reld(gs[n], rs, lane * 16, WS_STAT1 + w * 4096 + n * 1024); }
      reld(gm[0], rs, mo2, 0); reld(gm[1], rs, mo, 768 * 16); reld(gm[2], rs, mo, 1024 * 16);
      asm volatile("s_waitcnt vmcnt(0)" : "+v"(gx[0]), "+v"(gx[1]), "+v"(gx[2]), "+v"(gx[3]), "+v"(gs[0]), "+v"(gs[1]), "+v"(gs[2]), "+v"(gs[3]), "+v"(gm[0]), "+v"(gm[1]), "+v"(gm[2])::"memory");
      CNT(29, 1);
      if (!s1_bad()) break;
    }
  }
  STAMP(4);
  u32 xv[8];
  u32 h0[4], h1[4];
  u32 gav;
  {
    float qs[6];
    f2 m[4], r[4], a[4], c[4];
    auto fill = [&]() { SB(); };
    FORU(i, 6) qs[i] = dppadd<0xB1>(__uint_as_float((i & 1) ? gm[i >> 1].z : gm[i >> 1].x));
    FORU(k, 4) { xv[2 * k] = gx[k].x; xv[2 * k + 1] = gx[k].z; r[k] = f2{bfhi(gs[k].x), bfhi(gs[k].z)}; }
    fill();
    FORU(i, 6) qs[i] = dppadd<0x4E>(qs[i]);
    FORU(k, 4) m[k] = f2{bflo(gs[k].x), bflo(gs[k].z)};
    fill();
    const u32 mv = pkbf(qs[0], qs[1]), sbp = pkbf(qs[2], qs[3]), cbp = pkbf(qs[4], qs[5]);
    FORU(k, 4) r[k] = f2{__builtin_amdgcn_rcpf(r[k].x), __builtin_amdgcn_rcpf(r[k].y)};
    fill();
    const u32 sav = __builtin_amdgcn_readlane(mv, 0), cav = __builtin_amdgcn_readlane(mv, 4);
    gav = __builtin_amdgcn_readlane(mv, 8);
    const u32 scp = pkbf(bf2f(cbp) + 1.0f);
    FORU(k, 4) { a[k] = f2{bflo(gx[k].x), bflo(gx[k].z)}; c[k] = f2{bfhi(gx[k].x), bfhi(gx[k].z)}; }
    fill();
    const u32 s1v = pkbf(bf2f(cav) + 1.0f);
    FORU(k, 4) { a[k] -= m[k]; c[k] -= m[k]; }
    const u32 scb[4] = {dppu<0x150>(scp), dppu<0x154>(scp), dppu<0x158>(scp), dppu<0x15C>(scp)};
    fill();
    FORU(k, 4) { a[k] *= r[k]; c[k] *= r[k]; }
    f2 sc[4], sb[4], t[8], bw[2], sw[2];
    FORU(j, 4) sc[j] = bf2f(scb[j]);
    fill();
    FORU(k, 4) { a[k] = a[k] * bflo(s1v) + bflo(sav); c[k] = c[k] * bfhi(s1v) + bfhi(sav); }
    FORU(j, 8) t[j] = pv[j] * sc[j & 3];
    fill();
    v4u wpk[2];
    FORU(k, 4) { h0[k] = pkbf(a[k]); h1[k] = pkbf(c[k]); }
    FORU(j, 8) wpk[j >> 2][j & 3] = pkbf(t[j]);
    fill();
    *(LAS v4u*)up = v4u{h0[0], h0[1], h0[2], h0[3]};
    *(LAS v4u*)(up + U_CH) = v4u{h1[0], h1[1], h1[2], h1[3]};
    st128(rs, wio, wpk[0]);
    st128s(rs, wio, two ? 65536 : OOB, wpk[1]);
    const u32 sbb[4] = {dppu<0x150>(sbp), dppu<0x154>(sbp), dppu<0x158>(sbp), dppu<0x15C>(sbp)};
    FORU(j, 8) t[j] = bf2f(wpk[j >> 2][j & 3]);
    SB();
    FORU(j, 4) sb[j] = bf2f(sbb[j]);
    FORU(f, 2) sw[f] = (t[4 * f] + t[4 * f + 1]) + (t[4 * f + 2] + t[4 * f + 3]);
    SB();
    FORU(j, 8) pv[j] *= sb[j & 3];
    SB();
    FORU(f, 2) bw[f] = (pv[4 * f] + pv[4 * f + 1]) + (pv[4 * f + 2] + pv[4 * f + 3]);
    const float e0 = sw16sum(bw[0].x + bw[0].y, sw[0].x + sw[0].y), e1 = sw16sum(bw[1].x + bw[1].y, sw[1].x + sw[1].y);
    __builtin_amdgcn_raw_buffer_store_b32(__float_as_uint(sw32sum(e0, e1)), rs, bwo, 0, SC1);
  }
  __syncthreads();
  STAMP(5);
  v4u cdv;
  const u32 fpo = lane < 48 ? WS_WINP + pj * 196608 + (((pi & 7) * 4 + w) * 48 + lane) * 128 : OOB;
  auto side = [&](auto S, auto I) {
    constexpr int s = S.v, i = I.v, z = 10 * s + i;
    if constexpr (z % 4 == 1 && z / 4 < 32) ldw(z / 4);
    if (z == 131) pf3 = __builtin_amdgcn_raw_buffer_load_b32(rw, lane < 32 ? pfo : OOB, 512, 0);
    if (z == 27) {
      asm volatile("s_waitcnt vmcnt(7)" ::: "memory");
      __builtin_amdgcn_raw_buffer_store_b32(tag, rs, lane == 0 ? WS_CD + b * 64 + w * 4 : OOB, 0, SC1);
    }
    if (z == 103) pf1 = __builtin_amdgcn_raw_buffer_load_b32(rs, fpo, 0, SC1);
    if (z == 163) cdv = ld128(rs, WS_CD + tid * 64);
  };
  if (w & 1) conv2<1, 2>(bp, side);
  else conv2<0, 3>(bp, side);
  STAMP(21);
  {
    f2 y[2][4], t[2][4];
    const float ga0 = bflo(gav), ga1 = bfhi(gav);
    FORU(k, 4) { t[0][k] = bf2f(h0[k]); t[1][k] = bf2f(h1[k]); }
    FORU(k, 4) { y[0][k] = f2{bflo(xv[2 * k]), bflo(xv[2 * k + 1])}; y[1][k] = f2{bfhi(xv[2 * k]), bfhi(xv[2 * k + 1])}; }
    SB();
    FORU(k, 4) { t[0][k] *= bflo(p_dsk); t[1][k] *= bfhi(p_dsk); pin(y[0][k]); pin(y[1][k]); pin(t[0][k]); pin(t[1][k]); }
    __syncthreads();
    STAMP(6);
    f4 q[4][4];
    auto yld = [&](int B) { FORU(e, 4) q[B][e] = *(LAS f4*)(yp[B] + (e >> 1) * Y_CH + (e & 1) * 16); SB(); };
    auto yadd = [&](int B) { FORU(e, 4) { t[e >> 1][2 * (e & 1)] += f2{q[B][e].x, q[B][e].y}; t[e >> 1][2 * (e & 1) + 1] += f2{q[B][e].z, q[B][e].w}; } SB(); };
    yld(0);
    if (w >= 2) {
      yld(1); yld(2);
      if (w == 3) { yld(3); yadd(3); }
      yadd(1); yadd(2);
    } else if (w == 1) { yld(1); yadd(1); }
    yadd(0);
    FORU(k, 4) { y[0][k] += t[0][k] * ga0; y[1][k] += t[1][k] * ga1; }
    SB();
    v4u o0, o1;
    FORU(k, 4) { o0[k] = pkbf(y[0][k]); o1[k] = pkbf(y[1][k]); }
    SB();
    st128(rs, x1w, san4(o0));
    st128(rs, x1w + 128, san4(o1));
  }
  CFENCE();
  asm volatile("" ::"v"(pf1), "v"(pf3));
  auto cd_bad = [&]() { return __any(umax3(umax3(cdv.x, cdv.y, cdv.z), cdv.w, tag) != umin3(umin3(cdv.x, cdv.y, cdv.z), cdv.w, tag)); };
  if (RARE(cd_bad())) {
#pragma nounroll
    for (int sp = 0; sp < SPIN_CAP; ++sp) {
      asm volatile("" ::: "memory");
      cdv = ld128(rs, WS_CD + tid * 64);
      if (!cd_bad()) break;
    }
  }
  __syncthreads();
  STAMP(7);
  const u32 blkS = 4 + (w >> 1);
  const bool hasS = blkS < nb;
  const u32 fo[4] = {((pj * 2 + 1) * 6 + w), ((pj * 2 + 1) * 6 + (hasS ? blkS : 4)), (pj * 12 + w), (pj * 12 + (hasS ? blkS : 4))};
  v4u ones = {0x3f803f80u, 0x3f803f80u, 0x3f803f80u, 0x3f803f80u};
  pin(ones);
  v4u wf[4][16];
  auto ldf = [&](int s, int k) { wf[s][k] = ld128s(rs, lane * 16 + (k & 3) * 1024, WS_WINP + fo[s] * 16384 + (k >> 2) * 4096); };
  f4 sW[4], bW[4];
  auto ldb = [&](int n) { ((n & 1) ? sW : bW)[n >> 1] = *(LAS f4*)(L + L_BW + w * 1024 + (n >> 1) * 256 + (n & 1) * 64 + q4 * 16); };
  {
    v4u xr[16];
    f4 bq[8];
    const int xo = WS_X1 + pi * 65536 + w * 16384;
    sfor<0, 40>([&](auto z) {
      constexpr int n = z.v;
      if constexpr (n < 8) bq[n] = __builtin_bit_cast(f4, ld128s(rs, lane * 16 + (n & 1) * 1024, WS_BWP + fo[n >> 1] * 2048));
      else if constexpr (n < 16) ldf(n & 1, (n - 8) >> 1);
      else if constexpr (n < 32) xr[n - 16] = ld128s(rs, lane * 16 + (n & 3) * 1024, xo + rq((n - 16) >> 2) * 4096);
      else ldf(n & 1, (n - 24) >> 1);
      if constexpr (n < 32) { shuf(IC<2 * n>{}); shuf(IC<2 * n + 1>{}); }
      SB();
      if constexpr (n == 31) {
        FORU(i, 4) bq[i] = bq[2 * i] + bq[2 * i + 1];
        SB();
        f4 u, v;
        FORU(e, 4) { u[e] = sw32sum(bq[0][e], bq[2][e]); v[e] = sw32sum(bq[1][e], bq[3][e]); }
        FORU(e, 4) u[e] = sw16sum(u[e], v[e]);
        FORU(e, 4) u[e] = dppadd<0x128>(u[e]);
        *(LAS f4*)(L + L_BW + w * 1024 + lane * 16) = u;
        SB();
      }
    });
    STAMP(8);
    lptr xi = L + L_XIMG + w * 16384 + (lane >> 3) * 128 + (lane & 1) * 16;
    const u32 xs0 = (((lane & 7) >> 1) ^ ((lane >> 4) & 1)) * 32;
    u32 m = 0;
    FORU(k, 16) { *(LAS v4u*)(xi + rq(k >> 2) * 4096 + (k & 3) * 1024 + (xs0 ^ ((k & 1) * 64))) = xr[k]; m = vmax(m, xr[k]); ldf(k & 1, 8 + (k >> 1)); SB(); }
    if (RARE(__any(m == ~0u))) {
#pragma nounroll
      for (int sp = 0; sp < SPIN_CAP; ++sp) {
        asm volatile("" ::: "memory");
        FORU(k, 16) xr[k] = ld128s(rs, lane * 16, xo + rq(k >> 2) * 4096 + (k & 3) * 1024);
        SB();
        m = 0;
        FORU(k, 16) { *(LAS v4u*)(xi + rq(k >> 2) * 4096 + (k & 3) * 1024 + (xs0 ^ ((k & 1) * 64))) = xr[k]; m = vmax(m, xr[k]); SB(); }
        CNT(29, 100);
        if (!__any(m == ~0u)) break;
      }
    }
    STAMP(9);
  }
  __syncthreads();
  STAMP(10);
  const u32 xsw = ((c16 >> 3) & 1) | ((q4 & 1) << 1), xbo = (8 * q4 + (c16 >> 2)) * 128 + 8 * (c16 & 3);
  lptr xb[4];
#pragma unroll
  for (int t = 0; t < 4; ++t) xb[t] = L + xbo + ((t ^ xw ^ xsw) * 32);
  v2u bl[3][4], bh[3][4];
  auto ldx = [&](int k, int i) {
    if (k < 16) { if (i & 1) bh[k % 3][i >> 1] = trread(xb[i >> 1] + 4096 * k + 512); else bl[k % 3][i >> 1] = trread(xb[i >> 1] + 4096 * k); }
  };
  auto bx = [&](int k, int t) { return v4u{bl[k % 3][t].x, bl[k % 3][t].y, bh[k % 3][t].x, bh[k % 3][t].y}; };
  auto xwait = [&](int k) { asm volatile("" ::"v"(bh[k % 3][3])); };
  const int pco = WS_PB + ((pi * 4 + w) * 44 + blk0) * 512, pc2 = 128 + lane < 32 * nb ? lane * 16 : OOB;
  const int zco = WS_Z2N + ((pj * 6 + (w >> 1)) * 64 + 2 * pi + (w & 1)) * 1024, zc2 = w + 8 < 2 * nb ? zco + 262144 : OOB;
  f4 z2a[6];
  f2 rn[4];
  {
    f4 st0, st1;
    v2u sl[16], sh[16];
    FORU(k, 4) { sl[k] = trread(xb[0] + 4096 * k); sh[k] = trread(xb[0] + 4096 * k + 512); }
    MM_HEAD();
    sfor<0, 16>([&](auto z) {
      constexpr int k = z.v;
      const v4u v = {sl[k].x, sl[k].y, sh[k].x, sh[k].y};
      if (k) m_vv(st0, ones, v); else m_vv0(st0, ones, v);
      if constexpr (k + 4 < 16) sl[k + 4] = trread(xb[0] + 4096 * (k + 4)); else { ldx((k - 12) >> 1, 4 * (k & 1)); ldx((k - 12) >> 1, 4 * (k & 1) + 1); }
      SB();
      if (k) m_vv(st1, v, v); else m_vv0(st1, v, v);
      if constexpr (k + 4 < 16) sh[k + 4] = trread(xb[0] + 4096 * (k + 4) + 512); else { ldx((k - 12) >> 1, 4 * (k & 1) + 2); ldx((k - 12) >> 1, 4 * (k & 1) + 3); }
      if (k == 1) st128s(rs, lane * 16, pco, SENT);
      if (k == 5) st128s(rs, lane * 16, pco + 1024, SENT);
      if (k == 9) st128s(rs, pc2, pco + 2048, SENT);
      if (k == 3) st128s(rs, lane * 16, zco, SENT);
      if (k == 7) st128s(rs, lane * 16, zco + 131072, SENT);
      if (k == 11) st128s(rs, lane * 16, zc2, SENT);
      SB();
    });
    float sv0 = 0.f, sv1 = 0.f;
    FORU(k, 16) FORU(i, 6) {
      const int u = i == 0 ? 0 : i == 1 ? 4 : i == 2 ? 1 : i == 3 ? 5 : i - 2;
      if (i == 0) xwait(k);
      if (k) m_vv(z2a[u], wf[u >> 2][k], bx(k, u & 3)); else m_vv0(z2a[u], wf[u >> 2][k], bx(k, u & 3));
      if (i < 2) { ldx(k + 2, 2 * i); ldx(k + 2, 2 * i + 1); } else ldx(k + 2, i + 2);
      if (i == 2 && k) ldf(2 + ((k - 1) & 1), (k - 1) >> 1);
      if (i == 4 && (k & 1)) ldw(32 + (k >> 1));
      if (i == 3 || i == 5) {
        const int z = 2 * k + (i == 5);
        if (z == 4) sv1 = (c16 & 1) ? st1.y : st1.x;
        if (z == 5) sv0 = (c16 & 1) ? st1.w : st1.z;
        if (z == 6) sv1 = (c16 & 2) ? sv0 : sv1;
        if (z == 7) { pin(st0); sv0 = st0.x * (1.f / 512); }
        if (z == 8) sv1 *= (1.f / 512);
        if (z == 9) sv1 = fmaxf(__builtin_fmaf(-sv0, sv0, sv1), 0.f);
        if (z == 10) sv1 = rbf(sv1);
        if (z == 11) sv0 = rbf(sv0);
        if (z == 12) sv1 = rbf(sv1 + 1e-5f);
        if (z == 13) sv1 = __builtin_amdgcn_sqrtf(sv1);
        if (z == 14) sv1 = rbf(sv1);
        if (z == 15) sv1 = __builtin_amdgcn_rcpf(sv1);
        if (z == 16) sv0 *= -sv1;
        if (z == 17) *(volatile LAS f2*)(L + (q4 == (c16 >> 2) ? L_XST + (16 * xw + c16) * 8 : L_XST + 512 + lane * 8)) = f2{sv1, sv0};
        if (z >= 4 && z <= 16) { pin(sv0); pin(sv1); }
        if (z >= 30) { ldb(2 * z - 60); ldb(2 * z - 59); }
        if (z == 24) __builtin_amdgcn_s_barrier();
        if (z >= 26 && z < 30) rn[z - 26] = *(volatile LAS f2*)(L + L_XST + (16 * ((z - 26) ^ xw) + c16) * 8);
      }
      SB();
    }
    MM_TAIL();
  }
  STAMP(24);
  auto shuf2 = [&](auto C, auto N) {
    constexpr int n = N.v % 12, z = 16 * (C.v + N.v / 12);
    if constexpr (N.v < 24) { if constexpr (n < 8) shuf(IC<z + n>{}); else { shuf(IC<z + 2 * n - 8>{}); shuf(IC<z + 2 * n - 7>{}); } }
  };
  auto epi = [&](f4 (&a)[6], int s0, auto fill) {
    f2 v[12];
    sfor<0, 2>([&](auto z) {
      FORU(j, 6) {
        const int i = 6 * z.v + j;
        const f4 &sw = sW[s0 + (i >> 3)], &bw = bW[s0 + (i >> 3)];
        v[i] = ((i & 1) ? f2{sw.z, sw.w} : f2{sw.x, sw.y}) * rn[(i >> 1) & 3].y + ((i & 1) ? f2{bw.z, bw.w} : f2{bw.x, bw.y});
      }
      fill(IC<z.v>{});
    });
    sfor<0, 2>([&](auto z) {
      FORU(j, 6) { const int i = 6 * z.v + j; v[i] = ((i & 1) ? f2{a[i >> 1].z, a[i >> 1].w} : f2{a[i >> 1].x, a[i >> 1].y}) * rn[(i >> 1) & 3].x + v[i]; }
      fill(IC<2 + z.v>{});
    });
    gelu_n<12>(v, fill);
    FORU(i, 6) { a[i] = f4{v[2 * i].x, v[2 * i].y, v[2 * i + 1].x, v[2 * i + 1].y}; pin(a[i]); }
  };
  {
    epi(z2a, 0, [&](auto N) {
      constexpr int n = N.v;
      SB();
      if constexpr ((n & 1) && n < 22) ldf(2 + ((15 + n / 2) & 1), (15 + n / 2) >> 1);
      if constexpr (!(n & 1) && n < 24) ldw(40 + n / 2);
      SB();
    });
    if (!hasS) { z2a[4] = f4{0.f, 0.f, 0.f, 0.f}; z2a[5] = z2a[4]; }
    f2 zs[6], zq[6];
    FORU(i, 6) { zs[i] = f2{z2a[i].x, z2a[i].y} + f2{z2a[i].z, z2a[i].w}; zq[i] = f2{z2a[i].x, z2a[i].y} * f2{z2a[i].x, z2a[i].y}; }
    SB();
    FORU(i, 6) zq[i] = __builtin_elementwise_fma(f2{z2a[i].z, z2a[i].w}, f2{z2a[i].z, z2a[i].w}, zq[i]);
    FORU(i, 2) zs[i] += zs[4 + i];
    SB();
    float us[4], uq[4], u[4];
    FORU(i, 2) zq[i] += zq[4 + i];
    FORU(t, 4) us[t] = zs[t].x + zs[t].y;
    SB();
    FORU(t, 4) uq[3 - t] = zq[3 - t].x + zq[3 - t].y;
    SB();
    FORU(t, 4) u[3 - t] = sw32sum(us[3 - t], uq[3 - t]);
    LAS float* zp = (LAS float*)(L + L_ZST) + (w * 2 + (lane >> 5)) * 64 + c16;
    zp[16 * ((2 * (q4 & 1) + 1) ^ xw)] = sw16sum(u[1], u[3]);
    zp[16 * ((2 * (q4 & 1)) ^ xw)] = sw16sum(u[0], u[2]);
  }
  asm volatile("s_waitcnt vmcnt(20)" ::: "memory");
  __syncthreads();
  if (w == 0) {
    LAS float* zp = (LAS float*)(L + L_ZST) + lane;
    const float s = (zp[0] + zp[128]) + (zp[256] + zp[384]), ss = (zp[64] + zp[192]) + (zp[320] + zp[448]);
    st128(rs, WS_ST2 + ((pi * 8 + pj) * 64 + lane) * 16, v4u{__float_as_uint(s), tag, __float_as_uint(ss), tag});
  }
  __builtin_amdgcn_raw_buffer_store_b32(tag, rs, tid == 64 ? WS_F3 + (pj * 32 + pi) * 128 : OOB, 0, SC1);
  CFENCE();
  STAMP(11);
  v4u wo[6][4];
  const u32 wob = WS_WOP + (pj * 22 + 6 * w) * 4096, wobh = ssel(w < 3, wob, OOB);
  auto ldo = [&](int n) { wo[n >> 2][n & 3] = ld128s(rs, lane * 16 + (n & 3) * 1024, (n < 16 ? wob : wobh) + (n >> 2) * 4096); };
  f4 z1a[6];
  v2u xres[4];
  v4u g[8];
  u32 fv;
  auto f_ld = [&]() { fv = __builtin_amdgcn_raw_buffer_load_b32(rs, (lane & 7) * 128, WS_F3 + (pj * 32 + 8 * w) * 128, SC1); };
  auto s2b_ld = [&](int n) { g[n] = ld128s(rs, (2 * q4 * 64 + c16) * 16 + (n & 1) * 1024, WS_ST2 + pi * 8192 + ((n >> 1) ^ xw) * 256); };
  {
    FORU(i, 16) ldx(i >> 3, i & 7);
    MM_HEAD();
    sfor<0, 96>([&](auto z) {
      constexpr int k = z.v / 6, i = z.v % 6, u = i == 0 ? 0 : i == 1 ? 4 : i == 2 ? 1 : i == 3 ? 5 : i - 2;
      if (i == 0) xwait(k);
      if (k) m_vv(z1a[u], wf[2 + (u >> 2)][k], bx(k, u & 3)); else m_vv0(z1a[u], wf[2 + (u >> 2)][k], bx(k, u & 3));
      if (i < 2) { ldx(k + 2, 2 * i); ldx(k + 2, 2 * i + 1); } else ldx(k + 2, i + 2);
      if (k < 3 && (i == 2 || i == 4)) ldf(1 + (i >> 1), 13 + k);
      if (k >= 3 && k < 9 && i == 3) ldw(49 + k);
      if ((k == 9 || k == 10) && i >= 2) s2b_ld(4 * (k - 9) + i - 2);
      if (k >= 11 && k < 14 && (i == 2 || i == 4)) ldw(58 + 2 * (k - 11) + (i >> 2));
      if (k == 14 && i >= 2) ldb(2 + i);
      if (k == 15 && i == 3) f_ld();
      SB();
    });
    MM_TAIL();
#pragma unroll
    for (int n = 0; n < 4; ++n) {
      const u32 row = 16 * n + 4 * q4 + (c16 >> 2);
      xres[n] = trread(L + L_XIMG + pj * 8192 + row * 128 + ((w ^ (((row >> 1) & 1) | (((row >> 3) & 1) << 1))) * 32) + 8 * (c16 & 3));
    }
  }
  lptr twa[6], tra[4];
  {
    lptr tw = L + L_TRS + w * TRS_W;
    FORU(i, 6) { twa[i] = tw + (16 * ((i & 3) ^ xw) + c16) * 64 + 8 * (((i & 4) + q4) ^ (c16 >> 1)); pin(twa[i]); }
    const u32 r0 = 8 * q4 + (c16 >> 2);
    FORU(i, 4) {
      tra[i] = tw + ((i & 2) ? (w & 1) * 2048 : 0) + r0 * 64 + (i & 1) * 256 + 8 * ((2 * (i & 2) + (c16 & 3)) ^ (((r0 + 4 * (i & 1)) >> 1) & 7));
      pin(tra[i]);
    }
  }
  STAMP(12);
  {
    auto bad = [&]() { u32 mx = tag, mn = tag; FORU(n, 8) { mx = umax3(mx, g[n].y, g[n].w); mn = umin3(mn, g[n].y, g[n].w); } return __any(mx != mn); };
    if (RARE(bad())) {
#pragma nounroll
      for (int sp = 0; sp < SPIN_CAP; ++sp) {
        FORU(n, 8) reld(g[n], rs, (2 * q4 * 64 + c16) * 16, WS_ST2 + pi * 8192 + ((n >> 1) ^ xw) * 256 + (n & 1) * 1024);
        asm volatile("s_waitcnt vmcnt(0)" : "+v"(g[0]), "+v"(g[1]), "+v"(g[2]), "+v"(g[3]), "+v"(g[4]), "+v"(g[5]), "+v"(g[6]), "+v"(g[7])::"memory");
        CNT(29, 10000);
        if (!bad()) break;
      }
    }
    STAMP(13);
    float a[4], c[4];
    FORU(t, 4) { a[t] = __uint_as_float(g[2 * t].x) + __uint_as_float(g[2 * t + 1].x); c[t] = __uint_as_float(g[2 * t].z) + __uint_as_float(g[2 * t + 1].z); }
    SB();
    const float a0 = sw32sum(a[0], a[2]), a1 = sw32sum(a[1], a[3]), c0 = sw32sum(c[0], c[2]), c1 = sw32sum(c[1], c[3]);
    const float m = sw16sum(a0, a1) * (1.f / 688), var = fmaxf(sw16sum(c0, c1) * (1.f / 688) - m * m, 0.f);
    const float r = __builtin_amdgcn_rcpf(ln_den(var)), n0 = -rbf(m) * r;
    auto gather = [&](float v, float (&o)[4]) {
      auto e = __builtin_amdgcn_permlane16_swap(__float_as_uint(v), __float_as_uint(v), false, false);
      auto e0 = __builtin_amdgcn_permlane32_swap(e[0], e[0], false, false);
      auto e1 = __builtin_amdgcn_permlane32_swap(e[1], e[1], false, false);
      o[0] = __uint_as_float(e0[0]); o[2] = __uint_as_float(e0[1]); o[1] = __uint_as_float(e1[0]); o[3] = __uint_as_float(e1[1]);
    };
    gather(r, a); gather(n0, c);
    SB();
    FORU(i, 6) z2a[i] = z2a[i] * a[i & 3] + c[i & 3];
    SB();
    v2u zp[6], zl[3], zh[3];
    FORU(i, 6) zp[i] = v2u{pkbf(z2a[i].x, z2a[i].y), pkbf(z2a[i].z, z2a[i].w)};
    SB();
    FORU(i, 6) *(LAS v2u*)twa[i] = zp[i];
    FORU(n, 3) { zl[n] = trread(tra[n & 2] + (n & 1) * 2048); zh[n] = trread(tra[(n & 2) + 1] + (n & 1) * 2048); }
    SB();
    const u32 zo = WS_Z2N + (pj * 6 * 64 + 2 * pi) * 1024;
    FORU(n, 3) st128s(rs, lane * 16, n < 2 ? zo + w * 65536 + n * 1024 : (hasS ? zo + blkS * 65536 + (w & 1) * 1024 : OOB), san4(v4u{zl[n].x, zl[n].y, zh[n].x, zh[n].y}));
  }
  CFENCE();
  STAMP(14);
  u32 bsr[4], gbp[2];
  v4u gg[2];
  FORU(t, 4) bsr[t] = gld<u16>(bsp, (64 * pi + 16 * (t ^ xw) + c16) * 2);
  FORU(m, 2) gg[m] = ld128(rs, WS_MOD + ((lane & 3) * 1536 + 1280 + 32 * pj + 16 * m + 8 * (c16 >> 3) + 2 * q4 + ((c16 >> 2) & 1)) * 16);
  if (RARE(__any(fv != tag))) {
#pragma nounroll
    for (int sp = 0; sp < SPIN_CAP; ++sp) {
      asm volatile("" ::: "memory");
      f_ld();
      if (!__any(fv != tag)) break;
    }
  }
  CFENCE();
  v2u z1[6];
  f4 ga[2][4], q[20];
  lptr qa[6];
  auto ldq = [&](int n) { q[n] = *(volatile LAS f4*)(n < 12 ? qa[n & 3] + (n >> 2) * 4096 : qa[4 + (n & 1)] + ((n - 12) >> 1) * 4096); };
  {
    const u32 zo = WS_Z2N + (pj * 6 * 64 + 16 * w) * 1024;
    const u32 rot = ((nb == 6 ? 0x1420u : 0x1320u) >> (4 * (pi & 3))) & 15;
    v4u zr[16];
    u32 zv[4];
    FORU(g, 4) zv[g] = lane * 16 + rk(g) * 4096;
    u32 so[6], rsl[6];
    FORU(it, 5) {
      const u32 blk = smod(w + 1 + smod((it > 1 ? it + nb - 6 : it) + rot, nb - 1), nb);
      so[it] = zo + blk * 65536;
      rsl[it + 1] = ssel(blk < 4, blk * 3 + ((w - blk - 1) & 3), 4 * blk - 4 + w) * 4096;
    }
    so[5] = zo + w * 65536;
    auto ldz = [&](int k, u32 s0) { zr[k] = ld128s(rs, zv[k >> 2] + (k & 3) * 1024, s0); };
    FORU(n, 7) ldo(n);
    epi(z1a, 2, [&](auto N) {
      constexpr int n = N.v;
      SB();
      shuf2(IC<4>{}, N);
      if constexpr (n < 5) ldo(7 + n);
      if constexpr (n >= 6 && n < 22) ldz(n - 6, so[0]);
      SB();
    });
    FORU(i, 6) { z1[i] = v2u{pkbf(z1a[i].x, z1a[i].y), pkbf(z1a[i].z, z1a[i].w)}; pin(z1[i]); }
    FORU(m, 2) { gbp[m] = pkbf(quadsum(__uint_as_float(gg[m].x)), quadsum(__uint_as_float(gg[m].z))); pin(gbp[m]); }
    STAMP(25);
    __syncthreads();
    {
      auto bad = [&]() { u32 m = 0; FORU(k, 16) m = vmax(m, zr[k]); return __any(m == ~0u); };
      if (RARE(bad())) {
#pragma nounroll
        for (int sp = 0; sp < SPIN_CAP; ++sp) {
          asm volatile("" ::: "memory");
          FORU(k, 16) ldz(k, so[0]);
          CNT(28, 100);
          if (!bad()) break;
        }
      }
    }
    STAMP(15);
    u32 ra = 0, badm = 0;
    float ck = 0.f;
    v4u oth;
    MM_HEAD();
    auto slot = [&](auto z) {
      constexpr int it = z.v / 64, k = (z.v / 4) % 16, t = z.v % 4, h = it & 1;
      if (k) m_va(ga[h][t], zr[k], wsf[k][t]); else m_va0(ga[h][t], zr[k], wsf[k][t]);
      constexpr int pz = z.v / 4 - 1, pit = pz / 16, pk = pz % 16;
      if constexpr (t == 1 && pz >= 0) {
        if constexpr (pit < 5) ldz(pk, so[pit + 1]);
        else if constexpr (pk < 6) ldo(12 + pk);
      }
      if constexpr (it == 0 && t != 1 && 3 * k + t - (t > 0) < 32) shuf(IC<96 + 3 * k + t - (t > 0)>{});
      if constexpr (it > 0) {
        if (k == 2 && t == 0) { ck = ga[h ^ 1][0].x + ga[h ^ 1][0].y; pin(ck); }
        if (k == 3 && t == 0) { ck += ga[h ^ 1][0].z; pin(ck); }
        if (k == 4 && t == 0) { ck += ga[h ^ 1][0].w; pin(ck); }
        if (k == 5 && t == 0) badm |= __any(ck != ck) ? 1u << (it - 1) : 0u;
        if (k >= 2 && k < 6 && t == 2) { ra = rsl[it] + lane * 16 + (((k - 2) ^ xw) * 1024); pin(ra); }
        if constexpr (k >= 2 && k < 6 && t == 3) *(volatile LAS f4*)(L + L_RED + ra) = ga[h ^ 1][k - 2];
      }
      if constexpr (it == 5) {
        if constexpr (z.v >= 320 + 28 && z.v < 320 + 34) {
          constexpr int i = z.v - 348;
          u32 l16 = lane * 16;
          pin(l16);
          qa[i] = L + l16 + (L_RED + ((i < 4 ? w * 12 : (blkS * 4 - 4) * 4) + ((i & 3) ^ xw)) * 1024);
          pin(qa[i]);
        }
        if (k == 6 && t == 2) *(volatile LAS u32*)(L + L_XST + w * 4) = badm;
        if (k == 8 && t == 3) { asm volatile("s_waitcnt lgkmcnt(0)" ::: "memory"); __builtin_amdgcn_s_barrier(); }
        if (k == 9 && t == 0) oth = *(volatile LAS v4u*)(L + L_XST);
        if constexpr (z.v >= 320 + 37 && z.v < 320 + 57) ldq(z.v - 357);
      }
      SB();
    };
    sfor<0, 64>(slot);
    if (nb == 6) sfor<64, 128>(slot);
    else {
      MM_TAIL();
      const float t = (ga[0][0].x + ga[0][0].y) + (ga[0][0].z + ga[0][0].w);
      badm = __any(t != t) ? 1u : 0u;
      FORU(t, 4) { *(volatile LAS f4*)(L + L_RED + rsl[1] + lane * 16 + ((t ^ xw) * 1024)) = ga[0][t]; ga[1][t] = f4{0.f, 0.f, 0.f, 0.f}; }
      MM_HEAD();
    }
    sfor<128, 384>(slot);
    MM_TAIL();
    { const float t = (ga[1][0].x + ga[1][0].y) + (ga[1][0].z + ga[1][0].w); badm |= __any(t != t) ? 32u : 0u; }
    const u32 anyo = __builtin_amdgcn_readfirstlane(oth.x | oth.y | oth.z | oth.w);
    if (RARE(badm | anyo)) {
#pragma nounroll
      for (u32 it = 0; it < 6; ++it) {
        if (!((badm >> it) & 1)) continue;
        CNT(28, 1);
        const u32 s0 = it == 0 ? so[0] : it == 1 ? so[1] : it == 2 ? so[2] : it == 3 ? so[3] : it == 4 ? so[4] : so[5];
        const u32 r0 = it == 0 ? rsl[1] : it == 1 ? rsl[2] : it == 2 ? rsl[3] : it == 3 ? rsl[4] : rsl[5];
#pragma nounroll
        for (int sp = 0; sp < SPIN_CAP; ++sp) {
          u32 m = 0;
          FORU(k, 16) ldz(k, s0);
          FORU(k, 16) m = vmax(m, zr[k]);
          asm volatile("" ::: "memory");
          if (!__any(m == ~0u)) break;
        }
        MM_HEAD();
        FORU(k, 16) FORU(t, 4) { if (k) m_va(ga[0][t], zr[k], wsf[k][t]); else m_va0(ga[0][t], zr[k], wsf[k][t]); }
        MM_TAIL();
        FORU(t, 4) {
          if (it < 5) *(LAS f4*)(L + L_RED + r0 + lane * 16 + ((t ^ xw) * 1024)) = ga[0][t];
          else ga[1][t] = ga[0][t];
        }
      }
      if (anyo) __syncthreads();
      FORU(n, 20) ldq(n);
    }
  }
  __builtin_amdgcn_raw_buffer_store_b32(tag, rs, lane == 0 ? 0 : OOB, w == 0 ? WS_H4 + (pi * 8 + pj) * 128 : OOB, SC1);
  STAMP(26);
  {
    const u32 lane = fresh_lane(), c16 = lane & 15, q4 = lane >> 4;
    f2 zf[12], gs2[12];
    auto half = [](const f4& v, int hh) { return hh ? f2{v.z, v.w} : f2{v.x, v.y}; };
    FORU(i, 12) gs2[i] = i < 8 ? half(q[i >> 1], i & 1) + half(q[4 + (i >> 1)], i & 1) : half(q[8 + (i >> 1)], i & 1) + half(q[10 + (i >> 1)], i & 1);
    FORU(i, 12) zf[i] = i < 8 ? half(ga[1][i >> 1], i & 1) + half(q[8 + (i >> 1)], i & 1) : half(q[12 + (i >> 1)], i & 1) + half(q[14 + (i >> 1)], i & 1);
    SB();
    FORU(i, 12) gs2[i] += bflo(bsr[(i >> 1) & 3]);
    SB();
    FORU(i, 12) gs2[i] += zf[i];
    SB();
    FORU(i, 12) zf[i] = bf2f(z1[i >> 1][i & 1]);
    SB();
    FORU(i, 12) gs2[i] *= zf[i];
    SB();
    v2u P[6];
    FORU(i, 12) P[i >> 1][i & 1] = pkbf(gs2[i]);
    SB();
#pragma unroll
    for (int h = 0; h < 3; ++h) {
      auto rx = __builtin_amdgcn_permlane16_swap(P[2 * h].x, P[2 * h + 1].x, false, false);
      auto ry = __builtin_amdgcn_permlane16_swap(P[2 * h].y, P[2 * h + 1].y, false, false);
      const u32 tt = (2 * (h & 1) + (q4 & 1)) ^ xw;
      st128s(rs, WS_PB + ((pi * 4 + tt) * 44 + blk0) * 512 + (q4 >> 1) * 256 + c16 * 16, h < 2 ? w * 512 : (hasS ? blkS * 512 : OOB),
             san4(v4u{rx[0], ry[0], rx[1], ry[1]}));
    }
    FORU(n, 6) ldo(18 + n);
  }
  STAMP(17);
  {
    const u32 lane = fresh_lane(), c16 = lane & 15, q4 = lane >> 4;
    f4 oa[2][4];
    v4u pr[4][6];
    u32 pb[4], pbh[4];
    u32 rd[3];
    FORU(u, 4) {
      const u32 d = u < 3 ? 1 + smod(u + smod(pj & 3, 3), 3) : 4;
      pb[u] = WS_PB + ((pi * 4 + ((w + d) & 3)) * 44 + 12 * w) * 512; pbh[u] = ssel(w < 3, pb[u] + 4096, OOB);
      if (u < 3) rd[u] = L_RED2 + (((w + d) & 3) * 3 + 3 - d) * 4096 + lane * 16;
    }
    const u32 pv3 = (w == 3 && lane >= 32) ? OOB : lane * 16 + 3072;
    auto ldp = [&](int n) {
      const int u = n / 6, k = n % 6;
      pr[u][k] = ld128s(rs, k == 3 ? pv3 : lane * 16 + (k & 3) * 1024, k < 4 ? pb[u] : pbh[u]);
    };
#pragma nounroll
    for (int sp = 0; sp < SPIN_CAP; ++sp) {
      asm volatile("" ::: "memory");
      if (!__any(__builtin_amdgcn_raw_buffer_load_b32(rs, (lane & 7) * 128, WS_H4 + pi * 1024, SC1) != tag)) break;
      CNT(28, 10000);
      __builtin_amdgcn_s_sleep(2);
    }
    STAMP(18);
    FORU(n, 12) { ldp(n); SB(); }
    u32 bad = 0;
    MM_HEAD();
    sfor<0, 96>([&](auto z) {
      constexpr int u = z.v / 24, k = (z.v / 4) % 6, n = z.v % 4;
      if (k) m_vv(oa[u & 1][n], wo[k][n], pr[u][k]); else m_vv0(oa[u & 1][n], wo[k][n], pr[u][k]);
      if constexpr (n == 1 && z.v / 4 < 12) ldp(12 + z.v / 4);
      if constexpr (u > 0 && k == 2 && n == 0) bad |= __any(oa[(u & 1) ^ 1][0].x != oa[(u & 1) ^ 1][0].x) ? 1u << (u - 1) : 0u;
      if constexpr (u > 0 && k >= 2 && n == 2) *(LAS f4*)(L + rd[u - 1] + (k - 2) * 1024) = oa[(u & 1) ^ 1][k - 2];
      SB();
    });
    MM_TAIL();
    bad |= __any(oa[1][0].x != oa[1][0].x) ? 8u : 0u;
    if (RARE(bad)) {
#pragma nounroll
      for (u32 u = 0; u < 4; ++u) {
        if (!((bad >> u) & 1)) continue;
        CNT(28, 1000000);
        pb[0] = u == 0 ? pb[0] : u == 1 ? pb[1] : u == 2 ? pb[2] : pb[3];
        pbh[0] = u == 0 ? pbh[0] : u == 1 ? pbh[1] : u == 2 ? pbh[2] : pbh[3];
#pragma nounroll
        for (int sp = 0; sp < SPIN_CAP; ++sp) {
          u32 m = 0;
          asm volatile("" ::: "memory");
          FORU(k, 6) ldp(k);
          FORU(k, 6) m = vmax(m, pr[0][k]);
          if (!__any(m == ~0u)) break;
        }
        asm volatile("fk_text_b:");
        MM_HEAD();
        FORU(k, 6) FORU(n, 4) { if (k) m_vv(oa[0][n], wo[k][n], pr[0][k]); else m_vv0(oa[0][n], wo[k][n], pr[0][k]); }
        MM_TAIL();
        FORU(n, 4) {
          if (u < 3) *(LAS f4*)(L + (u == 0 ? rd[0] : u == 1 ? rd[1] : rd[2]) + n * 1024) = oa[0][n];
          else oa[1][n] = oa[0][n];
        }
      }
    }
    __syncthreads();
    f4 o[4];
    FORU(n, 4) {
      lptr p = L + L_RED2 + (w * 12 + n) * 1024 + lane * 16;
      o[n] = (oa[1][n] + *(LAS f4*)p) + (*(LAS f4*)(p + 4096) + *(LAS f4*)(p + 8192));
    }
    STAMP(19);
    v2u O[4];
#pragma unroll
    for (int n = 0; n < 4; ++n) {
      const u32 g0 = (n & 1) ? dppu<0x158>(gbp[n >> 1]) : dppu<0x150>(gbp[n >> 1]), g1 = (n & 1) ? dppu<0x15C>(gbp[n >> 1]) : dppu<0x154>(gbp[n >> 1]);
      O[n] = v2u{pkbf(bf2f(xres[n].x) + bf2f(g0) * f2{o[n].x, o[n].y}), pkbf(bf2f(xres[n].y) + bf2f(g1) * f2{o[n].z, o[n].w})};
    }
#pragma unroll
    for (int h = 0; h < 2; ++h) {
      auto rx = __builtin_amdgcn_permlane16_swap(O[2 * h].x, O[2 * h + 1].x, false, false);
      auto ry = __builtin_amdgcn_permlane16_swap(O[2 * h].y, O[2 * h + 1].y, false, false);
      *(v4u*)(out + (64 * pi + 16 * w + c16) * 512 + 64 * pj + 16 * (2 * h + (q4 & 1)) + 8 * (q4 >> 1)) = v4u{rx[0], ry[0], rx[1], ry[1]};
    }
  }
  if (b == 0 && w == 0) __builtin_amdgcn_raw_buffer_store_b32(tag, rs, WS_E, 0, SC1);
  STAMP(20);
  STAMP_FLUSH();
}

extern "C" void kernel_launch(void* const* d_in, const int* in_sizes, int n_in, void* d_out, int out_size, void* d_ws, size_t ws_size,
                              hipStream_t stream) {
  (void)in_sizes; (void)n_in; (void)out_size; (void)ws_size;
  (void)hipFuncSetAttribute((const void*)fused_kernel, hipFuncAttributeMaxDynamicSharedMemorySize, LDS_BYTES);
  fused_kernel<<<256, 256, LDS_BYTES, stream>>>((const u16*)d_in[0], (const u16*)d_in[1], (const u16*)d_in[2], (const u16*)d_in[3],
                                                (const u16*)d_in[4], (const u16*)d_in[5], (const u16*)d_in[6], (const u16*)d_in[8],
                                                (const u16*)d_in[9], (const u16*)d_in[10], (const u16*)d_in[11], (const u16*)d_in[12],
                                                (const u16*)d_in[13], (u16*)d_out, (unsigned char*)d_ws);
}
```

```cpp
#include <hip/hip_runtime.h>
#include <stdint.h>

typedef unsigned u32;
typedef unsigned short u16;
typedef unsigned long long u64;
typedef u32 v4u __attribute__((ext_vector_type(4)));
typedef u32 v2u __attribute__((ext_vector_type(2)));
typedef float f4 __attribute__((ext_vector_type(4)));
typedef float f2 __attribute__((ext_vector_type(2)));
typedef __bf16 bf2 __attribute__((ext_vector_type(2)));
typedef _Float16 h2 __attribute__((ext_vector_type(2)));
typedef short s4 __attribute__((ext_vector_type(4)));
#define LAS __attribute__((address_space(3)))
#define GAS __attribute__((address_space(1)))
#define CAS __attribute__((address_space(4)))
#define DI __device__ __forceinline__
typedef __amdgpu_buffer_rsrc_t rsrc_t;
typedef LAS unsigned char* lptr;

constexpr int HINT_Y = 48, T_X1 = 16, T_Z = 6, T_S2B = 9;

constexpr int WS_E = 0, WS_STAT1 = 4096, WS_CD = 32768, WS_ST2 = 65536, WS_F3 = 327680, WS_H4 = 360448, WS_MOD = 524288, WS_BWP = 655360;
constexpr int WS_XG = 1 << 20, WS_X1 = 5 << 20, WS_Z2N = 7 << 20, WS_PB = 10 << 20, WS_WINP = 13 << 20, WS_WOP = 29 << 19;
constexpr int WS_END = 16 << 20;
constexpr int OOB = 0x40000000;
constexpr int TABP = 544, TAB_CH = 63 * TABP;
constexpr int L_TAB = 0, L_Y = 0, Y_CH = 21504;
constexpr int L_PKW = 75776, L_SHF = 81920;
constexpr int L_PKI = 68544;
constexpr int L_TOE = 108544, TOE_C = 5312, TOE_CH = 4 * TOE_C;
constexpr int L_U = 151040, U_CH = 6320;
constexpr int L_XIMG = 0, L_TRS = 65536, TRS_W = 4096;
constexpr int L_RED = 0, L_RED2 = 81920;
constexpr int L_XST = 155648, L_ZST = L_XST + 2048, L_BW = L_ZST + 2048;
constexpr int LDS_BYTES = 163840;
constexpr int SC1 = 16;
constexpr int SPIN_CAP = 1 << 16;
static_assert(LDS_BYTES <= 163840, "LDS");

DI float bflo(u32 v) { return __uint_as_float(v << 16); }
DI float bfhi(u32 v) { return __uint_as_float(v & 0xffff0000u); }
DI f2 bf2f(u32 v) { return f2{bflo(v), bfhi(v)}; }
DI u32 pkbf(float a, float b) { f2 v = {a, b}; return __builtin_bit_cast(u32, __builtin_convertvector(v, bf2)); }
DI u32 pkbf(f2 v) { return __builtin_bit_cast(u32, __builtin_convertvector(v, bf2)); }
DI u32 pkbf_a(float a, float b) { u32 r; asm("v_cvt_pk_bf16_f32 %0, %1, %2" : "=v"(r) : "v"(a), "v"(b)); return r; }
DI u32 pkh(f2 v) { return __builtin_bit_cast(u32, __builtin_convertvector(v, h2)); }
DI float rbf(float a) { return bflo(pkbf(a, a)); }
DI u32 san(u32 v) { return v < 0xFFFFFFFEu ? v : 0xFFFFFFFEu; }
DI v4u san4(v4u v) { return v4u{san(v.x), san(v.y), san(v.z), san(v.w)}; }
DI u32 umax3(u32 a, u32 b, u32 c) { return max(max(a, b), c); }
DI u32 umin3(u32 a, u32 b, u32 c) { return min(min(a, b), c); }
DI u32 vmax(u32 m, v4u v) { return umax3(umax3(m, v.x, v.y), v.z, v.w); }
DI v4u ld128(rsrc_t rs, int off) { return __builtin_amdgcn_raw_buffer_load_b128(rs, off, 0, SC1); }
DI v4u ld128s(rsrc_t rs, int off, int soff) { return __builtin_amdgcn_raw_buffer_load_b128(rs, off, soff, SC1); }
DI void st128(rsrc_t rs, int off, v4u v) { __builtin_amdgcn_raw_buffer_store_b128(v, rs, off, 0, SC1); }
DI void st128s(rsrc_t rs, int off, int soff, v4u v) { __builtin_amdgcn_raw_buffer_store_b128(v, rs, off, soff, SC1); }
DI void st64(rsrc_t rs, int off, v2u v) { __builtin_amdgcn_raw_buffer_store_b64(v, rs, off, 0, SC1); }
DI void reld(v4u& v, rsrc_t rs, u32 voff, u32 soff) {
  asm volatile("s_nop 4\n\tbuffer_load_dwordx4 %0, %1, %2, %3 offen sc1" : "+v"(v) : "v"(voff), "s"(rs), "s"(soff) : "memory");
}
#define RARE(c) __builtin_expect(!!(c), 0)
#define SB() __builtin_amdgcn_sched_barrier(0)
#define CFENCE() do { asm volatile("" ::: "memory"); SB(); } while (0)
template <int C> DI u32 dppu(u32 v) { return (u32)__builtin_amdgcn_update_dpp(0, (int)v, C, 0xF, 0xF, true); }
template <int C> DI float dppadd(float v) { return v + __uint_as_float(dppu<C>(__float_as_uint(v))); }
DI float sw16sum(float a, float b) { auto r = __builtin_amdgcn_permlane16_swap(__float_as_uint(a), __float_as_uint(b), false, false); return __uint_as_float(r[0]) + __uint_as_float(r[1]); }
DI float sw32sum(float a, float b) { auto r = __builtin_amdgcn_permlane32_swap(__float_as_uint(a), __float_as_uint(b), false, false); return __uint_as_float(r[0]) + __uint_as_float(r[1]); }
DI float quadsum(float v) { v = dppadd<0xB1>(v); return dppadd<0x4E>(v); }
DI float rowsum(float v) { v = quadsum(v); v = dppadd<0x141>(v); return dppadd<0x140>(v); }
DI v2u trread(lptr p) { return __builtin_bit_cast(v2u, __builtin_amdgcn_ds_read_tr16_b64_v4i16((LAS s4*)p)); }
template <int N> struct IC { static constexpr int v = N; };
template <int I, int N, class F> DI void sfor(F f) { if constexpr (I < N) { f(IC<I>{}); sfor<I + 1, N>(f); } }
#define FORU(i, n) _Pragma("unroll") for (int i = 0; i < (n); ++i)
DI f2 cm1(f2 a, f2 b) { f2 t; asm("v_pk_mul_f32 %0, %1, %2 op_sel:[0,0] op_sel_hi:[0,1]" : "=v"(t) : "v"(a), "v"(b)); return t; }
DI f2 cm2(f2 a, f2 b, f2 t) { f2 r; asm("v_pk_fma_f32 %0, %1, %2, %3 op_sel:[1,1,0] op_sel_hi:[1,0,1] neg_lo:[0,1,0]" : "=v"(r) : "v"(a), "v"(b), "v"(t)); return r; }
DI f2 cexp_p(float al2, float rev, float p) {
  float m = __builtin_amdgcn_exp2f(al2 * p), f = __builtin_amdgcn_fractf(rev * p);
  return f2{m * __builtin_amdgcn_cosf(f), m * __builtin_amdgcn_sinf(f)};
}
template <int N, class F> DI void gelu_n(f2 (&v)[N], F fill) {
  const float c0 = -2.0f * 0.7978845608028654f * 1.4426950408889634f, c1 = c0 * 0.044715f;
  f2 a[N];
  sfor<0, 2>([&](auto z) { FORU(i, 6) a[6 * z.v + i] = v[6 * z.v + i] * v[6 * z.v + i]; fill(IC<4 + z.v>{}); });
  sfor<0, 2>([&](auto z) { FORU(i, 6) a[6 * z.v + i] = __builtin_elementwise_fma(a[6 * z.v + i], f2{c1, c1}, f2{c0, c0}); fill(IC<6 + z.v>{}); });
  sfor<0, 2>([&](auto z) { FORU(i, 6) a[6 * z.v + i] *= v[6 * z.v + i]; fill(IC<8 + z.v>{}); });
  sfor<0, 6>([&](auto z) { FORU(i, 2) a[2 * z.v + i] = f2{__builtin_amdgcn_exp2f(a[2 * z.v + i].x), __builtin_amdgcn_exp2f(a[2 * z.v + i].y)}; fill(IC<10 + z.v>{}); });
  sfor<0, 2>([&](auto z) { FORU(i, 6) a[6 * z.v + i] += 1.0f; fill(IC<16 + z.v>{}); });
  sfor<0, 6>([&](auto z) { FORU(i, 2) a[2 * z.v + i] = f2{__builtin_amdgcn_rcpf(a[2 * z.v + i].x), __builtin_amdgcn_rcpf(a[2 * z.v + i].y)}; fill(IC<18 + z.v>{}); });
  sfor<0, 2>([&](auto z) { FORU(i, 6) v[6 * z.v + i] *= a[6 * z.v + i]; fill(IC<24 + z.v>{}); });
}
DI float ln_den(float var) { return rbf(__builtin_amdgcn_sqrtf(rbf(rbf(var) + 1e-5f))); }
template <class T> DI T gld(const void* base, u32 off) { return *(const GAS T*)((const GAS char*)base + off); }
#define MF "v_mfma_f32_16x16x32_bf16 "
DI void m_va(f4& c, v4u a, v4u b) { asm volatile(MF "%0, %1, %2, %0" : "+v"(c) : "v"(a), "a"(b)); }
DI void m_vv(f4& c, v4u a, v4u b) { asm volatile(MF "%0, %1, %2, %0" : "+v"(c) : "v"(a), "v"(b)); }
DI void m_va0(f4& c, v4u a, v4u b) { asm volatile(MF "%0, %1, %2, 0" : "=&v"(c) : "v"(a), "a"(b)); }
DI void m_vv0(f4& c, v4u a, v4u b) { asm volatile(MF "%0, %1, %2, 0" : "=&v"(c) : "v"(a), "v"(b)); }
DI void m4_va(f4* c, v4u a, const v4u (&b)[4]) {
  asm volatile("s_nop 1\n\t" MF "%0, %4, %5, %0\n\t" MF "%1, %4, %6, %1\n\t" MF "%2, %4, %7, %2\n\t" MF "%3, %4, %8, %3\n\ts_nop 7"
               : "+v"(c[0]), "+v"(c[1]), "+v"(c[2]), "+v"(c[3]) : "v"(a), "a"(b[0]), "a"(b[1]), "a"(b[2]), "a"(b[3]));
}
DI void m4_vv(f4* c, const v4u (&a)[4], v4u b) {
  asm volatile("s_nop 1\n\t" MF "%0, %5, %4, %0\n\t" MF "%1, %6, %4, %1\n\t" MF "%2, %7, %4, %2\n\t" MF "%3, %8, %4, %3\n\ts_nop 7"
               : "+v"(c[0]), "+v"(c[1]), "+v"(c[2]), "+v"(c[3]) : "v"(b), "v"(a[0]), "v"(a[1]), "v"(a[2]), "v"(a[3]));
}
DI u32 ssel(bool c, u32 a, u32 b) { asm("" : "+s"(a)); asm("" : "+s"(b)); return c ? a : b; }
DI u32 smod(u32 v, u32 m) { u32 t = v - m; asm("" : "+s"(t)); return (int)t >= 0 ? t : v; }
DI u32 fresh_lane() { u32 l; asm volatile("v_mbcnt_lo_u32_b32 %0, -1, 0\n\tv_mbcnt_hi_u32_b32 %0, -1, %0" : "=v"(l)); return l; }
template <class T> DI void pin(T& v) { asm volatile("" : "+v"(v)); }
#define MM_HEAD() do { SB(); asm volatile("s_nop 7"); SB(); } while (0)
#define MM_TAIL() do { SB(); asm volatile("s_nop 7"); SB(); } while (0)
#define MM_DONE(a) asm volatile("s_nop 7" : "+v"((a)[0]), "+v"((a)[1]), "+v"((a)[2]), "+v"((a)[3]))
DI int toe_addr(int pos) { return 2 * pos + 8 * (pos >> 4); }


template <int BAND> struct Band {
  static constexpr int R = 128 - 32 * BAND, NT = R / 16, NS = 15 + NT / 2;
  lptr al, ah, bb, yp;
  f4 acc[NT];
  v2u lo[32], hi[32];
  v4u bf[NS];
  DI void load(int s, int i) {
    if (i < 4) {
      const int n = 2 * s + (i >> 1);
      if (n < 32) { if (i & 1) hi[n] = *(LAS v2u*)(ah + 40 * (31 - n)); else lo[n] = *(LAS v2u*)(al + 40 * (31 - n)); }
    } else if (s < NS) bf[s] = *(LAS v4u*)(bb + 80 * (NT / 2 - 1 - s + 15));
  }
  DI void mma(int s, int t) {
    const int n = t - NT + 2 + 2 * s;
    if (s < NS && n >= 0 && n < 32) {
      if (n < 2) m_vv0(acc[t], v4u{lo[n].x, lo[n].y, hi[n].x, hi[n].y}, bf[s]); else m_vv(acc[t], v4u{lo[n].x, lo[n].y, hi[n].x, hi[n].y}, bf[s]);
    }
  }
  DI void flush(int s, int t) {
    const int n = t - NT + 2 + 2 * (s - 1);
    if (n == 30 || n == 31) *(LAS f4*)(yp + 64 * t) = acc[t];
  }
};
template <int B1, int B2, class F> DI void conv2(const lptr (&bp)[8], F side) {
  Band<B1> u; Band<B2> v;
  u.al = bp[0]; u.ah = bp[1]; u.bb = bp[2]; u.yp = bp[3]; v.al = bp[4]; v.ah = bp[5]; v.bb = bp[6]; v.yp = bp[7];
  constexpr int N1 = Band<B1>::NT, NS = Band<B1>::NS;
#pragma unroll
  for (int s = 0; s < 2; ++s)
#pragma unroll
    for (int i = 0; i < 5; ++i) { u.load(s, i); v.load(s, i); SB(); }
  sfor<0, 10 * (NS + 1)>([&](auto z) {
    constexpr int s = z.v / 10, i = z.v % 10;
    if (s >= 17 && i == 0) asm volatile("s_nop %0" ::"i"(s == NS ? 7 : s == NS - 1 ? 5 : 2));
    if (i < N1) { u.mma(s, i); u.flush(s, i); } else { v.mma(s, i - N1); v.flush(s, i - N1); }
    if (i < 5) u.load(s + 2, i); else v.load(s + 2, i - 5);
    side(IC<s>{}, IC<i>{});
    SB();
  });
}

__global__ void __launch_bounds__(256, 1)
fused_kernel(const u16* __restrict__ x, const u16* __restrict__ lar, const u16* __restrict__ aim, const u16* __restrict__ cre,
             const u16* __restrict__ cim, const u16* __restrict__ ldt, const u16* __restrict__ dsk, const u16* __restrict__ win,
             const u16* __restrict__ wsp, const u16* __restrict__ bsp, const u16* __restrict__ wout, const u16* __restrict__ cvec,
             const u16* __restrict__ wmod, u16* __restrict__ out, unsigned char* ws) {
  extern __shared__ __attribute__((aligned(16))) unsigned char smem[];
  lptr L = (lptr)smem;
  const u32 b = blockIdx.x, tid = threadIdx.x, lane = tid & 63;
  const u32 w = __builtin_amdgcn_readfirstlane(tid >> 6);
  const u32 xc = b & 7, rr = b >> 3, cp = xc * 32 + rr;
  const u32 c16 = lane & 15, q4 = lane >> 4;
  u32 pf0 = 0, pf1 = 0;
  {
    u64 pc;
    u32 len;
    asm volatile("s_getpc_b64 %0\nfk_text_a:\n\ts_mov_b32 %1, fk_text_b-fk_text_a" : "=s"(pc), "=s"(len));
    const u32 o = (rr * 16 + tid) * 128;
    if (tid < 16 && o < len) pf0 = gld<u32>((const void*)pc, o);
  }
  CFENCE();
  u32 tag = *(const CAS u32*)(ws + WS_E) + 1u;
  const u32 tb = 512 * (b >> 6) + 32 * (b & 15) + 2 * ((b >> 4) & 3);
  const v4u sr0 = gld<v4u>(x, (tb + 8 * w) * 1024 + lane * 16), sr1 = gld<v4u>(x, (tb + 8 * w) * 1024 + 1024 + lane * 16);
  const u32 xgo = (tb + 8 * (lane & 3)) * 1024 + (16 * w + (lane >> 2)) * 16;
  const v4u xg0 = gld<v4u>(x, xgo), xg1 = gld<v4u>(x, xgo + 1024);
  const u32 mG = b % 48, mP = min(b / 48, 3u), mrow = mP * 128 + (lane >> 1);
  v4u wm[4];
  u32 cv[4];
#pragma unroll
  for (int n = 0; n < 4; ++n) wm[n] = gld<v4u>(wmod, (mrow + 32 * n) * 6144 + mG * 128 + w * 32 + (lane & 1) * 16);
#pragma unroll
  for (int n = 0; n < 4; ++n) cv[n] = gld<u16>(cvec, (mrow + 32 * n) * 2);
  const u32 pidx = (cp * 256 + tid) * 2;
  const u32 p_lar = gld<u16>(lar, pidx), p_aim = gld<u16>(aim, pidx), p_cr = gld<u16>(cre, pidx), p_ci = gld<u16>(cim, pidx);
  const u32 p_cr2 = gld<u16>(cre, pidx ^ 128), p_ci2 = gld<u16>(cim, pidx ^ 128);
  const u32 p_ldt = *(const CAS u32*)(ldt + 2 * cp), p_dsk = *(const CAS u32*)(dsk + 2 * cp);
  CFENCE();
  const u32 pi = (xc >> 1) * 8 + (rr & 7), pj = (xc & 1) * 4 + (rr >> 3);
  const u32 nb = pj < 3 ? 6 : 5, blk0 = 5 * pj + min(pj, 3u);
  const bool two = w + 4 < nb;
  const rsrc_t rs = __builtin_amdgcn_make_buffer_rsrc((void*)ws, 0, WS_END, 0x00020000);
  const v4u SENT = {~0u, ~0u, ~0u, ~0u};
  const int x1w = WS_X1 + ((tid >> 3) * 512 + 2 * cp) * 128 + (tid & 7) * 16;
  const u32 wio_src = ((32 * (pi >> 1) + (lane >> 1)) * 1376 + (pi & 1) * 688 + 16 * (blk0 + w)) * 2 + (lane & 1) * 16;
  const v4u wi0 = gld<v4u>(win, wio_src), wi1 = gld<v4u>(win, two ? wio_src + 128 : wio_src);
  const rsrc_t ro = __builtin_amdgcn_make_buffer_rsrc((void*)wout, 0, 688 * 1024, 0x00020000);
  const v4u wo0 = __builtin_amdgcn_raw_buffer_load_b128(ro, (lane >> 1) * 1024 + (lane & 1) * 16, b < 176 ? (b % 22) * 32768 + (b / 22) * 128 + w * 32 : OOB, 0);
  CFENCE();
  if (tid < 40) *(LAS v4u*)(L + L_TOE + (tid / 5) * TOE_C + 5120 + (tid % 5) * 16) = v4u{0, 0, 0, 0};
  if (tid < 150) *(LAS v4u*)(L + L_U + (tid / 75) * U_CH + (tid % 75) * 16) = v4u{0, 0, 0, 0};
  lptr ka[5], pa[3], t0, t1, up, bp[8], yp[4];
  int mo, mo2, wio, bwo;
  {
    const u32 hi = w & 1, ch = w >> 1;
    lptr tb_ = L + L_TAB + ch * TAB_CH + 16 * q4;
    const u32 i2 = (c16 + ((c16 >> 1) & 1)) >> 1, i4 = (c16 + 1 + ((c16 >> 2) & 1)) >> 2, i8 = (c16 + 3 + ((c16 >> 3) & 1)) >> 3;
    const u32 row[5] = {hi ? 35 + i8 : c16, hi ? 38 + i8 : 16 + i2, hi ? 41 + i8 : 25 + i4, hi ? 44 + i8 : 30 + i4, 47 + c16};
    FORU(i, 5) { ka[i] = tb_ + row[i] * TABP; pin(ka[i]); }
    FORU(a, 3) { pa[a] = L + (a == 0 ? L_PKI + w * 1024 : a == 1 ? L_PKI + 4096 + hi * 1024 : L_PKW + w * 8192) + (8 * q4 + (c16 >> 2)) * 32 + 8 * (c16 & 3); pin(pa[a]); }
    const int i0 = 2044 - (1024 * hi + 768 + 16 * c16 + 4 * q4);
    t0 = L + L_TOE + ch * TOE_CH + toe_addr(i0); t1 = L + L_TOE + ch * TOE_CH + toe_addr(i0 + 4);
    up = L + L_U + 80 * (15 + (tid >> 2)) + 16 * (tid & 3);
    pin(t0); pin(t1); pin(up);
    FORU(n, 2) {
      const u32 B = n ? 3 - hi : hi, R = 128 - 32 * B, c = (3 - c16) & 3, P = 2048 - 512 * B - 496 - 4 * (c16 >> 2) + 8 * q4;
      bp[4 * n] = L + L_TOE + ch * TOE_CH + c * TOE_C + toe_addr(P);
      bp[4 * n + 1] = L + L_TOE + ch * TOE_CH + c * TOE_C + toe_addr(P + 4);
      bp[4 * n + 2] = L + L_U + ch * U_CH + 80 * (4 - B) * c16 + 16 * q4;
      bp[4 * n + 3] = L + L_Y + ch * Y_CH + (n ? (hi ? 14848 : 19200) : (hi ? 8448 : 0)) + c16 * (4 * R + 16) + 16 * q4;
    }
    FORU(n, 8) pin(bp[n]);
    FORU(B, 4) {
      const u32 tp = 8 * tid - 512 * B;
      yp[B] = L + L_Y + (B == 0 ? 0 : B == 1 ? 8448 : B == 2 ? 14848 : 19200) + ((tp & 2047) / (128 - 32 * B)) * 16 + tp * 4;
      pin(yp[B]);
    }
    mo = WS_MOD + ((lane & 3) * 1536 + 16 * (pi >> 1) + (lane >> 2)) * 16;
    mo2 = WS_MOD + ((lane & 3) * 1536 + 256 * min(lane >> 2, 2u) + cp) * 16;
    wio = WS_WINP + (((pj * 2 + (pi & 1)) * 6 + w) * 16 + (pi >> 1)) * 1024 + lane * 16;
    bwo = WS_BWP + (((pj * 2 + (pi & 1)) * 6 + w) * 16 + (pi >> 1)) * 128 + (lane & 31) * 4;
    bwo = lane < 32 ? bwo : two ? bwo + 8192 : OOB;
    pin(mo); pin(mo2); pin(wio); pin(bwo);
  }
  {
    f2 a[8];
#pragma unroll
    for (int e = 0; e < 4; ++e) { a[2 * e] = f2{bflo(sr0[e]), bflo(sr1[e])}; a[2 * e + 1] = f2{bfhi(sr0[e]), bfhi(sr1[e])}; }
    f2 s = ((a[0] + a[1]) + (a[2] + a[3])) + ((a[4] + a[5]) + (a[6] + a[7]));
    float t = rowsum(sw32sum(s.x, s.y));
    t = sw16sum(t, t) * (1.f / 512);
    auto mm = __builtin_amdgcn_permlane32_swap(__float_as_uint(t), __float_as_uint(t), false, false);
    const f2 m = {__uint_as_float(mm[0]), __uint_as_float(mm[1])};
    s = f2{0.f, 0.f};
#pragma unroll
    for (int e = 0; e < 8; ++e) { const f2 u = a[e] - m; s = __builtin_elementwise_fma(u, u, s); }
    float v = rowsum(sw32sum(s.x, s.y));
    v = sw16sum(v, v) * (1.f / 512);
    asm volatile("" : "+s"(tag));
    st64(rs, (lane & 31) == 0 ? WS_STAT1 + b * 64 + w * 16 + (lane >> 5) * 8 : OOB, v2u{pkbf(t, ln_den(v)), tag});
  }
  {
    const int o = WS_XG + (4 * (16 * w + (lane >> 2))) * 16384 + b * 64 + (lane & 3) * 16;
    st128(rs, o, v4u{xg0.x, tag, xg1.x, tag});
    st128s(rs, o, 16384, v4u{xg0.y, tag, xg1.y, tag});
    st128s(rs, o, 32768, v4u{xg0.z, tag, xg1.z, tag});
    st128s(rs, o, 49152, v4u{xg0.w, tag, xg1.w, tag});
  }
  CFENCE();
  {
    f2 a[4];
#pragma unroll
    for (int n = 0; n < 4; ++n) {
      const float c = bflo(cv[n]);
      const float s = rbf(c * __builtin_amdgcn_rcpf(1.f + __builtin_amdgcn_exp2f(-1.4426950408889634f * c)));
#pragma unroll
      for (int e = 0; e < 4; ++e) a[e] = n ? a[e] + bf2f(wm[n][e]) * s : bf2f(wm[n][e]) * s;
    }
    const float u0 = sw32sum(a[0].x, a[2].x), u1 = sw32sum(a[0].y, a[2].y), u2 = sw32sum(a[1].x, a[3].x), u3 = sw32sum(a[1].y, a[3].y);
    float t0 = sw16sum(u0, u2), t1 = sw16sum(u1, u3);
    t0 = dppadd<0x4E>(dppadd<0x124>(dppadd<0x128>(t0)));
    t1 = dppadd<0x4E>(dppadd<0x124>(dppadd<0x128>(t1)));
    st128(rs, (c16 < 2 && b < 192) ? WS_MOD + (mP * 1536 + mG * 32 + w * 8 + c16 * 4 + q4) * 16 : OOB, v4u{__float_as_uint(t0), tag, __float_as_uint(t1), tag});
  }
  CFENCE();
  const u32 xw = 2 * (w & 1) + (w >> 1);
  const u32 pfo = (64 * pi + 8 * pj + 2 * w + ((lane >> 4) & 1)) * 4096 + ((lane >> 2) & 3) * 1024 + (lane & 3) * 128;
  u32 pf2 = 0, pf3;
  if (lane < 32) pf2 = gld<u32>(wsp, pfo);
  st128(rs, x1w, SENT);
  st128(rs, x1w + 128, SENT);
  CFENCE();
  const rsrc_t rw = __builtin_amdgcn_make_buffer_rsrc((void*)wsp, 0, 2048 * 4096, 0x00020000);
  const u32 wvo = (lane >> 3) * 4096 + (lane & 7) * 16;
  const u32 wsb[4] = {(64 * pi + 16 * xw) * 4096 + w * 1024, (64 * pi + 16 * (xw ^ 1)) * 4096 + w * 1024,
                      (64 * pi + 16 * (xw ^ 2)) * 4096 + w * 1024, (64 * pi + 16 * (xw ^ 3)) * 4096 + w * 1024};
  v4u wraw[64], wsf[16][4];
  auto rq = [&](int g) { return (g + pj) & 3; };
  auto rk = [&](int g) { return g ^ (pj & 1); };
  auto ldw = [&](int n) { wraw[n] = __builtin_amdgcn_raw_buffer_load_b128(rw, (wvo + rk(n >> 4) * 256) + ((n >> 3) & 1) * 128, wsb[(n & 7) >> 1] + (n & 1) * 32768, 0); };
  const u32 wwa = (u32)(size_t)L + L_SHF + w * 8192 + (lane >> 3) * 128 + ((lane & 7) ^ (lane >> 3)) * 16;
  auto shuf = [&](auto Z) {
    constexpr int c = Z.v >> 4, j = Z.v & 15, i = j & 7;
    if constexpr (j < 8) asm volatile("ds_write_b128 %0, %1 offset:%2" ::"v"(wwa), "a"(wraw[8 * c + i]), "i"(i * 1024) : "memory");
    else wsf[2 * c + (i & 1)][i >> 1] = *(LAS v4u*)(L + L_SHF + w * 8192 + (i >> 1) * 2048 + c16 * 128 + (((4 * (i & 1) + q4) ^ (c16 & 7)) * 16));
  };
  float kscale;
  {
    const float L2E = 1.4426950408889634f;
    const float dt = rbf(__builtin_amdgcn_exp2f(((w >> 1) ? bfhi(p_ldt) : bflo(p_ldt)) * L2E));
    const float al2 = (-rbf(__builtin_amdgcn_exp2f(bflo(p_lar) * L2E)) * dt) * L2E;
    const float rev = (bflo(p_aim) * dt) * 0.15915494309189535f;
    u32 mx = max(max(p_cr & 0x7fffu, p_ci & 0x7fffu), max(p_cr2 & 0x7fffu, p_ci2 & 0x7fffu));
    mx = max(mx, dppu<0xB1>(mx)); mx = max(mx, dppu<0x4E>(mx)); mx = max(mx, dppu<0x141>(mx)); mx = max(mx, dppu<0x140>(mx));
    { auto r = __builtin_amdgcn_permlane16_swap(mx, mx, false, false); mx = max(r[0], r[1]); }
    { auto r = __builtin_amdgcn_permlane32_swap(mx, mx, false, false); mx = max(r[0], r[1]); }
    u32 ex = mx >> 7;
    ex = min(max(ex, 1u), 253u);
    const float dn = __uint_as_float((254u - ex) << 23), up = __uint_as_float(ex << 23);
    kscale = 2.0f * dt * up;
    LAS u32* tp = (LAS u32*)(L + L_TAB + (tid >> 7) * TAB_CH + (tid & 127) * 4);
    f2 U[17], Y[16], V[8], X[31], wp[4], t[31];
    u32 pk[31];
    wp[0] = cexp_p(al2, rev, 1.f);
    Y[1] = cexp_p(al2, -rev, 16.f);
    V[1] = cexp_p(al2, rev, 256.f);
    Y[0] = f2{1.f, 0.f};
    U[0] = f2{bflo(p_cr) * dn, bflo(p_ci) * dn};
    SB();
    FORU(l, 4) {
      const int h = 1 << l;
      FORU(i, h) t[i] = cm1(U[i], wp[l]);
      if (l < 3) { t[8] = cm1(wp[l], wp[l]); t[9] = cm1(Y[h], Y[h]); }
      if (l < 2) t[10] = cm1(V[h], V[h]);
      FORU(i, h - 1) t[11 + i] = cm1(Y[1 + i], Y[h]);
      if (l < 3) { FORU(i, h - 1) t[18 + i] = cm1(V[1 + i], V[h]); }
      SB();
      FORU(i, h) U[h + i] = cm2(U[i], wp[l], t[i]);
      if (l < 3) { wp[l + 1] = cm2(wp[l], wp[l], t[8]); Y[2 * h] = cm2(Y[h], Y[h], t[9]); }
      if (l < 2) V[2 * h] = cm2(V[h], V[h], t[10]);
      FORU(i, h - 1) Y[h + 1 + i] = cm2(Y[1 + i], Y[h], t[11 + i]);
      if (l < 3) { FORU(i, h - 1) V[h + 1 + i] = cm2(V[1 + i], V[h], t[18 + i]); }
      SB();
    }
    auto xa = [](int i) { return i < 9 ? 1 : i < 19 ? 2 + (i - 9) / 5 : 4 + (i - 19) / 3; };
    auto xo = [](int i) { return i < 9 ? 2 * i : i < 19 ? 4 * ((i - 9) % 5) : 8 * ((i - 19) % 3); };
    u32 py[16], pu[16];
    t[0] = cm1(U[8], wp[3]);
    FORU(i, 16) { py[i] = pkh(Y[i]); if (i == 2) U[16] = cm2(U[8], wp[3], t[0]); if (i & 1) tp[(47 + (i >> 1)) * (TABP / 4)] = py[i >> 1]; SB(); }
    FORU(i, 16) { pu[i] = pkh(U[i]); if (i & 1) tp[(55 + (i >> 1)) * (TABP / 4)] = py[8 + (i >> 1)]; SB(); }
    FORU(i, 31) { t[i] = cm1(U[xo(i)], V[xa(i)]); if (!(i & 1)) tp[(i >> 1) * (TABP / 4)] = pu[i >> 1]; SB(); }
    FORU(i, 35) {
      if (i < 31) X[i] = cm2(U[xo(i)], V[xa(i)], t[i]);
      if (i >= 2 && i < 33) pk[i - 2] = pkh(X[i - 2]);
      if (i >= 4) tp[(12 + i) * (TABP / 4)] = pk[i - 4];
      SB();
    }
  }
  *(LAS v4u*)(L + L_PKI + w * 1024 + lane * 16) = wi0;
  if (w < 2) *(LAS v4u*)(L + L_PKI + 4096 + w * 1024 + lane * 16) = wi1;
  *(LAS v4u*)(L + L_PKW + w * 8192 + lane * 16) = wo0;
  v4u gx[4], gst[4], gm[3];
  auto s1_ld = [&](int n) {
    if (n < 4) gx[n] = ld128s(rs, lane * 16 + n * 1024, WS_XG + cp * 16384 + w * 4096);
    else if (n < 8) gst[n - 4] = ld128s(rs, lane * 16 + (n - 4) * 1024, WS_STAT1 + w * 4096);
    else gm[n - 8] = n == 8 ? ld128(rs, mo2) : ld128s(rs, mo, (n == 9 ? 768 : 1024) * 16);
  };
  FORU(n, 3) s1_ld(n);
  __syncthreads();
  f4 kacc[4];
  v2u pl[3], ph[3];
  {
    v4u yf[8], xf[8][4];
    auto ldk = [&](int k, int i) {
      if (k < 8) { if (i < 4) xf[k][i] = *(LAS v4u*)(ka[i] + 64 * k); else yf[k] = *(LAS v4u*)(ka[4] + 64 * k); }
    };
    FORU(k, 2) FORU(i, 5) ldk(k, 4 - i);
    SB();
    FORU(k, 8) FORU(a, 4) {
      if (k) asm volatile("v_mfma_f32_16x16x32_f16 %0, %1, %2, %0" : "+v"(kacc[3 - a]) : "v"(xf[k][3 - a]), "v"(yf[k]));
      else asm volatile("v_mfma_f32_16x16x32_f16 %0, %1, %2, 0" : "=&v"(kacc[3 - a]) : "v"(xf[k][3 - a]), "v"(yf[k]));
      if (a == 0) ldk(k + 2, 4);
      ldk(k + 2, 3 - a);
      if (k < 4 && (a & 1)) s1_ld(3 + 2 * k + (a >> 1));
      if (k >= 6 && a < 3) {
        if (k == 6) pl[a] = trread(pa[a]); else ph[a] = trread(pa[a] + 128);
      }
      SB();
    }
  }
  MM_TAIL();
  f2 pv[8];
  {
    u32 P0[4], P1[4], M[4];
    FORU(a, 4) kacc[a] *= kscale;
    SB();
    FORU(a, 4) { P0[a] = pkbf_a(kacc[a].w, kacc[a].z); P1[a] = pkbf_a(kacc[a].y, kacc[a].x); M[a] = pkbf_a(kacc[a].z, kacc[a].y); }
    SB();
    FORU(a, 4) {
      lptr p0 = t0 + 640 * (3 - a), p1 = t1 + 640 * (3 - a);
      *(LAS v2u*)(p1) = v2u{P0[a], P1[a]};
      *(LAS u16*)(p0 + TOE_C + 6) = (u16)P0[a]; *(LAS u32*)(p1 + TOE_C) = M[a]; *(LAS u16*)(p1 + TOE_C + 4) = (u16)(P1[a] >> 16);
      *(LAS u32*)(p0 + 2 * TOE_C + 4) = P0[a]; *(LAS u32*)(p1 + 2 * TOE_C) = P1[a];
      *(LAS u16*)(p0 + 3 * TOE_C + 2) = (u16)P0[a]; *(LAS u32*)(p0 + 3 * TOE_C + 4) = M[a]; *(LAS u16*)(p1 + 3 * TOE_C) = (u16)(P1[a] >> 16);
      FORU(j, 2) { pv[2 * a + j] = bf2f((a & 1) ? ph[a >> 1][j] : pl[a >> 1][j]); pin(pv[2 * a + j]); }
      SB();
    }
    st128s(rs, lane * 16, b < 176 ? WS_WOP + (b * 4 + w) * 1024 : OOB, v4u{pl[2].x, pl[2].y, ph[2].x, ph[2].y});
  }
  asm volatile("" ::"v"(pf0), "v"(pf2));
  auto s1_bad = [&]() {
    u32 mx = umax3(gm[0].y, gm[0].w, tag), mn = umin3(gm[0].y, gm[0].w, tag);
    FORU(k, 2) { mx = umax3(mx, gm[1 + k].y, gm[1 + k].w); mn = umin3(mn, gm[1 + k].y, gm[1 + k].w); }
    FORU(k, 4) {
      mx = umax3(umax3(mx, gx[k].y, gx[k].w), gst[k].y, gst[k].w);
      mn = umin3(umin3(mn, gx[k].y, gx[k].w), gst[k].y, gst[k].w);
    }
    return __any(mx != mn);
  };
  if (RARE(s1_bad())) {
#pragma nounroll
    for (int sp = 0; sp < SPIN_CAP; ++sp) {
      auto miss = [&](const v4u& v) { return __any(v.y != tag || v.w != tag); };
      FORU(n, 4) {
        if (miss(gx[n])) reld(gx[n], rs, lane * 16, WS_XG + cp * 16384 + w * 4096 + n * 1024);
        if (miss(gst[n])) reld(gst[n], rs, lane * 16, WS_STAT1 + w * 4096 + n * 1024);
      }
      if (miss(gm[0])) reld(gm[0], rs, mo2, 0);
      if (miss(gm[1])) reld(gm[1], rs, mo, 768 * 16);
      if (miss(gm[2])) reld(gm[2], rs, mo, 1024 * 16);
      asm volatile("s_waitcnt vmcnt(0)" : "+v"(gx[0]), "+v"(gx[1]), "+v"(gx[2]), "+v"(gx[3]), "+v"(gst[0]), "+v"(gst[1]), "+v"(gst[2]), "+v"(gst[3]), "+v"(gm[0]), "+v"(gm[1]), "+v"(gm[2])::"memory");
      if (!s1_bad()) break;
    }
  }
  u32 xv[8];
  u32 h0[4], h1[4];
  u32 gav;
  {
    float qs[6];
    f2 m[4], r[4], a[4], c[4];
    auto fill = [&]() { SB(); };
    FORU(i, 6) qs[i] = dppadd<0xB1>(__uint_as_float((i & 1) ? gm[i >> 1].z : gm[i >> 1].x));
    FORU(k, 4) { xv[2 * k] = gx[k].x; xv[2 * k + 1] = gx[k].z; r[k] = f2{bfhi(gst[k].x), bfhi(gst[k].z)}; }
    fill();
    FORU(i, 6) qs[i] = dppadd<0x4E>(qs[i]);
    FORU(k, 4) m[k] = f2{bflo(gst[k].x), bflo(gst[k].z)};
    fill();
    const u32 mv = pkbf(qs[0], qs[1]), sbp = pkbf(qs[2], qs[3]), cbp = pkbf(qs[4], qs[5]);
    FORU(k, 4) r[k] = f2{__builtin_amdgcn_rcpf(r[k].x), __builtin_amdgcn_rcpf(r[k].y)};
    fill();
    const u32 sav = __builtin_amdgcn_readlane(mv, 0), cav = __builtin_amdgcn_readlane(mv, 4);
    gav = __builtin_amdgcn_readlane(mv, 8);
    const u32 scp = pkbf(bf2f(cbp) + 1.0f);
    FORU(k, 4) { a[k] = f2{bflo(gx[k].x), bflo(gx[k].z)}; c[k] = f2{bfhi(gx[k].x), bfhi(gx[k].z)}; }
    fill();
    const u32 s1v = pkbf(bf2f(cav) + 1.0f);
    FORU(k, 4) { a[k] -= m[k]; c[k] -= m[k]; }
    const u32 scb[4] = {dppu<0x150>(scp), dppu<0x154>(scp), dppu<0x158>(scp), dppu<0x15C>(scp)};
    fill();
    FORU(k, 4) { a[k] *= r[k]; c[k] *= r[k]; }
    f2 sc[4], sb[4], t[8], bw[2], sw[2];
    FORU(j, 4) sc[j] = bf2f(scb[j]);
    fill();
    FORU(k, 4) { a[k] = a[k] * bflo(s1v) + bflo(sav); c[k] = c[k] * bfhi(s1v) + bfhi(sav); }
    FORU(j, 8) t[j] = pv[j] * sc[j & 3];
    fill();
    v4u wpk[2];
    FORU(k, 4) { h0[k] = pkbf(a[k]); h1[k] = pkbf(c[k]); }
    FORU(j, 8) wpk[j >> 2][j & 3] = pkbf(t[j]);
    fill();
    *(LAS v4u*)up = v4u{h0[0], h0[1], h0[2], h0[3]};
    *(LAS v4u*)(up + U_CH) = v4u{h1[0], h1[1], h1[2], h1[3]};
    st128(rs, wio, wpk[0]);
    st128s(rs, wio, two ? 65536 : OOB, wpk[1]);
    const u32 sbb[4] = {dppu<0x150>(sbp), dppu<0x154>(sbp), dppu<0x158>(sbp), dppu<0x15C>(sbp)};
    FORU(j, 8) t[j] = bf2f(wpk[j >> 2][j & 3]);
    SB();
    FORU(j, 4) sb[j] = bf2f(sbb[j]);
    FORU(f, 2) sw[f] = (t[4 * f] + t[4 * f + 1]) + (t[4 * f + 2] + t[4 * f + 3]);
    SB();
    FORU(j, 8) pv[j] *= sb[j & 3];
    SB();
    FORU(f, 2) bw[f] = (pv[4 * f] + pv[4 * f + 1]) + (pv[4 * f + 2] + pv[4 * f + 3]);
    const float e0 = sw16sum(bw[0].x + bw[0].y, sw[0].x + sw[0].y), e1 = sw16sum(bw[1].x + bw[1].y, sw[1].x + sw[1].y);
    __builtin_amdgcn_raw_buffer_store_b32(__float_as_uint(sw32sum(e0, e1)), rs, bwo, 0, SC1);
  }
  __syncthreads();
  v4u cdv;
  const u32 fpo = lane < 48 ? WS_WINP + pj * 196608 + (((pi & 7) * 4 + w) * 48 + lane) * 128 : OOB;
  auto side = [&](auto S, auto I) {
    constexpr int s = S.v, i = I.v, z = 10 * s + i;
    if constexpr (z % 4 == 1 && z / 4 < 32) ldw(z / 4);
    if (z == 131) pf3 = __builtin_amdgcn_raw_buffer_load_b32(rw, lane < 32 ? pfo : OOB, 512, 0);
    if (z == 27) {
      asm volatile("s_waitcnt vmcnt(7)" ::: "memory");
      __builtin_amdgcn_raw_buffer_store_b32(tag, rs, lane == 0 ? WS_CD + b * 64 + w * 4 : OOB, 0, SC1);
    }
    if (z == 103) pf1 = __builtin_amdgcn_raw_buffer_load_b32(rs, fpo, 0, SC1);
    if (z == 163) cdv = ld128(rs, WS_CD + tid * 64);
  };
  if (w & 1) conv2<1, 2>(bp, side);
  else conv2<0, 3>(bp, side);
  {
    f2 y[2][4], t[2][4];
    const float ga0 = bflo(gav), ga1 = bfhi(gav);
    FORU(k, 4) { t[0][k] = bf2f(h0[k]); t[1][k] = bf2f(h1[k]); }
    FORU(k, 4) { y[0][k] = f2{bflo(xv[2 * k]), bflo(xv[2 * k + 1])}; y[1][k] = f2{bfhi(xv[2 * k]), bfhi(xv[2 * k + 1])}; }
    SB();
    FORU(k, 4) { t[0][k] *= bflo(p_dsk); t[1][k] *= bfhi(p_dsk); pin(y[0][k]); pin(y[1][k]); pin(t[0][k]); pin(t[1][k]); }
    __syncthreads();
    f4 q[4][4];
    auto yld = [&](int B) { FORU(e, 4) q[B][e] = *(LAS f4*)(yp[B] + (e >> 1) * Y_CH + (e & 1) * 16); SB(); };
    auto yadd = [&](int B) { FORU(e, 4) { t[e >> 1][2 * (e & 1)] += f2{q[B][e].x, q[B][e].y}; t[e >> 1][2 * (e & 1) + 1] += f2{q[B][e].z, q[B][e].w}; } SB(); };
    yld(0);
    if (w >= 2) {
      yld(1); yld(2);
      if (w == 3) { yld(3); yadd(3); }
      yadd(1); yadd(2);
    } else if (w == 1) { yld(1); yadd(1); }
    yadd(0);
    FORU(k, 4) { y[0][k] += t[0][k] * ga0; y[1][k] += t[1][k] * ga1; }
    SB();
    v4u o0, o1;
    FORU(k, 4) { o0[k] = pkbf(y[0][k]); o1[k] = pkbf(y[1][k]); }
    SB();
    st128(rs, x1w, san4(o0));
    st128(rs, x1w + 128, san4(o1));
  }
  CFENCE();
  asm volatile("" ::"v"(pf1), "v"(pf3));
  auto cd_bad = [&]() { return __any(umax3(umax3(cdv.x, cdv.y, cdv.z), cdv.w, tag) != umin3(umin3(cdv.x, cdv.y, cdv.z), cdv.w, tag)); };
  if (RARE(cd_bad())) {
#pragma nounroll
    for (int sp = 0; sp < SPIN_CAP; ++sp) {
      asm volatile("" ::: "memory");
      cdv = ld128(rs, WS_CD + tid * 64);
      if (!cd_bad()) break;
    }
  }
  __syncthreads();
  const u32 blkS = 4 + (w >> 1);
  const bool hasS = blkS < nb;
  const u32 fo[4] = {(pj * 2 + 1) * 6 + w, hasS ? (pj * 2 + 1) * 6 + blkS : 65536u, pj * 12 + w, hasS ? pj * 12 + blkS : 65536u};
  v4u ones = {0x3f803f80u, 0x3f803f80u, 0x3f803f80u, 0x3f803f80u};
  pin(ones);
  v4u wf[4][16];
  auto ldf = [&](int s, int k) { wf[s][k] = ld128s(rs, lane * 16 + (k & 3) * 1024, WS_WINP + fo[s] * 16384 + (k >> 2) * 4096); };
  f4 sW[4], bW[4];
  auto ldb = [&](int n) { ((n & 1) ? sW : bW)[n >> 1] = *(LAS f4*)(L + L_BW + w * 1024 + (n >> 1) * 256 + (n & 1) * 64 + q4 * 16); };
  {
    v4u xr[16];
    f4 bq[8];
    const int xo = WS_X1 + pi * 65536 + w * 16384;
    sfor<0, 40>([&](auto z) {
      constexpr int n = z.v;
      if constexpr (n < 8) bq[n] = __builtin_bit_cast(f4, ld128s(rs, lane * 16 + (n & 1) * 1024, WS_BWP + fo[n >> 1] * 2048));
      else if constexpr (n < T_X1) ldf(n & 1, (n - 8) >> 1);
      else if constexpr (n < T_X1 + 16) xr[n - T_X1] = ld128s(rs, lane * 16 + ((n - T_X1) & 3) * 1024, xo + rq((n - T_X1) >> 2) * 4096);
      else ldf(n & 1, (n - 24) >> 1);
      if constexpr (n < 32) { shuf(IC<2 * n>{}); shuf(IC<2 * n + 1>{}); }
      SB();
      if constexpr (n == 31) {
        FORU(i, 4) bq[i] = bq[2 * i] + bq[2 * i + 1];
        SB();
        f4 u, v;
        FORU(e, 4) { u[e] = sw32sum(bq[0][e], bq[2][e]); v[e] = sw32sum(bq[1][e], bq[3][e]); }
        FORU(e, 4) u[e] = sw16sum(u[e], v[e]);
        FORU(e, 4) u[e] = dppadd<0x128>(u[e]);
        *(LAS f4*)(L + L_BW + w * 1024 + lane * 16) = u;
        SB();
      }
    });
    lptr xi = L + L_XIMG + w * 16384 + (lane >> 3) * 128 + (lane & 1) * 16;
    const u32 xs0 = (((lane & 7) >> 1) ^ ((lane >> 4) & 1)) * 32;
    u32 m[4] = {0, 0, 0, 0};
    FORU(k, 16) { *(LAS v4u*)(xi + rq(k >> 2) * 4096 + (k & 3) * 1024 + (xs0 ^ ((k & 1) * 64))) = xr[k]; m[k >> 2] = vmax(m[k >> 2], xr[k]); ldf(k & 1, 8 + (k >> 1)); SB(); }
    if (RARE(__any(umax3(umax3(m[0], m[1], m[2]), m[3], 0u) == ~0u))) {
      u32 todo = 0;
      FORU(g, 4) todo |= __any(m[g] == ~0u) ? 1u << rq(g) : 0u;
#pragma nounroll
      for (u32 g = 0; g < 4; ++g) {
        if (!((todo >> g) & 1)) continue;
#pragma nounroll
        for (int sp = 0; sp < SPIN_CAP; ++sp) {
          asm volatile("" ::: "memory");
          u32 mm = 0;
          FORU(k, 4) xr[k] = ld128s(rs, lane * 16 + k * 1024, xo + g * 4096);
          FORU(k, 4) mm = vmax(mm, xr[k]);
          if (!__any(mm == ~0u)) break;
        }
        FORU(k, 4) *(LAS v4u*)(xi + g * 4096 + k * 1024 + (xs0 ^ ((k & 1) * 64))) = xr[k];
      }
    }
  }
  __syncthreads();
  const u32 xsw = ((c16 >> 3) & 1) | ((q4 & 1) << 1), xbo = (8 * q4 + (c16 >> 2)) * 128 + 8 * (c16 & 3);
  lptr xb[4];
#pragma unroll
  for (int t = 0; t < 4; ++t) xb[t] = L + xbo + ((t ^ xw ^ xsw) * 32);
  v2u bl[3][4], bh[3][4];
  auto ldx = [&](int k, int i) {
    if (k < 16) { if (i & 1) bh[k % 3][i >> 1] = trread(xb[i >> 1] + 4096 * k + 512); else bl[k % 3][i >> 1] = trread(xb[i >> 1] + 4096 * k); }
  };
  auto bx = [&](int k, int t) { return v4u{bl[k % 3][t].x, bl[k % 3][t].y, bh[k % 3][t].x, bh[k % 3][t].y}; };
  auto xwait = [&](int k) { asm volatile("" ::"v"(bh[k % 3][3])); };
  const int pco = WS_PB + ((pi * 4 + w) * 44 + blk0) * 512, pc2 = 128 + lane < 32 * nb ? lane * 16 : OOB;
  const int zco = WS_Z2N + ((pj * 6 + (w >> 1)) * 64 + 2 * pi + (w & 1)) * 1024, zc2 = w + 8 < 2 * nb ? zco + 262144 : OOB;
  f4 z2a[6];
  f2 rn[4];
  {
    f4 st0, st1;
    v2u sl[16], sh[16];
    FORU(k, 4) { sl[k] = trread(xb[0] + 4096 * k); sh[k] = trread(xb[0] + 4096 * k + 512); }
    MM_HEAD();
    sfor<0, 16>([&](auto z) {
      constexpr int k = z.v;
      const v4u v = {sl[k].x, sl[k].y, sh[k].x, sh[k].y};
      if (k) m_vv(st0, ones, v); else m_vv0(st0, ones, v);
      if constexpr (k + 4 < 16) sl[k + 4] = trread(xb[0] + 4096 * (k + 4)); else { ldx((k - 12) >> 1, 4 * (k & 1)); ldx((k - 12) >> 1, 4 * (k & 1) + 1); }
      SB();
      if (k) m_vv(st1, v, v); else m_vv0(st1, v, v);
      if constexpr (k + 4 < 16) sh[k + 4] = trread(xb[0] + 4096 * (k + 4) + 512); else { ldx((k - 12) >> 1, 4 * (k & 1) + 2); ldx((k - 12) >> 1, 4 * (k & 1) + 3); }
      if (k == 1) st128s(rs, lane * 16, pco, SENT);
      if (k == 5) st128s(rs, lane * 16, pco + 1024, SENT);
      if (k == 9) st128s(rs, pc2, pco + 2048, SENT);
      if (k == 3) st128s(rs, lane * 16, zco, SENT);
      if (k == 7) st128s(rs, lane * 16, zco + 131072, SENT);
      if (k == 11) st128s(rs, lane * 16, zc2, SENT);
      SB();
    });
    float sv0 = 0.f, sv1 = 0.f;
    FORU(k, 16) FORU(i, 6) {
      const int u = i == 0 ? 0 : i == 1 ? 4 : i == 2 ? 1 : i == 3 ? 5 : i - 2;
      if (i == 0) xwait(k);
      if (k) m_vv(z2a[u], wf[u >> 2][k], bx(k, u & 3)); else m_vv0(z2a[u], wf[u >> 2][k], bx(k, u & 3));
      if (i < 2) { ldx(k + 2, 2 * i); ldx(k + 2, 2 * i + 1); } else ldx(k + 2, i + 2);
      if (i == 2 && k) ldf(2 + ((k - 1) & 1), (k - 1) >> 1);
      if (i == 4 && (k & 1)) ldw(32 + (k >> 1));
      if (i == 3 || i == 5) {
        const int z = 2 * k + (i == 5);
        if (z == 4) sv1 = (c16 & 1) ? st1.y : st1.x;
        if (z == 5) sv0 = (c16 & 1) ? st1.w : st1.z;
        if (z == 6) sv1 = (c16 & 2) ? sv0 : sv1;
        if (z == 7) { pin(st0); sv0 = st0.x * (1.f / 512); }
        if (z == 8) sv1 *= (1.f / 512);
        if (z == 9) sv1 = fmaxf(__builtin_fmaf(-sv0, sv0, sv1), 0.f);
        if (z == 10) sv1 = rbf(sv1);
        if (z == 11) sv0 = rbf(sv0);
        if (z == 12) sv1 = rbf(sv1 + 1e-5f);
        if (z == 13) sv1 = __builtin_amdgcn_sqrtf(sv1);
        if (z == 14) sv1 = rbf(sv1);
        if (z == 15) sv1 = __builtin_amdgcn_rcpf(sv1);
        if (z == 16) sv0 *= -sv1;
        if (z == 17) *(volatile LAS f2*)(L + (q4 == (c16 >> 2) ? L_XST + (16 * xw + c16) * 8 : L_XST + 512 + lane * 8)) = f2{sv1, sv0};
        if (z >= 4 && z <= 16) { pin(sv0); pin(sv1); }
        if (z >= 30) { ldb(2 * z - 60); ldb(2 * z - 59); }
        if (z == 24) __builtin_amdgcn_s_barrier();
        if (z >= 26 && z < 30) rn[z - 26] = *(volatile LAS f2*)(L + L_XST + (16 * ((z - 26) ^ xw) + c16) * 8);
      }
      SB();
    }
    MM_TAIL();
  }
  auto shuf2 = [&](auto C, auto N) {
    constexpr int n = N.v % 12, z = 16 * (C.v + N.v / 12);
    if constexpr (N.v < 24) { if constexpr (n < 8) shuf(IC<z + n>{}); else { shuf(IC<z + 2 * n - 8>{}); shuf(IC<z + 2 * n - 7>{}); } }
  };
  auto epi = [&](f4 (&a)[6], int s0, auto fill) {
    f2 v[12];
    sfor<0, 2>([&](auto z) {
      FORU(j, 6) {
        const int i = 6 * z.v + j;
        const f4 &sw = sW[s0 + (i >> 3)], &bw = bW[s0 + (i >> 3)];
        v[i] = ((i & 1) ? f2{sw.z, sw.w} : f2{sw.x, sw.y}) * rn[(i >> 1) & 3].y + ((i & 1) ? f2{bw.z, bw.w} : f2{bw.x, bw.y});
      }
      fill(IC<z.v>{});
    });
    sfor<0, 2>([&](auto z) {
      FORU(j, 6) { const int i = 6 * z.v + j; v[i] = ((i & 1) ? f2{a[i >> 1].z, a[i >> 1].w} : f2{a[i >> 1].x, a[i >> 1].y}) * rn[(i >> 1) & 3].x + v[i]; }
      fill(IC<2 + z.v>{});
    });
    gelu_n<12>(v, fill);
    FORU(i, 6) { a[i] = f4{v[2 * i].x, v[2 * i].y, v[2 * i + 1].x, v[2 * i + 1].y}; pin(a[i]); }
  };
  {
    epi(z2a, 0, [&](auto N) {
      constexpr int n = N.v;
      SB();
      if constexpr ((n & 1) && n < 22) ldf(2 + ((15 + n / 2) & 1), (15 + n / 2) >> 1);
      if constexpr (!(n & 1) && n < 24) ldw(40 + n / 2);
      SB();
    });
    if (!hasS) { z2a[4] = f4{0.f, 0.f, 0.f, 0.f}; z2a[5] = z2a[4]; }
    f2 zs[6], zq[6];
    FORU(i, 6) { zs[i] = f2{z2a[i].x, z2a[i].y} + f2{z2a[i].z, z2a[i].w}; zq[i] = f2{z2a[i].x, z2a[i].y} * f2{z2a[i].x, z2a[i].y}; }
    SB();
    FORU(i, 6) zq[i] = __builtin_elementwise_fma(f2{z2a[i].z, z2a[i].w}, f2{z2a[i].z, z2a[i].w}, zq[i]);
    FORU(i, 2) zs[i] += zs[4 + i];
    SB();
    float us[4], uq[4], u[4];
    FORU(i, 2) zq[i] += zq[4 + i];
    FORU(t, 4) us[t] = zs[t].x + zs[t].y;
    SB();
    FORU(t, 4) uq[3 - t] = zq[3 - t].x + zq[3 - t].y;
    SB();
    FORU(t, 4) u[3 - t] = sw32sum(us[3 - t], uq[3 - t]);
    LAS float* zp = (LAS float*)(L + L_ZST) + (w * 2 + (lane >> 5)) * 64 + c16;
    zp[16 * ((2 * (q4 & 1) + 1) ^ xw)] = sw16sum(u[1], u[3]);
    zp[16 * ((2 * (q4 & 1)) ^ xw)] = sw16sum(u[0], u[2]);
  }
  asm volatile("s_waitcnt vmcnt(40)" ::: "memory");
  __syncthreads();
  if (w == 0) {
    LAS float* zp = (LAS float*)(L + L_ZST) + lane;
    const float s = (zp[0] + zp[128]) + (zp[256] + zp[384]), ss = (zp[64] + zp[192]) + (zp[320] + zp[448]);
    st128(rs, WS_ST2 + ((pi * 8 + pj) * 64 + lane) * 16, v4u{__float_as_uint(s), tag, __float_as_uint(ss), tag});
  }
  __builtin_amdgcn_raw_buffer_store_b32(tag, rs, tid == 64 ? WS_F3 + (pj * 32 + pi) * 128 : OOB, 0, SC1);
  CFENCE();
  v4u wo[6][4];
  const u32 wob = WS_WOP + (pj * 22 + 6 * w) * 4096, wobh = ssel(w < 3, wob, OOB);
  auto ldo = [&](int n) { wo[n >> 2][n & 3] = ld128s(rs, lane * 16 + (n & 3) * 1024, (n < 16 ? wob : wobh) + (n >> 2) * 4096); };
  f4 z1a[6];
  v2u xres[4];
  v4u g[8];
  u32 fv;
  auto f_ld = [&]() { fv = __builtin_amdgcn_raw_buffer_load_b32(rs, (lane & 7) * 128, WS_F3 + (pj * 32 + 8 * w) * 128, SC1); };
  auto s2b_ld = [&](int n) { g[n] = ld128s(rs, (2 * q4 * 64 + c16) * 16 + (n & 1) * 1024, WS_ST2 + pi * 8192 + ((n >> 1) ^ xw) * 256); };
  {
    FORU(i, 16) ldx(i >> 3, i & 7);
    MM_HEAD();
    sfor<0, 96>([&](auto z) {
      constexpr int k = z.v / 6, i = z.v % 6, u = i == 0 ? 0 : i == 1 ? 4 : i == 2 ? 1 : i == 3 ? 5 : i - 2;
      if (i == 0) xwait(k);
      if (k) m_vv(z1a[u], wf[2 + (u >> 2)][k], bx(k, u & 3)); else m_vv0(z1a[u], wf[2 + (u >> 2)][k], bx(k, u & 3));
      if (i < 2) { ldx(k + 2, 2 * i); ldx(k + 2, 2 * i + 1); } else ldx(k + 2, i + 2);
      if (k < 3 && (i == 2 || i == 4)) ldf(1 + (i >> 1), 13 + k);
      if (k >= 3 && k < 9 && i == 3) ldw(49 + k);
      if ((k == T_S2B || k == T_S2B + 1) && i >= 2) s2b_ld(4 * (k - T_S2B) + i - 2);
      if (k >= 11 && k < 14 && (i == 2 || i == 4)) ldw(58 + 2 * (k - 11) + (i >> 2));
      if (k == 14 && i >= 2) ldb(2 + i);
      if (k == 15 && i == 3) f_ld();
      SB();
    });
    MM_TAIL();
#pragma unroll
    for (int n = 0; n < 4; ++n) {
      const u32 row = 16 * n + 4 * q4 + (c16 >> 2);
      xres[n] = trread(L + L_XIMG + pj * 8192 + row * 128 + ((w ^ (((row >> 1) & 1) | (((row >> 3) & 1) << 1))) * 32) + 8 * (c16 & 3));
    }
  }
  lptr twa[6], tra[4];
  {
    lptr tw = L + L_TRS + w * TRS_W;
    FORU(i, 6) { twa[i] = tw + (16 * ((i & 3) ^ xw) + c16) * 64 + 8 * (((i & 4) + q4) ^ (c16 >> 1)); pin(twa[i]); }
    const u32 r0 = 8 * q4 + (c16 >> 2);
    FORU(i, 4) {
      tra[i] = tw + ((i & 2) ? (w & 1) * 2048 : 0) + r0 * 64 + (i & 1) * 256 + 8 * ((2 * (i & 2) + (c16 & 3)) ^ (((r0 + 4 * (i & 1)) >> 1) & 7));
      pin(tra[i]);
    }
  }
  {
    auto bad = [&]() { u32 mx = tag, mn = tag; FORU(n, 8) { mx = umax3(mx, g[n].y, g[n].w); mn = umin3(mn, g[n].y, g[n].w); } return __any(mx != mn); };
    if (RARE(bad())) {
#pragma nounroll
      for (int sp = 0; sp < SPIN_CAP; ++sp) {
        FORU(n, 8) reld(g[n], rs, (2 * q4 * 64 + c16) * 16, WS_ST2 + pi * 8192 + ((n >> 1) ^ xw) * 256 + (n & 1) * 1024);
        asm volatile("s_waitcnt vmcnt(0)" : "+v"(g[0]), "+v"(g[1]), "+v"(g[2]), "+v"(g[3]), "+v"(g[4]), "+v"(g[5]), "+v"(g[6]), "+v"(g[7])::"memory");
        if (!bad()) break;
      }
    }
    float a[4], c[4];
    FORU(t, 4) { a[t] = __uint_as_float(g[2 * t].x) + __uint_as_float(g[2 * t + 1].x); c[t] = __uint_as_float(g[2 * t].z) + __uint_as_float(g[2 * t + 1].z); }
    SB();
    const float a0 = sw32sum(a[0], a[2]), a1 = sw32sum(a[1], a[3]), c0 = sw32sum(c[0], c[2]), c1 = sw32sum(c[1], c[3]);
    const float m = sw16sum(a0, a1) * (1.f / 688), var = fmaxf(sw16sum(c0, c1) * (1.f / 688) - m * m, 0.f);
    const float r = __builtin_amdgcn_rcpf(ln_den(var)), n0 = -rbf(m) * r;
    auto gather = [&](float v, float (&o)[4]) {
      auto e = __builtin_amdgcn_permlane16_swap(__float_as_uint(v), __float_as_uint(v), false, false);
      auto e0 = __builtin_amdgcn_permlane32_swap(e[0], e[0], false, false);
      auto e1 = __builtin_amdgcn_permlane32_swap(e[1], e[1], false, false);
      o[0] = __uint_as_float(e0[0]); o[2] = __uint_as_float(e0[1]); o[1] = __uint_as_float(e1[0]); o[3] = __uint_as_float(e1[1]);
    };
    gather(r, a); gather(n0, c);
    SB();
    FORU(i, 6) z2a[i] = z2a[i] * a[i & 3] + c[i & 3];
    SB();
    v2u zp[6], zl[3], zh[3];
    FORU(i, 6) zp[i] = v2u{pkbf(z2a[i].x, z2a[i].y), pkbf(z2a[i].z, z2a[i].w)};
    SB();
    FORU(i, 6) *(LAS v2u*)twa[i] = zp[i];
    FORU(n, 3) { zl[n] = trread(tra[n & 2] + (n & 1) * 2048); zh[n] = trread(tra[(n & 2) + 1] + (n & 1) * 2048); }
    SB();
    const u32 zo = WS_Z2N + (pj * 6 * 64 + 2 * pi) * 1024;
    FORU(n, 3) st128s(rs, lane * 16, n < 2 ? zo + w * 65536 + n * 1024 : (hasS ? zo + blkS * 65536 + (w & 1) * 1024 : OOB), san4(v4u{zl[n].x, zl[n].y, zh[n].x, zh[n].y}));
  }
  CFENCE();
  u32 bsr[4], gbp[2];
  v4u gg[2];
  FORU(t, 4) bsr[t] = gld<u16>(bsp, (64 * pi + 16 * (t ^ xw) + c16) * 2);
  FORU(m, 2) gg[m] = ld128(rs, WS_MOD + ((lane & 3) * 1536 + 1280 + 32 * pj + 16 * m + 8 * (c16 >> 3) + 2 * q4 + ((c16 >> 2) & 1)) * 16);
  if (RARE(__any(fv != tag))) {
#pragma nounroll
    for (int sp = 0; sp < SPIN_CAP; ++sp) {
      asm volatile("" ::: "memory");
      f_ld();
      if (!__any(fv != tag)) break;
    }
  }
  CFENCE();
  f4 ga[2][4], gs[6], q[12];
  {
    const u32 zo = WS_Z2N + (pj * 6 * 64 + 16 * w) * 1024;
    v4u zr[16];
    u32 zv[4];
    FORU(g, 4) zv[g] = lane * 16 + rk(g) * 4096;
    u32 so[6], rsl[6];
    FORU(it, 3) {
      const u32 d = 1 + smod(it + ((0x21021021u >> (4 * (pi & 7))) & 3), 3), blk = (w + d) & 3;
      so[it] = zo + blk * 65536;
      rsl[it + 1] = (blk * 3 + 3 - d) * 4096;
    }
    FORU(it, 2) {
      const u32 e = (it + pi) & (nb - 5);
      so[3 + it] = zo + (4 + e) * 65536;
      rsl[4 + it] = (12 + 4 * e + w) * 4096;
    }
    so[5] = zo + w * 65536;
    auto ldz = [&](int k, u32 s0) { zr[k] = ld128s(rs, zv[k >> 2] + (k & 3) * 1024, s0); };
    FORU(n, 7) ldo(n);
    epi(z1a, 2, [&](auto N) {
      constexpr int n = N.v;
      SB();
      shuf2(IC<4>{}, N);
      if constexpr (n < 5) ldo(7 + n);
      if constexpr (n >= T_Z && n < T_Z + 16) ldz(n - T_Z, so[0]);
      SB();
    });
    FORU(t, 4) { bsr[t] <<= 16; pin(bsr[t]); }
    FORU(m, 2) { gbp[m] = pkbf(quadsum(__uint_as_float(gg[m].x)), quadsum(__uint_as_float(gg[m].z))); pin(gbp[m]); }
    __syncthreads();
    {
      auto bad = [&]() { u32 m = 0; FORU(k, 16) m = vmax(m, zr[k]); return __any(m == ~0u); };
      if (RARE(bad())) {
#pragma nounroll
        for (int sp = 0; sp < SPIN_CAP; ++sp) {
          asm volatile("" ::: "memory");
          FORU(k, 16) ldz(k, so[0]);
          if (!bad()) break;
        }
      }
    }
    u32 ra = 0, badm = 0;
    float ck = 0.f;
    v4u oth;
    lptr qa[4];
    auto qaddr = [&](int i, bool sh) {
      u32 l16 = lane * 16;
      pin(l16);
      qa[i] = L + l16 + (L_RED + ((sh ? (blkS * 4 - 4) * 4 : w * 12) + (i ^ xw)) * 1024);
      pin(qa[i]);
    };
    auto ldq = [&](int n, bool sh) { q[n] = *(volatile LAS f4*)(sh ? qa[n & 1] + (n >> 1) * 4096 : qa[n & 3] + (n >> 2) * 4096); };
    auto qadd = [&](int e, bool sh) {
      const int nt = sh ? 2 : 4, n = e / 4, i = n % nt, c = e & 3;
      float v = (n < nt ? __uint_as_float(bsr[i]) : gs[(sh ? 4 : 0) + i][c]) + q[n][c];
      pin(v);
      gs[(sh ? 4 : 0) + i][c] = v;
    };
    MM_HEAD();
    auto slot = [&](auto z) {
      constexpr int it = z.v / 64, k = (z.v / 4) % 16, t = z.v % 4, h = it & 1;
      if (it == 5) m_va(gs[t], zr[k], wsf[k][t]); else if (k) m_va(ga[h][t], zr[k], wsf[k][t]); else m_va0(ga[h][t], zr[k], wsf[k][t]);
      constexpr int y = z.v % 64;
      constexpr int pz = z.v / 4 - 1, pit = pz / 16, pk = pz % 16;
      if constexpr (t == 1 && pz >= 0) {
        if constexpr (pit < 5) ldz(pk, so[pit + 1]);
      }
      if constexpr (it == 0 && t != 1 && 3 * k + t - (t > 0) < 32) shuf(IC<96 + 3 * k + t - (t > 0)>{});
      if constexpr (it > 0) {
        if (k == 2 && t == 0) { ck = ga[h ^ 1][0].x + ga[h ^ 1][0].y; pin(ck); }
        if (k == 3 && t == 0) { ck += ga[h ^ 1][0].z; pin(ck); }
        if (k == 4 && t == 0) { ck += ga[h ^ 1][0].w; pin(ck); }
        if (k == 5 && t == 0) badm |= __any(ck != ck) ? 1u << (it - 1) : 0u;
        if (k >= 2 && k < 6 && t == 2) { ra = rsl[it] + lane * 16 + (((k - 2) ^ xw) * 1024); pin(ra); }
        if constexpr (k >= 2 && k < 6 && t == 3) *(volatile LAS f4*)(L + L_RED + ra) = ga[h ^ 1][k - 2];
      }
      if constexpr (it == 4) {
        if constexpr (y < 4) qaddr(y, false);
        if (y == 4) { asm volatile("s_waitcnt lgkmcnt(0)" ::: "memory"); __builtin_amdgcn_s_barrier(); }
        if constexpr (y >= 5 && y < 8) { ldq(2 * y - 10, false); ldq(2 * y - 9, false); }
        if constexpr (y >= 8 && y < 32 && y % 4 == 1) ldq(4 + y / 4, false);
        if constexpr (y >= 24 && y < 60) { qadd((y - 24) * 4 / 3, false); if constexpr ((y - 24) % 3 == 2) qadd((y - 24) * 4 / 3 + 1, false); }
      }
      if constexpr (it == 5) {
        if constexpr (y == 24 || y == 25) qaddr(y - 24, true);
        if (y == 26) *(volatile LAS u32*)(L + L_XST + w * 4) = badm;
        if (y == 30) { asm volatile("s_waitcnt lgkmcnt(0)" ::: "memory"); __builtin_amdgcn_s_barrier(); }
        if constexpr (y >= 31 && y < 39) ldq(y - 31, true);
        if (y == 39) oth = *(volatile LAS v4u*)(L + L_XST);
        if constexpr (y >= 44 && y < 60) { qadd(2 * (y - 44), true); qadd(2 * (y - 44) + 1, true); }
        if (y == HINT_Y) __builtin_amdgcn_raw_buffer_store_b32(tag, rs, lane == 0 ? 0 : OOB, w == 0 ? WS_H4 + (pi * 8 + pj) * 128 : OOB, SC1);
      }
      SB();
    };
    sfor<0, 192>(slot);
    MM_DONE(ga[0]);
    if (nb == 6) { sfor<192, 256>(slot); MM_DONE(ga[1]); }
    else {
      const float t = (ga[0][0].x + ga[0][0].y) + (ga[0][0].z + ga[0][0].w);
      badm |= __any(t != t) ? 4u : 0u;
      FORU(t, 4) { *(volatile LAS f4*)(L + L_RED + rsl[3] + lane * 16 + ((t ^ xw) * 1024)) = ga[0][t]; ga[1][t] = f4{0.f, 0.f, 0.f, 0.f}; }
      MM_HEAD();
    }
    sfor<256, 384>(slot);
    MM_DONE(gs);
    { const float t = (gs[0].x + gs[0].y) + (gs[0].z + gs[0].w); badm |= __any(t != t) ? 32u : 0u; }
    const u32 anyo = __builtin_amdgcn_readfirstlane(oth.x | oth.y | oth.z | oth.w);
    if (RARE(badm | anyo)) {
#pragma nounroll
      for (u32 it = 0; it < 6; ++it) {
        if (it == 5) {
          if (anyo) __syncthreads();
          FORU(n, 8) ldq(n, true);
          FORU(e, 32) qadd(e, true);
          FORU(i, 4) qaddr(i, false);
          FORU(n, 12) ldq(n, false);
          FORU(e, 48) qadd(e, false);
        } else {
          if (!((badm >> it) & 1)) continue;
          FORU(t, 4) gs[t] = f4{0.f, 0.f, 0.f, 0.f};
        }
        const u32 s0 = it == 0 ? so[0] : it == 1 ? so[1] : it == 2 ? so[2] : it == 3 ? so[3] : it == 4 ? so[4] : so[5];
        const u32 r0 = it == 0 ? rsl[1] : it == 1 ? rsl[2] : it == 2 ? rsl[3] : it == 3 ? rsl[4] : rsl[5];
#pragma nounroll
        for (int sp = 0; sp < SPIN_CAP; ++sp) {
          u32 m = 0;
          FORU(k, 16) ldz(k, s0);
          FORU(k, 16) m = vmax(m, zr[k]);
          asm volatile("" ::: "memory");
          if (!__any(m == ~0u)) break;
        }
        FORU(k, 16) m4_va(gs, zr[k], wsf[k]);
        if (it < 5) { FORU(t, 4) *(LAS f4*)(L + L_RED + r0 + lane * 16 + ((t ^ xw) * 1024)) = gs[t]; }
      }
    }
  }
  {
    const u32 lane = fresh_lane(), c16 = lane & 15, q4 = lane >> 4;
    FORU(i, 6) gs[i] *= z1a[i];
    SB();
    v2u P[6];
    FORU(i, 6) P[i] = v2u{pkbf(gs[i].x, gs[i].y), pkbf(gs[i].z, gs[i].w)};
    SB();
#pragma unroll
    for (int h = 0; h < 3; ++h) {
      auto rx = __builtin_amdgcn_permlane16_swap(P[2 * h].x, P[2 * h + 1].x, false, false);
      auto ry = __builtin_amdgcn_permlane16_swap(P[2 * h].y, P[2 * h + 1].y, false, false);
      const u32 tt = (2 * (h & 1) + (q4 & 1)) ^ xw;
      st128s(rs, WS_PB + ((pi * 4 + tt) * 44 + blk0) * 512 + (q4 >> 1) * 256 + c16 * 16, h < 2 ? w * 512 : (hasS ? blkS * 512 : OOB),
             san4(v4u{rx[0], ry[0], rx[1], ry[1]}));
    }
  }
  FORU(n, 12) ldo(12 + n);
  {
    const u32 lane = fresh_lane(), c16 = lane & 15, q4 = lane >> 4;
    f4 oa[2][4];
    v4u pr[4][6];
    u32 pb[4], pbh[4];
    u32 rd[3];
    FORU(u, 4) {
      const u32 d = u < 3 ? 1 + smod(u + smod(pj & 3, 3), 3) : 4;
      pb[u] = WS_PB + ((pi * 4 + ((w + d) & 3)) * 44 + 12 * w) * 512; pbh[u] = ssel(w < 3, pb[u] + 4096, OOB);
      if (u < 3) rd[u] = L_RED2 + (((w + d) & 3) * 3 + 3 - d) * 4096 + lane * 16;
    }
    const u32 pv3 = (w == 3 && lane >= 32) ? OOB : lane * 16 + 3072;
    auto ldp = [&](int n) {
      const int u = n / 6, k = n % 6;
      pr[u][k] = ld128s(rs, k == 3 ? pv3 : lane * 16 + (k & 3) * 1024, k < 4 ? pb[u] : pbh[u]);
    };
#pragma nounroll
    for (int sp = 0; sp < SPIN_CAP; ++sp) {
      asm volatile("" ::: "memory");
      if (!__any(__builtin_amdgcn_raw_buffer_load_b32(rs, (lane & 7) * 128, WS_H4 + pi * 1024, SC1) != tag)) break;
      __builtin_amdgcn_s_sleep(2);
    }
    FORU(n, 12) { ldp(n); SB(); }
    u32 bad = 0;
    MM_HEAD();
    sfor<0, 96>([&](auto z) {
      constexpr int u = z.v / 24, k = (z.v / 4) % 6, n = z.v % 4;
      if (k) m_vv(oa[u & 1][n], wo[k][n], pr[u][k]); else m_vv0(oa[u & 1][n], wo[k][n], pr[u][k]);
      if constexpr (n == 1 && z.v / 4 < 12) ldp(12 + z.v / 4);
      if constexpr (u > 0 && k == 2 && n == 0) bad |= __any(oa[(u & 1) ^ 1][0].x != oa[(u & 1) ^ 1][0].x) ? 1u << (u - 1) : 0u;
      if constexpr (u > 0 && k >= 2 && n == 2) *(LAS f4*)(L + rd[u - 1] + (k - 2) * 1024) = oa[(u & 1) ^ 1][k - 2];
      SB();
    });
    MM_DONE(oa[1]);
    bad |= __any(oa[1][0].x != oa[1][0].x) ? 8u : 0u;
    if (RARE(bad)) {
#pragma nounroll
      for (u32 u = 0; u < 4; ++u) {
        if (!((bad >> u) & 1)) continue;
        pb[0] = u == 0 ? pb[0] : u == 1 ? pb[1] : u == 2 ? pb[2] : pb[3];
        pbh[0] = u == 0 ? pbh[0] : u == 1 ? pbh[1] : u == 2 ? pbh[2] : pbh[3];
#pragma nounroll
        for (int sp = 0; sp < SPIN_CAP; ++sp) {
          u32 m = 0;
          asm volatile("" ::: "memory");
          FORU(k, 6) ldp(k);
          FORU(k, 6) m = vmax(m, pr[0][k]);
          if (!__any(m == ~0u)) break;
        }
        asm volatile("fk_text_b:");
        FORU(n, 4) oa[0][n] = f4{0.f, 0.f, 0.f, 0.f};
        FORU(k, 6) m4_vv(oa[0], wo[k], pr[0][k]);
        FORU(n, 4) {
          if (u < 3) *(LAS f4*)(L + (u == 0 ? rd[0] : u == 1 ? rd[1] : rd[2]) + n * 1024) = oa[0][n];
          else oa[1][n] = oa[0][n];
        }
      }
    }
    __syncthreads();
    f4 o[4];
    FORU(n, 4) {
      lptr p = L + L_RED2 + (w * 12 + n) * 1024 + lane * 16;
      o[n] = (oa[1][n] + *(LAS f4*)p) + (*(LAS f4*)(p + 4096) + *(LAS f4*)(p + 8192));
    }
    v2u O[4];
#pragma unroll
    for (int n = 0; n < 4; ++n) {
      const u32 g0 = (n & 1) ? dppu<0x158>(gbp[n >> 1]) : dppu<0x150>(gbp[n >> 1]), g1 = (n & 1) ? dppu<0x15C>(gbp[n >> 1]) : dppu<0x154>(gbp[n >> 1]);
      O[n] = v2u{pkbf(bf2f(xres[n].x) + bf2f(g0) * f2{o[n].x, o[n].y}), pkbf(bf2f(xres[n].y) + bf2f(g1) * f2{o[n].z, o[n].w})};
    }
#pragma unroll
    for (int h = 0; h < 2; ++h) {
      auto rx = __builtin_amdgcn_permlane16_swap(O[2 * h].x, O[2 * h + 1].x, false, false);
      auto ry = __builtin_amdgcn_permlane16_swap(O[2 * h].y, O[2 * h + 1].y, false, false);
      *(v4u*)(out + (64 * pi + 16 * w + c16) * 512 + 64 * pj + 16 * (2 * h + (q4 & 1)) + 8 * (q4 >> 1)) = v4u{rx[0], ry[0], rx[1], ry[1]};
    }
  }
  if (b == 0 && w == 0) __builtin_amdgcn_raw_buffer_store_b32(tag, rs, WS_E, 0, SC1);
}

extern "C" void kernel_launch(void* const* d_in, const int* in_sizes, int n_in, void* d_out, int out_size, void* d_ws, size_t ws_size,
                              hipStream_t stream) {
  (void)in_sizes; (void)n_in; (void)out_size; (void)ws_size;
  (void)hipFuncSetAttribute((const void*)fused_kernel, hipFuncAttributeMaxDynamicSharedMemorySize, LDS_BYTES);
  fused_kernel<<<256, 256, LDS_BYTES, stream>>>((const u16*)d_in[0], (const u16*)d_in[1], (const u16*)d_in[2], (const u16*)d_in[3],
                                                (const u16*)d_in[4], (const u16*)d_in[5], (const u16*)d_in[6], (const u16*)d_in[8],
                                                (const u16*)d_in[9], (const u16*)d_in[10], (const u16*)d_in[11], (const u16*)d_in[12],
                                                (const u16*)d_in[13], (u16*)d_out, (unsigned char*)d_ws);
}
```
